```python
import torch

def _softmax(x, axis=-1):
    return torch.softmax(x, dim=axis)
def _gelu(x):
    return torch.nn.functional.gelu(x, approximate="tanh")
def _silu(x):
    return torch.nn.functional.silu(x)
def _sigmoid(x):
    return torch.sigmoid(x)
def _layernorm(x, axis=-1, eps=1e-5):
    m = x.mean(dim=axis, keepdim=True)
    v = x.var(dim=axis, keepdim=True, unbiased=False)
    return (x - m) / torch.sqrt(v + eps)
def _scatter_add(tgt, idx, src, axis=0):
    return tgt.clone().index_add_(axis, idx, src)
def _segment_sum(src, seg, n):
    out = torch.zeros((n,) + src.shape[1:], dtype=src.dtype)
    return out.index_add_(0, seg, src)
def _pad(x, pw):
    flat = tuple(v for pair in reversed(pw) for v in pair)
    return torch.nn.functional.pad(x, flat)

kernel_name = 'recurrent_scan__gqa_block_cached__dense_activation_silu__decode'

def setup_inputs():

    _w = (lambda a: (lambda t: t.to(torch.bfloat16) if t.is_floating_point() else t)(torch.as_tensor(a)))
    def _A_setup():
        B, Sq, D = 2, 1, 512
        Hq, Hkv, Sk, Dh = 32, 8, 32768, 128
        assert Hq % Hkv == 0, (Hq, Hkv)
        return {
            "x": _w((lambda *s: torch.randn(*s, dtype=torch.bfloat16) * 0.4)(B, Sq, D)),
            "k_cache": _w((lambda *s: torch.randn(*s, dtype=torch.bfloat16) * 1.6)(B, Hkv, Sk, Dh)),
            "v_cache": _w((lambda *s: torch.randn(*s, dtype=torch.bfloat16) * 1.6)(B, Hkv, Sk, Dh)),
            "wq": _w((lambda *s: torch.randn(*s, dtype=torch.bfloat16) * 1.6)(D, Hq * Dh) * (D ** -0.5)),
            "wo": _w((lambda *s: torch.randn(*s, dtype=torch.bfloat16) * 1.6)(Hq * Dh, D) * ((Hq * Dh) ** -0.5)),
        }
    _A_in = _A_setup()

    def _B_setup():
        B, S, D, F = 2, 1, 512, 1792
        return {
            "x": _w((lambda *s: torch.randn(*s, dtype=torch.bfloat16) * 0.4)(B, S, D)),
            "w1": _w((lambda *s: torch.randn(*s, dtype=torch.bfloat16) * 1.6)(D, F) * (D ** -0.5)),
            "w2": _w((lambda *s: torch.randn(*s, dtype=torch.bfloat16) * 1.6)(F, D) * (F ** -0.5)),
        }
    _B_in = _B_setup()

    return {
        "A_x": _A_in["x"],
        "A_k_cache": _A_in["k_cache"],
        "A_v_cache": _A_in["v_cache"],
        "A_wq": _A_in["wq"],
        "A_wo": _A_in["wo"],
        "B_x": _B_in["x"],
        "B_w1": _B_in["w1"],
        "B_w2": _B_in["w2"],
    }

def reference(A_x, A_k_cache, A_v_cache, A_wq, A_wo, B_x, B_w1, B_w2):
    def _A_ref(x, k_cache, v_cache, wq, wo):
        B, Sq, D = x.shape
        Hq, Dh = 32, 128
        Hkv = k_cache.shape[1]
        rep = Hq // Hkv
        q = (lambda x, *d: x.permute(*d))((x @ wq).reshape(B, Sq, Hq, Dh), 0, 2, 1, 3)
        k = (lambda x, n, axis: torch.repeat_interleave(x, n, dim=axis))(k_cache, rep, axis=1)
        v = (lambda x, n, axis: torch.repeat_interleave(x, n, dim=axis))(v_cache, rep, axis=1)
        scores = torch.einsum("bhqd,bhkd->bhqk", q, k) * (Dh ** -0.5)
        p = _softmax(scores, axis=-1)
        o = torch.einsum("bhqk,bhkd->bhqd", p, v)
        return (lambda x, *d: x.permute(*d))(o, 0, 2, 1, 3).reshape(B, Sq, Hq * Dh) @ wo

    def _B_ref(x, w1, w2):
        h = torch.einsum("bsd,df->bsf", x, w1)
        h = _silu(h)
        out = torch.einsum("bsf,fd->bsd", h, w2)
        return out

    x = A_x
    xn = _layernorm(x, axis=-1)
    s = _A_ref(xn, A_k_cache, A_v_cache, A_wq, A_wo)
    g = _silu(_B_ref(xn, B_w1, B_w2))
    return x + s * g
```

```cpp
#include <hip/hip_runtime.h>
#include <hip/hip_bf16.h>
#include <stdint.h>

#define MIMIC 0
typedef unsigned short u16;
typedef unsigned v4u __attribute__((ext_vector_type(4)));
typedef __bf16 bf16x2 __attribute__((ext_vector_type(2)));
constexpr int B = 2, D = 512, HQ = 32, HKV = 8, SK = 32768, DH = 128, F = 1792;
constexpr int NSPLIT = 32;

__device__ __forceinline__ float bf2f(u16 v) { return __uint_as_float((unsigned)v << 16); }
__device__ __forceinline__ float rbf(float f) { return __bfloat162float(__float2bfloat16(f)); }
__device__ __forceinline__ float mim(float f) { return MIMIC ? rbf(f) : f; }

__device__ void layernorm(const u16* x, float* xn, float* red) {
  const int t = threadIdx.x;
  for (int b = 0; b < B; ++b) {
    float v0 = bf2f(x[b * D + t]), v1 = bf2f(x[b * D + 256 + t]);
    float s = v0 + v1;
    for (int m = 32; m; m >>= 1) s += __shfl_xor(s, m);
    if ((t & 63) == 0) red[t >> 6] = s;
    __syncthreads();
    float mean = (red[0] + red[1] + red[2] + red[3]) * (1.f / D);
    __syncthreads();
    float q = (v0 - mean) * (v0 - mean) + (v1 - mean) * (v1 - mean);
    for (int m = 32; m; m >>= 1) q += __shfl_xor(q, m);
    if ((t & 63) == 0) red[t >> 6] = q;
    __syncthreads();
    float var = (red[0] + red[1] + red[2] + red[3]) * (1.f / D);
    __syncthreads();
    float mb = mim(mean), den = mim(sqrtf(mim(mim(var) + 1e-5f)));
    xn[b * D + t] = mim(mim(v0 - mb) / den);
    xn[b * D + 256 + t] = mim(mim(v1 - mb) / den);
  }
  __syncthreads();
}

__global__ void __launch_bounds__(256) k_pre(const u16* x, const u16* wq, const u16* w1, float* q, float* h) {
  __shared__ float xn[B * D], red[4], part[4][2][64];
  layernorm(x, xn, red);
  const int t = threadIdx.x, c = t & 63, g = t >> 6;
  const bool isq = blockIdx.x < 64;
  const u16* w = isq ? wq : w1;
  const int N = isq ? HQ * DH : F, col = (isq ? blockIdx.x : blockIdx.x - 64) * 64 + c;
  float a0 = 0, a1 = 0;
  for (int r = g * 128; r < g * 128 + 128; ++r) { float wv = bf2f(w[(size_t)r * N + col]); a0 += xn[r] * wv; a1 += xn[D + r] * wv; }
  part[g][0][c] = a0; part[g][1][c] = a1;
  __syncthreads();
  if (t < 128) {
    int b = t >> 6;
    float s = part[0][b][c] + part[1][b][c] + part[2][b][c] + part[3][b][c];
    s = mim(s);
    if (isq) q[b * HQ * DH + col] = s;
    else h[b * F + col] = mim(s / (1.f + __expf(-s)));
  }
}

__global__ void __launch_bounds__(256) k_attn(const u16* K, const u16* V, const float* q, float* pml, float* po) {
  __shared__ float sc[4][1024];
  __shared__ float red[4][4];
  __shared__ float acc_s[4][4][128];
  const int s = blockIdx.x, kvh = blockIdx.y, b = blockIdx.z, t = threadIdx.x, lane = t & 63, w = t >> 6, r = lane >> 4, c = lane & 15;
  constexpr int KPS = SK / NSPLIT;
  const u16* kb = K + ((size_t)(b * HKV + kvh) * SK + s * KPS) * DH;
  const u16* vb = V + ((size_t)(b * HKV + kvh) * SK + s * KPS) * DH;
  float qf[4][8];
  for (int g = 0; g < 4; ++g) for (int e = 0; e < 8; ++e) qf[g][e] = q[b * HQ * DH + (kvh * 4 + g) * DH + 8 * c + e];
  for (int k = w * 4 + r; k < KPS; k += 16) {
    v4u kv = __builtin_nontemporal_load((const v4u*)(kb + (size_t)k * DH + 8 * c));
    float kf[8];
    for (int e = 0; e < 4; ++e) { kf[2 * e] = __uint_as_float(kv[e] << 16); kf[2 * e + 1] = __uint_as_float(kv[e] & 0xffff0000u); }
    for (int g = 0; g < 4; ++g) {
      float d = 0; for (int e = 0; e < 8; ++e) d += kf[e] * qf[g][e];
      d += __shfl_xor(d, 1); d += __shfl_xor(d, 2); d += __shfl_xor(d, 4); d += __shfl_xor(d, 8);
      if (c == 0) sc[g][k] = mim(mim(d) * mim(0.08838834764831845f));
    }
  }
  __syncthreads();
  float m = -INFINITY;
  for (int k = lane; k < KPS; k += 64) m = fmaxf(m, sc[w][k]);
  for (int mm = 32; mm; mm >>= 1) m = fmaxf(m, __shfl_xor(m, mm));
  float l = 0;
  for (int k = lane; k < KPS; k += 64) { float p = __expf(sc[w][k] - m); sc[w][k] = p; l += p; }
  for (int mm = 32; mm; mm >>= 1) l += __shfl_xor(l, mm);
  const size_t slot = ((size_t)(b * HQ + kvh * 4 + w)) * NSPLIT + s;
  if (lane == 0) { pml[slot * 2] = m; pml[slot * 2 + 1] = l; }
  __syncthreads();
  float acc[4][8] = {};
  for (int k = w * 4 + r; k < KPS; k += 16) {
    v4u vv = __builtin_nontemporal_load((const v4u*)(vb + (size_t)k * DH + 8 * c));
    float vf[8];
    for (int e = 0; e < 4; ++e) { vf[2 * e] = __uint_as_float(vv[e] << 16); vf[2 * e + 1] = __uint_as_float(vv[e] & 0xffff0000u); }
    for (int g = 0; g < 4; ++g) { float p = sc[g][k]; for (int e = 0; e < 8; ++e) acc[g][e] += p * vf[e]; }
  }
  for (int g = 0; g < 4; ++g) for (int e = 0; e < 8; ++e) { float a = acc[g][e]; a += __shfl_xor(a, 16); a += __shfl_xor(a, 32); if (r == 0) acc_s[w][g][8 * c + e] = a; }
  __syncthreads();
  for (int i = t; i < 512; i += 256) { int g = i >> 7, d = i & 127;
    po[(((size_t)(b * HQ + kvh * 4 + g)) * NSPLIT + s) * DH + d] = acc_s[0][g][d] + acc_s[1][g][d] + acc_s[2][g][d] + acc_s[3][g][d]; }
}

__global__ void __launch_bounds__(128) k_comb(const float* pml, const float* po, float* o) {
  const int row = blockIdx.x, d = threadIdx.x;
  float M = -INFINITY;
  for (int s = 0; s < NSPLIT; ++s) M = fmaxf(M, pml[(row * NSPLIT + s) * 2]);
  float L = 0, a = 0;
  for (int s = 0; s < NSPLIT; ++s) { float w = __expf(pml[(row * NSPLIT + s) * 2] - M); L += w * pml[(row * NSPLIT + s) * 2 + 1]; a += w * po[((size_t)row * NSPLIT + s) * DH + d]; }
  o[row * DH + d] = mim(a / L);
}

__global__ void __launch_bounds__(256) k_out(const u16* x, const u16* wo, const u16* w2, const float* o, const float* h, __hip_bfloat16* out) {
  __shared__ float part[2][32][2][8];
  const int t = threadIdx.x, c = t & 7, g = t >> 3, col = blockIdx.x * 8 + c;
  float a0 = 0, a1 = 0, f0 = 0, f1 = 0;
  for (int r = g; r < HQ * DH; r += 32) { float wv = bf2f(wo[(size_t)r * D + col]); a0 += o[r] * wv; a1 += o[HQ * DH + r] * wv; }
  for (int r = g; r < F; r += 32) { float wv = bf2f(w2[(size_t)r * D + col]); f0 += h[r] * wv; f1 += h[F + r] * wv; }
  part[0][g][0][c] = a0; part[0][g][1][c] = a1; part[1][g][0][c] = f0; part[1][g][1][c] = f1;
  __syncthreads();
  if (t < 16) {
    int b = t >> 3; float s = 0, f = 0;
    for (int i = 0; i < 32; ++i) { s += part[0][i][b][c]; f += part[1][i][b][c]; }
    s = mim(s); f = mim(f);
    float gg = mim(f / (1.f + __expf(-f)));
    out[b * D + col] = __float2bfloat16(bf2f(x[b * D + col]) + mim(s * gg));
  }
}

extern "C" void kernel_launch(void* const* d_in, const int* in_sizes, int n_in, void* d_out, int out_size, void* d_ws, size_t ws_size, hipStream_t stream) {
  const u16* x = (const u16*)d_in[0]; const u16* K = (const u16*)d_in[1]; const u16* V = (const u16*)d_in[2];
  const u16* wq = (const u16*)d_in[3]; const u16* wo = (const u16*)d_in[4]; const u16* w1 = (const u16*)d_in[6]; const u16* w2 = (const u16*)d_in[7];
  float* ws = (float*)d_ws;
  float* q = ws; float* h = ws + 8192; float* o = ws + 16384; float* pml = ws + 32768; float* po = ws + 65536;
  k_pre<<<64 + 28, 256, 0, stream>>>(x, wq, w1, q, h);
  k_attn<<<dim3(NSPLIT, HKV, B), 256, 0, stream>>>(K, V, q, pml, po);
  k_comb<<<B * HQ, 128, 0, stream>>>(pml, po, o);
  k_out<<<64, 256, 0, stream>>>(x, wo, w2, o, h, (__hip_bfloat16*)d_out);
}
```

```cpp
#include <hip/hip_runtime.h>
#include <hip/hip_bf16.h>
#include <stdint.h>

#define WIN 20
#define DBG 0
#define TAILW 16
#define FFNPOS 1
#define W1NT 1
#define NOW1 0
#define WQFIRST 0
#define FFNAT 40
#define COLROT 0
#define NOCONS 0
#define NOFFN 0
#define NOQ 0
#define NOFLAG 0
#define NOPF 0
#define NOPFST 0
#define NOWQ 0
#define QSLEEP 4
#define PSLEEP 1

#define GAS __attribute__((address_space(1)))
#define LAS __attribute__((address_space(3)))
#define RLX_AGENT __ATOMIC_RELAXED, __HIP_MEMORY_SCOPE_AGENT
typedef unsigned long long u64;
typedef unsigned v4u __attribute__((ext_vector_type(4)));
typedef unsigned v2u __attribute__((ext_vector_type(2)));
typedef float f32x4 __attribute__((ext_vector_type(4)));
typedef float f32x2 __attribute__((ext_vector_type(2)));
typedef __bf16 bf16x8 __attribute__((ext_vector_type(8)));
typedef __bf16 bf16x4 __attribute__((ext_vector_type(4)));
typedef __bf16 bf16x2 __attribute__((ext_vector_type(2)));

constexpr int D = 512, SK = 32768, FF = 1792, NWG = 256, NFFN = FF / 8, NTILE = 32, NSLOT = 9, SLOT = 16384;
constexpr int SEQ_KV = 1, SEQ_WO = SEQ_KV + 2 * NTILE, SL_WO = (SEQ_WO % NSLOT) * SLOT;
constexpr int G_KV = 4 * SEQ_WO, G_WO = G_KV + 4;
constexpr unsigned SENT = 0xFFFFFFFFu;
constexpr int AUX_SC1 = 16, OOB = 0x7ffffff0;

constexpr int WS_EPOCH = 0, WS_FLAG = 4096, WS_QG = 8192, WS_P1 = 131072, WS_P2 = WS_P1 + NWG * 2048,
              WS_PF = WS_P2 + NWG * 1024, WS_END = WS_PF + NWG * 4096, WS_DBG = 131072 + 3 * 256 * 4096;

constexpr int L_CTRL = NSLOT * SLOT, L_XN = L_CTRL + 256, L_Q = L_XN + 2048, L_O = L_Q + 1024, L_W = L_O + 256, L_TR = L_W + 8192, L_BYTES = L_TR + (DBG == 1 ? 256 : 0);
constexpr int C_LANDED = 0, C_DEAD = 1, C_Q = 2, C_FLAG = 3, C_FREED = 4, C_O = 8;
constexpr int LDS_SPIN_CAP = 1 << 22, GLB_SPIN_CAP = 1 << 18;

__device__ __forceinline__ unsigned pk_bf16(float lo, float hi) {
  return __builtin_bit_cast(unsigned, __builtin_convertvector(f32x2{lo, hi}, bf16x2));
}
__device__ __forceinline__ float bf_lo(unsigned u) { return __uint_as_float(u << 16); }
__device__ __forceinline__ float bf_hi(unsigned u) { return __uint_as_float(u & 0xffff0000u); }
__device__ __forceinline__ float rbf(float f) { return bf_lo(pk_bf16(f, f)); }
template <int CTRL> __device__ __forceinline__ float dpp(float x) {
  return __builtin_bit_cast(float, __builtin_amdgcn_mov_dpp(__builtin_bit_cast(int, x), CTRL, 0xf, 0xf, true));
}
constexpr int XOR1 = 0xB1, XOR2 = 0x4E, ROR4 = 0x124, ROR8 = 0x128;
__device__ __forceinline__ float xrow_max(float x) {
  auto s = __builtin_amdgcn_permlane16_swap(__float_as_uint(x), __float_as_uint(x), false, false);
  x = fmaxf(__uint_as_float(s[0]), __uint_as_float(s[1]));
  auto t = __builtin_amdgcn_permlane32_swap(__float_as_uint(x), __float_as_uint(x), false, false);
  return fmaxf(__uint_as_float(t[0]), __uint_as_float(t[1]));
}
__device__ __forceinline__ float xrow_sum(float x) {
  auto s = __builtin_amdgcn_permlane16_swap(__float_as_uint(x), __float_as_uint(x), false, false);
  x = __uint_as_float(s[0]) + __uint_as_float(s[1]);
  auto t = __builtin_amdgcn_permlane32_swap(__float_as_uint(x), __float_as_uint(x), false, false);
  return __uint_as_float(t[0]) + __uint_as_float(t[1]);
}
__device__ __forceinline__ float quad_stride_sum(float x) {
  x += dpp<ROR8>(x); x += dpp<ROR4>(x);
  return xrow_sum(x);
}
__device__ __forceinline__ float wave_sum(float x) {
  x += dpp<XOR1>(x); x += dpp<XOR2>(x);
  return quad_stride_sum(x);
}
__device__ __forceinline__ f32x4 mfma32(v4u a, v4u b, f32x4 c) {
  return __builtin_amdgcn_mfma_f32_16x16x32_bf16(__builtin_bit_cast(bf16x8, a), __builtin_bit_cast(bf16x8, b), c, 0, 0, 0);
}
__device__ __forceinline__ v2u lds_tr(LAS char* p) {
  return __builtin_bit_cast(v2u, __builtin_amdgcn_ds_read_tr16_b64_v4bf16((LAS bf16x4*)p));
}
__device__ __forceinline__ v4u cat(v2u a, v2u b) { return v4u{a[0], a[1], b[0], b[1]}; }
__device__ __forceinline__ v4u not_sent(v4u u) {
  return v4u{min(u[0], SENT - 1), min(u[1], SENT - 1), min(u[2], SENT - 1), min(u[3], SENT - 1)};
}
__device__ __forceinline__ bool ready(v4u u) { return max(max(u[0], u[1]), max(u[2], u[3])) != SENT; }
__device__ __forceinline__ v4u refill(v4u cur, __amdgpu_buffer_rsrc_t rs, int off) {
  const bool ok = ready(cur);
  const v4u nv = __builtin_amdgcn_raw_buffer_load_b128(rs, ok ? OOB : off, 0, AUX_SC1);
  return ok ? cur : nv;
}
__device__ __forceinline__ float silu(float v) { return v / (1.f + __expf(-v)); }

__device__ __forceinline__ void dma1(unsigned voff, const char* sbase, unsigned ldsaddr) {
  asm volatile("s_mov_b32 m0, %2\n\ts_nop 0\n\tglobal_load_lds_dwordx4 %0, %1 nt" :: "v"(voff), "s"(sbase), "s"(ldsaddr) : "memory");
}
__device__ __forceinline__ void dma1c(unsigned voff, const char* sbase, unsigned ldsaddr) {
  asm volatile("s_mov_b32 m0, %2\n\ts_nop 0\n\tglobal_load_lds_dwordx4 %0, %1" :: "v"(voff), "s"(sbase), "s"(ldsaddr) : "memory");
}
__device__ __forceinline__ void dma4(unsigned v0, unsigned v1, unsigned v2, unsigned v3, const char* sbase, unsigned ldsaddr) {
  asm volatile("s_mov_b32 m0, %5\n\ts_nop 0\n\t"
               "global_load_lds_dwordx4 %0, %4 nt\n\tglobal_load_lds_dwordx4 %1, %4 offset:1024 nt\n\t"
               "global_load_lds_dwordx4 %2, %4 offset:2048 nt\n\tglobal_load_lds_dwordx4 %3, %4 offset:3072 nt"
               :: "v"(v0), "v"(v1), "v"(v2), "v"(v3), "s"(sbase), "s"(ldsaddr) : "memory");
}
template <int N> __device__ __forceinline__ void wait_vm() { asm volatile("s_waitcnt vmcnt(%0)" :: "i"(N) : "memory"); }
__device__ __forceinline__ void wait_lgkm() { asm volatile("s_waitcnt lgkmcnt(0)" ::: "memory"); }

typedef volatile LAS unsigned* ctrl_t;
template <int SLEEP = 0> __device__ __forceinline__ unsigned lds_wait_ge(ctrl_t ctrl, int word, unsigned want) {
  unsigned v;
  for (int n = 0; (v = __builtin_amdgcn_readfirstlane(ctrl[word])) < want; ++n) {
    if ((n & 255) == 255) {
      if (n > LDS_SPIN_CAP) ctrl[C_DEAD] = 1u;
      if (__builtin_amdgcn_readfirstlane(ctrl[C_DEAD])) break;
    }
    if (SLEEP) __builtin_amdgcn_s_sleep(SLEEP);
  }
  asm volatile("" ::: "memory");
  return v;
}
__device__ __forceinline__ bool glb_give_up(ctrl_t ctrl, int& n) {
  if ((++n & 63) != 0) return false;
  if (n > GLB_SPIN_CAP) ctrl[C_DEAD] = 1u;
  return __builtin_amdgcn_readfirstlane(ctrl[C_DEAD]) != 0;
}

#define STAMP(i) do {} while (0)
#define STAMP2(i) do {} while (0)
#define FLUSH() do {} while (0)

__global__ void __launch_bounds__(384) fused_decode(const char* __restrict__ x, const char* __restrict__ kc, const char* __restrict__ vc,
                                                    const char* __restrict__ wq, const char* __restrict__ wo, const char* __restrict__ w1,
                                                    const char* __restrict__ w2, char* __restrict__ out, char* ws) {
  extern __shared__ __attribute__((aligned(16))) char smem[];
  LAS char* L = (LAS char*)smem;
  ctrl_t ctrl = (ctrl_t)(L + L_CTRL);
  const int lane = threadIdx.x & 63, wave = __builtin_amdgcn_readfirstlane(threadIdx.x >> 6), bid = blockIdx.x;
  const int xg = bid & 7, tg = bid >> 3, kvh = tg >> 2, gi = 4 * xg + (tg & 3), wid = kvh * 32 + gi;
  const int bb = gi >> 4, split = gi & 15, qc = COLROT ? 4 * ((xg + kvh) & 7) + (tg & 3) : gi;
  const int g = lane >> 4, col = lane & 15, q4 = (lane >> 2) & 3, p4 = lane & 3;
  const int ffn = xg * 28 + tg;
  const bool has_ffn = tg < 28 && !NOFFN;
  const __amdgpu_buffer_rsrc_t rs = __builtin_amdgcn_make_buffer_rsrc((void*)ws, 0, WS_END, 0x00020000);

  if (wave == 0) {
    __builtin_amdgcn_s_setprio(3);
    STAMP2(0);
    const unsigned lbase = (unsigned)(uintptr_t)L;
    if (lane < 16) ctrl[lane] = 0u;
    wait_lgkm();
    if (WQFIRST) __builtin_amdgcn_s_barrier();
    int issued = 4, slot = SEQ_KV, pub = 0;
    auto throttle = [&]() {
      ++issued;
      wait_vm<WIN>();
      pub = max(pub, issued - WIN / 4);
      if (lane == 0) ctrl[C_LANDED] = pub;
    };
    auto wait_free = [&](int seq) {
      if (seq < NSLOT || NOCONS) return;
      const unsigned need = seq - NSLOT + 1;
      for (int n = 0;; ++n) {
        const v4u f = *(volatile LAS v4u*)(ctrl + C_FREED);
        if ((unsigned)__builtin_amdgcn_readfirstlane(min(min(f[0], f[1]), min(f[2], f[3]))) >= need) break;
        if (n == 0) {
          wait_vm<0>(); pub = issued; if (lane == 0) ctrl[C_LANDED] = pub;
        }
        if ((n & 255) == 255) {
          if (n > LDS_SPIN_CAP) ctrl[C_DEAD] = 1u;
          if (__builtin_amdgcn_readfirstlane(ctrl[C_DEAD])) break;
        }
        __builtin_amdgcn_s_sleep(1);
      }
      asm volatile("" ::: "memory");
    };
    const size_t kvoff = ((size_t)(bb * 8 + kvh) * SK + split * 2048) * 256;
    const char* kb = kc + kvoff;
    const char* vb = vc + kvoff;
    unsigned vk[4], vv[2];
#pragma unroll
    for (int j = 0; j < 4; ++j) vk[j] = g * 256 + ((col ^ (4 * j + g)) * 16);
#pragma unroll
    for (int j = 0; j < 2; ++j) vv[j] = g * 256 + ((col ^ ((4 * j + g) << 1)) * 16);
    for (int t = 0; t < NTILE; ++t) {
      wait_free(SEQ_KV + 2 * t);
      unsigned la = lbase + slot * SLOT;
#pragma unroll
      for (int j = 0; j < 4; ++j) {
        dma4(vk[0], vk[1], vk[2], vk[3], kb + t * SLOT + j * 4096, la + j * 4096);
        if (!WQFIRST && t == 0 && j == 3) __builtin_amdgcn_s_barrier();
        throttle();
      }
      slot = slot + 1 == NSLOT ? 0 : slot + 1;
      wait_free(SEQ_KV + 2 * t + 1);
      la = lbase + slot * SLOT;
#pragma unroll
      for (int j = 0; j < 4; ++j) { dma4(vv[0], vv[1], vv[0], vv[1], vb + t * SLOT + j * 4096, la + j * 4096); throttle(); }
      slot = slot + 1 == NSLOT ? 0 : slot + 1;
    }
    STAMP(1);
#define DRAIN_KV(N) if (WIN > (N) && (N) >= TAILW) { wait_vm<(N)>(); pub = max(pub, G_KV - (N) / 4); if (lane == 0) ctrl[C_LANDED] = pub; }
    DRAIN_KV(28) DRAIN_KV(24) DRAIN_KV(20) DRAIN_KV(16) DRAIN_KV(12) DRAIN_KV(8) DRAIN_KV(4) DRAIN_KV(0)
#undef DRAIN_KV
    wait_free(SEQ_WO);
    {
      const int dq = gi >> 2;
      const char* src = wo + (size_t)(kvh * 512 + 32 * (dq >> 1) + 8 * (dq & 1)) * 1024 + (gi & 3) * 256;
      const unsigned v0 = vv[0] + g * 768, v1 = vv[1] + g * 768;
#pragma unroll
      for (int j = 0; j < 16; ++j)
        dma1((j & 1) ? v1 : v0, src + ((j & 3) * 128 + ((j >> 2) & 1) * 16 + (j >> 3) * 4) * 1024, lbase + SL_WO + j * 1024);
    }
#define DRAIN_TO(N) if ((TAILW < WIN ? TAILW : WIN) + 16 > (N)) { wait_vm<(N)>(); pub = max(pub, G_WO - (N) / 4); if (lane == 0) ctrl[C_LANDED] = pub; }
    DRAIN_TO(44) DRAIN_TO(40) DRAIN_TO(36) DRAIN_TO(32) DRAIN_TO(28) DRAIN_TO(24) DRAIN_TO(20) DRAIN_TO(16)
    DRAIN_TO(0)
#undef DRAIN_TO
    STAMP(2);
    FLUSH();
    return;
  }

  if (wave == 5) {
    const unsigned lbase = (unsigned)(uintptr_t)L;
    u64 tag;
    v4u x0, x1;
    asm volatile("global_load_dwordx2 %0, %3, %4 sc1\n\tglobal_load_dwordx4 %1, %5, %6\n\tglobal_load_dwordx4 %2, %5, %6 offset:1024"
                 : "=&v"(tag), "=&v"(x0), "=&v"(x1) : "v"(WS_EPOCH), "s"(ws), "v"(lane * 16), "s"(x) : "memory");
    {
      const unsigned vo = (lane >> 1) * 8192 + (lane & 1) * 16;
      const char* src = wq + (kvh * 512 + qc * 16) * 2;
#pragma unroll
      for (int j = 0; j < 16; ++j) dma1(NOWQ ? lane * 16 : vo, NOWQ ? wq + bid * 16384 + j * 1024 : src + (size_t)j * (32 * 8192), lbase + j * 1024);
    }
    auto load_w1 = [&]() {
      const unsigned vo = NOW1 ? lane * 16 : lane * (FF * 2);
#pragma unroll
      for (int j = 0; j < 8; ++j) {
        const char* src = NOW1 ? w1 + ffn * 8192 + j * 1024 : w1 + ffn * 16 + (size_t)j * (64 * FF * 2);
        if (W1NT) dma1(vo, src, lbase + L_W + j * 1024); else dma1c(vo, src, lbase + L_W + j * 1024);
      }
    };
    __builtin_amdgcn_s_barrier();
    asm volatile("s_waitcnt vmcnt(16)" : "+v"(x0), "+v"(x1), "+v"(tag) :: "memory");
    tag += 1;
    if (FFNPOS == 0 && has_ffn) load_w1();
    asm volatile("" ::: "memory");
#pragma unroll
    for (int b = 0; b < 2; ++b) {
      const v4u xv = b ? x1 : x0;
      float v[8];
#pragma unroll
      for (int e = 0; e < 4; ++e) { v[2 * e] = bf_lo(xv[e]); v[2 * e + 1] = bf_hi(xv[e]); }
      const float mean = wave_sum(((v[0] + v[1]) + (v[2] + v[3])) + ((v[4] + v[5]) + (v[6] + v[7]))) * (1.f / D);
      float sq = 0.f;
#pragma unroll
      for (int e = 0; e < 8; ++e) sq = fmaf(v[e] - mean, v[e] - mean, sq);
      const float var = wave_sum(sq) * (1.f / D);
      const float mb = rbf(mean), den = rbf(sqrtf(rbf(rbf(var) + 1e-5f))), rden = 1.f / den;
      v4u o;
#pragma unroll
      for (int e = 0; e < 4; ++e) {
        const unsigned dp = pk_bf16(v[2 * e] - mb, v[2 * e + 1] - mb);
        const float d0 = bf_lo(dp), d1 = bf_hi(dp), q0 = d0 * rden, q1 = d1 * rden;
        o[e] = pk_bf16(fmaf(fmaf(-q0, den, d0), rden, q0), fmaf(fmaf(-q1, den, d1), rden, q1));
      }
      *(LAS v4u*)(L + L_XN + b * 1024 + lane * 16) = o;
    }
    v4u xb[16];
    {
      LAS char* xa = L + L_XN + (((col >> 1) & 1) * 512 + 4 * g) * 2;
#pragma unroll
      for (int ks = 0; ks < 16; ++ks) xb[ks] = cat(*(LAS v2u*)(xa + ks * 64), *(LAS v2u*)(xa + ks * 64 + 32));
    }
    __builtin_amdgcn_sched_barrier(0);
    STAMP(0);
    if (FFNPOS == 0 && has_ffn) wait_vm<8>(); else wait_vm<0>();
    STAMP(1);
    {
      f32x4 qp[2] = {{0.f, 0.f, 0.f, 0.f}, {0.f, 0.f, 0.f, 0.f}};
      LAS char* wa = L + 8 * lane;
      v4u fa[16];
#pragma unroll
      for (int ks = 0; ks < 16; ++ks) fa[ks] = cat(lds_tr(wa + ks * 1024), lds_tr(wa + ks * 1024 + 512));
      __builtin_amdgcn_sched_barrier(0);
#pragma unroll
      for (int ks = 0; ks < 16; ++ks) qp[ks & 1] = mfma32(fa[ks], xb[ks], qp[ks & 1]);
      const f32x4 qa = qp[0] + qp[1];
      const int r = (col & 1) | ((col >> 1) & 2);
      const float qv = r == 0 ? qa[0] : r == 1 ? qa[1] : r == 2 ? qa[2] : qa[3];
      const u64 gran = (tag << 16) | (pk_bf16(qv, qv) & 0xffffu);
      if (col < 8) __hip_atomic_store((GAS u64*)(ws + WS_QG) + (kvh * 2 + ((col >> 1) & 1)) * 512 + qc * 16 + 4 * g + r, gran, RLX_AGENT);
    }
    if (lane < 4) ctrl[C_FREED + lane] = 1u;
    STAMP(2);
    f32x4 fo[4];
    auto ffn_slice = [&]() {
      if (FFNPOS != 0) load_w1();
      wait_vm<0>();
      f32x4 hp[2] = {{0.f, 0.f, 0.f, 0.f}, {0.f, 0.f, 0.f, 0.f}};
      LAS char* wa = L + L_W + (4 * g + q4) * 16 + 8 * (p4 & 1);
      v4u fa[16];
#pragma unroll
      for (int ks = 0; ks < 16; ++ks) fa[ks] = cat(lds_tr(wa + ks * 512), lds_tr(wa + ks * 512 + 256));
      __builtin_amdgcn_sched_barrier(0);
      wait_lgkm();
      dma4(lane * 16, (lane ^ 2) * 16, (lane ^ 4) * 16, (lane ^ 6) * 16, w2 + (size_t)ffn * 8192, lbase + L_W);
      dma4((lane ^ 8) * 16, (lane ^ 10) * 16, (lane ^ 12) * 16, (lane ^ 14) * 16, w2 + (size_t)ffn * 8192 + 4096, lbase + L_W + 4096);
#pragma unroll
      for (int ks = 0; ks < 16; ++ks) hp[ks & 1] = mfma32(fa[ks], xb[ks], hp[ks & 1]);
      const f32x4 h = hp[0] + hp[1];
      const v2u hb = {pk_bf16(silu(rbf(h[0])), silu(rbf(h[1]))), pk_bf16(silu(rbf(h[2])), silu(rbf(h[3])))};
      wait_vm<0>();
      const int row = 4 * (g & 1) + q4;
      LAS char* w2a = L + L_W + row * 1024 + 8 * (p4 & 1);
      v2u wf[32];
#pragma unroll
      for (int n = 0; n < 32; ++n) wf[n] = lds_tr(w2a + (((2 * n + (p4 >> 1)) ^ (2 * row)) * 16));
      __builtin_amdgcn_sched_barrier(0);
#pragma unroll
      for (int n = 0; n < 4; ++n) {
        const bool on = g < 2 && (col >> 2) == n, on0 = on && !(col & 1), on1 = on && (col & 1);
        const v4u bm = {on0 ? hb[0] : 0u, on0 ? hb[1] : 0u, on1 ? hb[0] : 0u, on1 ? hb[1] : 0u};
#pragma unroll
        for (int c = 0; c < 4; ++c) fo[c] = mfma32(cat(wf[8 * c + 2 * n], wf[8 * c + 2 * n + 1]), bm, n ? fo[c] : f32x4{0.f, 0.f, 0.f, 0.f});
      }
    };
    {
      const int qo = WS_QG + (kvh * 2 + bb) * 4096 + lane * 8;
      const unsigned thi = (unsigned)(tag >> 16), tlo = (unsigned)tag << 16;
      v2u a[8];
#pragma unroll
      for (int k = 0; k < 8; ++k) a[k] = v2u{~tlo, ~thi};
      for (int n = 0; !NOQ;) {
        asm volatile("" ::: "memory");
        bool all = true;
        v2u nv[8];
#pragma unroll
        for (int k = 0; k < 8; ++k) {
          const bool ok = a[k][1] == thi && ((a[k][0] ^ tlo) >> 16) == 0;
          nv[k] = __builtin_amdgcn_raw_buffer_load_b64(rs, ok ? OOB : qo + 512 * k, 0, AUX_SC1);
        }
#pragma unroll
        for (int k = 0; k < 8; ++k) {
          const bool ok = a[k][1] == thi && ((a[k][0] ^ tlo) >> 16) == 0;
          a[k] = ok ? a[k] : nv[k];
          all = all && a[k][1] == thi && ((a[k][0] ^ tlo) >> 16) == 0;
        }
        if (__all(all) || glb_give_up(ctrl, n)) break;
        if (QSLEEP) __builtin_amdgcn_s_sleep(QSLEEP);
      }
#pragma unroll
      for (int k = 0; k < 8; ++k) *(LAS unsigned short*)(L + L_Q + (64 * k + lane) * 2) = (unsigned short)a[k][0];
      wait_lgkm();
      if (lane == 0) ctrl[C_Q] = 1u;
    }
    STAMP(3);
    {
      const v4u sv = {SENT, SENT, SENT, SENT};
      if (has_ffn) {
        if (FFNPOS == 2) lds_wait_ge<8>(ctrl, C_LANDED, FFNAT);
        ffn_slice();
#pragma unroll
        for (int c = 0; c < 4; ++c)
          __builtin_amdgcn_raw_buffer_store_b128(__builtin_bit_cast(v4u, fo[c]), rs, NOPFST ? OOB : WS_PF + ffn * 4096 + c * 1024 + lane * 16, 0, AUX_SC1);
      }
      __builtin_amdgcn_raw_buffer_store_b128(sv, rs, WS_P1 + wid * 2048 + lane * 16, 0, AUX_SC1);
      __builtin_amdgcn_raw_buffer_store_b128(sv, rs, WS_P1 + wid * 2048 + 1024 + lane * 16, 0, AUX_SC1);
      __builtin_amdgcn_raw_buffer_store_b128(sv, rs, WS_P2 + wid * 1024 + lane * 16, 0, AUX_SC1);
      wait_vm<0>();
      if (lane == 0) __hip_atomic_store((GAS u64*)(ws + WS_FLAG) + bid, tag, RLX_AGENT);
    }
    STAMP(4);
    lds_wait_ge<8>(ctrl, C_LANDED, 4 * 32);
    for (int n = 0; !NOFLAG;) {
      u64 f[4];
#pragma unroll
      for (int k = 0; k < 4; ++k) f[k] = __hip_atomic_load((GAS u64*)(ws + WS_FLAG) + k * 64 + lane, RLX_AGENT);
      if (__all(((f[0] ^ tag) | (f[1] ^ tag) | (f[2] ^ tag) | (f[3] ^ tag)) == 0) || glb_give_up(ctrl, n)) break;
      __builtin_amdgcn_s_sleep(8);
    }
    asm volatile("" ::: "memory");
    if (lane == 0) ctrl[C_FLAG] = 1u;
    if (bid == 0 && lane == 0) __hip_atomic_store((GAS u64*)(ws + WS_EPOCH), tag, RLX_AGENT);
    if (bid >= 64) { FLUSH(); return; }

    const int rho = xg * 8 + tg, cb = rho >> 4, cw = (rho >> 2) & 3, gw = rho & 3;
    const int oidx = (p4 >> 1) * 512 + cb * 128 + cw * 32 + (p4 & 1) * 16 + gw * 4;
    const v2u xin = *(const GAS v2u*)(x + oidx * 2);
    f32x4 gate = {0.f, 0.f, 0.f, 0.f};
    {
      v4u fv[NFFN / 16];
#pragma unroll
      for (int k = 0; k < NFFN / 16; ++k)
        fv[k] = __builtin_amdgcn_raw_buffer_load_b128(rs, NOPF ? OOB : WS_PF + ((lane >> 2) + 16 * k) * 4096 + cb * 1024 + gw * 256 + cw * 64 + p4 * 16, 0, AUX_SC1);
#pragma unroll
      for (int k = 0; k < NFFN / 16; ++k) gate += __builtin_bit_cast(f32x4, fv[k]);
#pragma unroll
      for (int j = 0; j < 4; ++j) gate[j] = rbf(silu(rbf(quad_stride_sum(gate[j]))));
    }
    asm volatile("" : "+v"(gate));
    lds_wait_ge<8>(ctrl, C_LANDED, G_KV - 8);
    const int po = WS_P2 + (4 * (lane >> 2) + cb) * 1024 + cw * 256 + gw * 64 + p4 * 16;
    v4u sv[4];
#pragma unroll
    for (int k = 0; k < 4; ++k) sv[k] = v4u{SENT, SENT, SENT, SENT};
    for (int n = 0;;) {
      asm volatile("" ::: "memory");
#pragma unroll
      for (int k = 0; k < 4; ++k) sv[k] = refill(sv[k], rs, po + k * (64 * 1024));
      if (__all(ready(sv[0]) && ready(sv[1]) && ready(sv[2]) && ready(sv[3])) || glb_give_up(ctrl, n)) break;
      if (PSLEEP) __builtin_amdgcn_s_sleep(PSLEEP);
    }
    STAMP(5);
    const f32x4 ss = (__builtin_bit_cast(f32x4, sv[0]) + __builtin_bit_cast(f32x4, sv[1])) + (__builtin_bit_cast(f32x4, sv[2]) + __builtin_bit_cast(f32x4, sv[3]));
    float y[4];
#pragma unroll
    for (int j = 0; j < 4; ++j)
      y[j] = ((j & 1) ? bf_hi(xin[j >> 1]) : bf_lo(xin[j >> 1])) + rbf(rbf(quad_stride_sum(ss[j])) * gate[j]);
    v2u yo = {pk_bf16(y[0], y[1]), pk_bf16(y[2], y[3])};
    if (ctrl[C_DEAD]) yo = v2u{0x7fc07fc0u, 0x7fc07fc0u};
    if (lane < 4) *(GAS v2u*)(out + oidx * 2) = yo;
    STAMP2(6);
    FLUSH();
    return;
  }

  const int c = wave - 1;
  __builtin_amdgcn_s_barrier();
  asm volatile("" ::: "memory");
  lds_wait_ge<2>(ctrl, C_Q, 1u);
  const int blk = col >> 2;
  v4u qf[4][4];
#pragma unroll
  for (int k = 0; k < 4; ++k) {
    const v4u qv = *(LAS v4u*)(L + L_Q + ((col & 3) * 128 + 32 * k + 8 * g) * 2);
#pragma unroll
    for (int a = 0; a < 4; ++a) qf[a][k] = blk == a ? qv : v4u{0u, 0u, 0u, 0u};
  }
  unsigned ka[4], va[2];
#pragma unroll
  for (int k = 0; k < 4; ++k) ka[k] = col * 256 + (((4 * k + g) ^ col) * 16);
#pragma unroll
  for (int n = 0; n < 2; ++n)
    va[n] = (4 * g + q4) * 256 + (((4 * c + 2 * n + (p4 >> 1)) ^ ((4 * (g & 1) + q4) << 1)) * 16) + 8 * (p4 & 1);

  constexpr float SCALE = 0.08838834764831845f, LOG2E = 1.4426950408889634f;
  float ms = -3.0e38f, lsum = 0.f;
  f32x4 acc0 = {0.f, 0.f, 0.f, 0.f}, acc1 = {0.f, 0.f, 0.f, 0.f};
  int slot = SEQ_KV;
  unsigned seen = 0;
  auto need = [&](unsigned want) {
    if (seen < want) seen = lds_wait_ge(ctrl, C_LANDED, want);
    asm volatile("" ::: "memory");
  };
  if (NOCONS) lds_wait_ge<32>(ctrl, C_LANDED, G_KV - 8);
  for (int t = NOCONS ? NTILE - 1 : 0; t < NTILE; ++t) {
    const int seq = SEQ_KV + 2 * t;
    LAS char* sb = L + slot * SLOT;
    slot = slot + 1 == NSLOT ? 0 : slot + 1;
    need(4 * seq + 4);
    v4u kf[4][4];
#pragma unroll
    for (int k = 0; k < 4; ++k)
#pragma unroll
      for (int a = 0; a < 4; ++a) kf[a][k] = *(LAS v4u*)(sb + ka[k] + a * 4096);
    __builtin_amdgcn_sched_barrier(0);
    f32x4 s0 = {0.f, 0.f, 0.f, 0.f}, s1 = {0.f, 0.f, 0.f, 0.f};
#pragma unroll
    for (int k = 0; k < 4; ++k) {
      s0 = mfma32(kf[0][k], qf[0][k], s0); s1 = mfma32(kf[1][k], qf[1][k], s1);
      s0 = mfma32(kf[2][k], qf[2][k], s0); s1 = mfma32(kf[3][k], qf[3][k], s1);
    }
    __builtin_amdgcn_sched_barrier(0);
    wait_lgkm();
    if (lane == 0) ctrl[C_FREED + c] = seq + 1;
    s0 += s1;
    float s[4];
#pragma unroll
    for (int j = 0; j < 2; ++j) {
      const unsigned a = pk_bf16(s0[2 * j], s0[2 * j + 1]), b = pk_bf16(bf_lo(a) * SCALE, bf_hi(a) * SCALE);
      s[2 * j] = bf_lo(b); s[2 * j + 1] = bf_hi(b);
    }
    float mx = fmaxf(fmaxf(s[0], s[1]), fmaxf(s[2], s[3]));
    mx = fmaxf(mx, dpp<ROR8>(mx)); mx = fmaxf(mx, dpp<ROR4>(mx)); mx = xrow_max(mx);
    const float mn = fmaxf(ms, mx * LOG2E), alpha = __builtin_amdgcn_exp2f(ms - mn);
    ms = mn;
#pragma unroll
    for (int j = 0; j < 4; ++j) s[j] = __builtin_amdgcn_exp2f(fmaf(s[j], LOG2E, -mn));
    lsum = fmaf(lsum, alpha, (s[0] + s[1]) + (s[2] + s[3]));
    const unsigned p0 = pk_bf16(s[0], s[1]), p1 = pk_bf16(s[2], s[3]);
    v4u pA = {blk == 0 ? p0 : 0u, blk == 0 ? p1 : 0u, blk == 1 ? p0 : 0u, blk == 1 ? p1 : 0u};
    v4u pB = {blk == 2 ? p0 : 0u, blk == 2 ? p1 : 0u, blk == 3 ? p0 : 0u, blk == 3 ? p1 : 0u};
    acc0 *= alpha; acc1 *= alpha;
    asm volatile("" : "+v"(pA), "+v"(pB), "+v"(acc0), "+v"(acc1), "+v"(lsum));
    sb = L + slot * SLOT;
    slot = slot + 1 == NSLOT ? 0 : slot + 1;
    need(4 * seq + 6);
    acc0 = mfma32(cat(lds_tr(sb + va[0]), lds_tr(sb + va[0] + 4096)), pA, acc0);
    acc1 = mfma32(cat(lds_tr(sb + va[1]), lds_tr(sb + va[1] + 4096)), pA, acc1);
    need(4 * seq + 8);
    v4u v0 = cat(lds_tr(sb + va[0] + 8192), lds_tr(sb + va[0] + 12288)), v1 = cat(lds_tr(sb + va[1] + 8192), lds_tr(sb + va[1] + 12288));
    wait_lgkm();
    if (lane == 0) ctrl[C_FREED + c] = seq + 2;
    asm volatile("" : "+v"(v0), "+v"(v1) :: "memory");
    acc0 = mfma32(v0, pB, acc0);
    acc1 = mfma32(v1, pB, acc1);
  }
  STAMP(0);

  lds_wait_ge(ctrl, C_FLAG, 1u);
  {
    const float lt = quad_stride_sum(lsum), inv = 1.f / lt;
    const unsigned lam = __float_as_uint(ms + __builtin_amdgcn_logf(lt));
    v4u pk;
#pragma unroll
    for (int j = 0; j < 4; ++j) {
      float a0 = acc0[j], a1 = acc1[j];
      a0 += dpp<ROR8>(a0); a0 += dpp<ROR4>(a0);
      a1 += dpp<ROR8>(a1); a1 += dpp<ROR4>(a1);
      const unsigned u = __float_as_uint(((col & 4) ? a1 : a0) * inv);
      pk[j] = ((u + 0x80u) & 0xffffff00u) | ((lam >> (8 * j)) & 0xffu);
    }
    if (col < 8) __builtin_amdgcn_raw_buffer_store_b128(not_sent(pk), rs, WS_P1 + wid * 2048 + c * 512 + g * 128 + col * 16, 0, AUX_SC1);
  }
  STAMP(1);
  {
    const int rec = WS_P1 + (kvh * 32 + (c >> 1) * 16 + (lane >> 3)) * 2048 + (gi >> 2) * 256 + (c & 1) * 128 + (lane & 7) * 16;
    v4u a0 = {SENT, SENT, SENT, SENT}, a1 = a0, wa[4];
    a0 = refill(a0, rs, rec); a1 = refill(a1, rs, rec + 8 * 2048);
    __builtin_amdgcn_sched_barrier(0);
    need(G_WO);
    LAS char* wp = L + SL_WO;
#pragma unroll
    for (int n = 0; n < 4; ++n) wa[n] = cat(lds_tr(wp + va[n & 1] + (n >> 1) * 8192), lds_tr(wp + va[n & 1] + (n >> 1) * 8192 + 4096));
    __builtin_amdgcn_sched_barrier(0);
    STAMP(6);
    for (int n = 0; !__all(ready(a0) && ready(a1)) && !glb_give_up(ctrl, n);) {
      if (PSLEEP) __builtin_amdgcn_s_sleep(PSLEEP);
      asm volatile("" ::: "memory");
      a0 = refill(a0, rs, rec); a1 = refill(a1, rs, rec + 8 * 2048);
    }
    STAMP(2);
    const float lA = __uint_as_float((a0[0] & 0xffu) | ((a0[1] & 0xffu) << 8) | ((a0[2] & 0xffu) << 16) | (a0[3] << 24));
    const float lB = __uint_as_float((a1[0] & 0xffu) | ((a1[1] & 0xffu) << 8) | ((a1[2] & 0xffu) << 16) | (a1[3] << 24));
    float M = fmaxf(lA, lB);
    M = xrow_max(fmaxf(M, dpp<ROR8>(M)));
    const float wA = __builtin_amdgcn_exp2f(lA - M), wB = __builtin_amdgcn_exp2f(lB - M);
    float r[5];
    r[4] = wA + wB;
#pragma unroll
    for (int j = 0; j < 4; ++j) r[j] = fmaf(wA, __uint_as_float(a0[j] & 0xffffff00u), wB * __uint_as_float(a1[j] & 0xffffff00u));
#pragma unroll
    for (int j = 0; j < 5; ++j) r[j] = xrow_sum(r[j] + dpp<ROR8>(r[j]));
    const float inv = 1.f / r[4];
    if (lane < 8) *(LAS v2u*)(L + L_O + c * 64 + lane * 8) = v2u{pk_bf16(r[0] * inv, r[1] * inv), pk_bf16(r[2] * inv, r[3] * inv)};
    wait_lgkm();
    if (lane == 0) __hip_atomic_fetch_add((LAS unsigned*)(ctrl + C_O), 1u, __ATOMIC_RELAXED, __HIP_MEMORY_SCOPE_WORKGROUP);
    lds_wait_ge(ctrl, C_O, 4u);
    LAS char* op = L + L_O + ((col >> 1) & 1) * 128 + g * 8;
    const v4u o0 = cat(*(LAS v2u*)op, *(LAS v2u*)(op + 32)), o1 = cat(*(LAS v2u*)(op + 64), *(LAS v2u*)(op + 96));
    const v4u z = {0u, 0u, 0u, 0u};
    f32x4 sp = {0.f, 0.f, 0.f, 0.f};
    sp = mfma32(wa[0], (col & 1) ? z : o0, sp);
    sp = mfma32(wa[1], (col & 1) ? o0 : z, sp);
    sp = mfma32(wa[2], (col & 1) ? z : o1, sp);
    sp = mfma32(wa[3], (col & 1) ? o1 : z, sp);
    if (col < 4) __builtin_amdgcn_raw_buffer_store_b128(not_sent(__builtin_bit_cast(v4u, sp)), rs, WS_P2 + wid * 1024 + c * 256 + g * 64 + col * 16, 0, AUX_SC1);
  }
  STAMP(3);
  FLUSH();
}

extern "C" void kernel_launch(void* const* d_in, const int* in_sizes, int n_in, void* d_out, int out_size, void* d_ws, size_t ws_size,
                              hipStream_t stream) {
  (void)hipFuncSetAttribute((const void*)fused_decode, hipFuncAttributeMaxDynamicSharedMemorySize, L_BYTES);
  hipLaunchKernelGGL(fused_decode, dim3(NWG), dim3(384), L_BYTES, stream, (const char*)d_in[0], (const char*)d_in[1], (const char*)d_in[2],
                     (const char*)d_in[3], (const char*)d_in[4], (const char*)d_in[6], (const char*)d_in[7], (char*)d_out, (char*)d_ws);
}
```

```cpp
#include <hip/hip_runtime.h>
#include <hip/hip_bf16.h>
#include <stdint.h>

#define WIN 20
#define DBG 0
#define TAILW 16
#define FFNPOS 1
#define W1NT 1
#define NOW1 0
#define WQFIRST 0
#define FFNAT 40
#define COLROT 0
#define NOCONS 0
#define NOFFN 0
#define NOQ 0
#define NOFLAG 0
#define NOPF 0
#define NOPFST 0
#define NOWQ 0
#define KPOL 1
#define VPOL 3
#define QSLEEP 4
#define PSLEEP 1

#define GAS __attribute__((address_space(1)))
#define LAS __attribute__((address_space(3)))
#define RLX_AGENT __ATOMIC_RELAXED, __HIP_MEMORY_SCOPE_AGENT
typedef unsigned long long u64;
typedef unsigned v4u __attribute__((ext_vector_type(4)));
typedef unsigned v2u __attribute__((ext_vector_type(2)));
typedef float f32x4 __attribute__((ext_vector_type(4)));
typedef float f32x2 __attribute__((ext_vector_type(2)));
typedef __bf16 bf16x8 __attribute__((ext_vector_type(8)));
typedef __bf16 bf16x4 __attribute__((ext_vector_type(4)));
typedef __bf16 bf16x2 __attribute__((ext_vector_type(2)));

constexpr int D = 512, SK = 32768, FF = 1792, NWG = 256, NFFN = FF / 8, NTILE = 32, NSLOT = 9, SLOT = 16384;
constexpr int SEQ_KV = 1, SEQ_WO = SEQ_KV + 2 * NTILE, SL_WO = (SEQ_WO % NSLOT) * SLOT;
constexpr int G_KV = 4 * SEQ_WO, G_WO = G_KV + 4;
constexpr unsigned SENT = 0xFFFFFFFFu;
constexpr int AUX_SC1 = 16, OOB = 0x7ffffff0;

constexpr int WS_EPOCH = 0, WS_FLAG = 4096, WS_QG = 8192, WS_P1 = 131072, WS_P2 = WS_P1 + NWG * 2048,
              WS_PF = WS_P2 + NWG * 1024, WS_END = WS_PF + NWG * 4096, WS_DBG = 131072 + 3 * 256 * 4096;

constexpr int L_CTRL = NSLOT * SLOT, L_XN = L_CTRL + 256, L_Q = L_XN + 2048, L_O = L_Q + 1024, L_W = L_O + 256, L_TR = L_W + 8192, L_BYTES = L_TR + (DBG == 1 ? 256 : 0);
constexpr int C_LANDED = 0, C_DEAD = 1, C_Q = 2, C_FLAG = 3, C_FREED = 4, C_O = 8;
constexpr int LDS_SPIN_CAP = 1 << 22, GLB_SPIN_CAP = 1 << 18;

__device__ __forceinline__ unsigned pk_bf16(float lo, float hi) {
  return __builtin_bit_cast(unsigned, __builtin_convertvector(f32x2{lo, hi}, bf16x2));
}
__device__ __forceinline__ float bf_lo(unsigned u) { return __uint_as_float(u << 16); }
__device__ __forceinline__ float bf_hi(unsigned u) { return __uint_as_float(u & 0xffff0000u); }
__device__ __forceinline__ float rbf(float f) { return bf_lo(pk_bf16(f, f)); }
template <int CTRL> __device__ __forceinline__ float dpp(float x) {
  return __builtin_bit_cast(float, __builtin_amdgcn_mov_dpp(__builtin_bit_cast(int, x), CTRL, 0xf, 0xf, true));
}
constexpr int XOR1 = 0xB1, XOR2 = 0x4E, ROR4 = 0x124, ROR8 = 0x128;
__device__ __forceinline__ float xrow_max(float x) {
  auto s = __builtin_amdgcn_permlane16_swap(__float_as_uint(x), __float_as_uint(x), false, false);
  x = fmaxf(__uint_as_float(s[0]), __uint_as_float(s[1]));
  auto t = __builtin_amdgcn_permlane32_swap(__float_as_uint(x), __float_as_uint(x), false, false);
  return fmaxf(__uint_as_float(t[0]), __uint_as_float(t[1]));
}
__device__ __forceinline__ float xrow_sum(float x) {
  auto s = __builtin_amdgcn_permlane16_swap(__float_as_uint(x), __float_as_uint(x), false, false);
  x = __uint_as_float(s[0]) + __uint_as_float(s[1]);
  auto t = __builtin_amdgcn_permlane32_swap(__float_as_uint(x), __float_as_uint(x), false, false);
  return __uint_as_float(t[0]) + __uint_as_float(t[1]);
}
__device__ __forceinline__ float quad_stride_sum(float x) {
  x += dpp<ROR8>(x); x += dpp<ROR4>(x);
  return xrow_sum(x);
}
__device__ __forceinline__ float wave_sum(float x) {
  x += dpp<XOR1>(x); x += dpp<XOR2>(x);
  return quad_stride_sum(x);
}
__device__ __forceinline__ f32x4 mfma32(v4u a, v4u b, f32x4 c) {
  return __builtin_amdgcn_mfma_f32_16x16x32_bf16(__builtin_bit_cast(bf16x8, a), __builtin_bit_cast(bf16x8, b), c, 0, 0, 0);
}
__device__ __forceinline__ v2u lds_tr(LAS char* p) {
  return __builtin_bit_cast(v2u, __builtin_amdgcn_ds_read_tr16_b64_v4bf16((LAS bf16x4*)p));
}
__device__ __forceinline__ v4u cat(v2u a, v2u b) { return v4u{a[0], a[1], b[0], b[1]}; }
__device__ __forceinline__ v4u not_sent(v4u u) {
  return v4u{min(u[0], SENT - 1), min(u[1], SENT - 1), min(u[2], SENT - 1), min(u[3], SENT - 1)};
}
__device__ __forceinline__ bool ready(v4u u) { return max(max(u[0], u[1]), max(u[2], u[3])) != SENT; }
__device__ __forceinline__ v4u refill(v4u cur, __amdgpu_buffer_rsrc_t rs, int off) {
  const bool ok = ready(cur);
  const v4u nv = __builtin_amdgcn_raw_buffer_load_b128(rs, ok ? OOB : off, 0, AUX_SC1);
  return ok ? cur : nv;
}
__device__ __forceinline__ float silu(float v) { return v / (1.f + __expf(-v)); }

__device__ __forceinline__ void dma1(unsigned voff, const char* sbase, unsigned ldsaddr) {
  asm volatile("s_mov_b32 m0, %2\n\ts_nop 0\n\tglobal_load_lds_dwordx4 %0, %1 nt" :: "v"(voff), "s"(sbase), "s"(ldsaddr) : "memory");
}
__device__ __forceinline__ void dma1c(unsigned voff, const char* sbase, unsigned ldsaddr) {
  asm volatile("s_mov_b32 m0, %2\n\ts_nop 0\n\tglobal_load_lds_dwordx4 %0, %1" :: "v"(voff), "s"(sbase), "s"(ldsaddr) : "memory");
}
__device__ __forceinline__ void dma4(unsigned v0, unsigned v1, unsigned v2, unsigned v3, const char* sbase, unsigned ldsaddr) {
  asm volatile("s_mov_b32 m0, %5\n\ts_nop 0\n\t"
               "global_load_lds_dwordx4 %0, %4 nt\n\tglobal_load_lds_dwordx4 %1, %4 offset:1024 nt\n\t"
               "global_load_lds_dwordx4 %2, %4 offset:2048 nt\n\tglobal_load_lds_dwordx4 %3, %4 offset:3072 nt"
               :: "v"(v0), "v"(v1), "v"(v2), "v"(v3), "s"(sbase), "s"(ldsaddr) : "memory");
}
__device__ __forceinline__ void dma4c(unsigned v0, unsigned v1, unsigned v2, unsigned v3, const char* sbase, unsigned ldsaddr) {
  asm volatile("s_mov_b32 m0, %5\n\ts_nop 0\n\t"
               "global_load_lds_dwordx4 %0, %4\n\tglobal_load_lds_dwordx4 %1, %4 offset:1024\n\t"
               "global_load_lds_dwordx4 %2, %4 offset:2048\n\tglobal_load_lds_dwordx4 %3, %4 offset:3072"
               :: "v"(v0), "v"(v1), "v"(v2), "v"(v3), "s"(sbase), "s"(ldsaddr) : "memory");
}
template <int N> __device__ __forceinline__ void wait_vm() { asm volatile("s_waitcnt vmcnt(%0)" :: "i"(N) : "memory"); }
__device__ __forceinline__ void wait_lgkm() { asm volatile("s_waitcnt lgkmcnt(0)" ::: "memory"); }

typedef volatile LAS unsigned* ctrl_t;
template <int SLEEP = 0> __device__ __forceinline__ unsigned lds_wait_ge(ctrl_t ctrl, int word, unsigned want) {
  unsigned v;
  for (int n = 0; (v = __builtin_amdgcn_readfirstlane(ctrl[word])) < want; ++n) {
    if ((n & 255) == 255) {
      if (n > LDS_SPIN_CAP) ctrl[C_DEAD] = 1u;
      if (__builtin_amdgcn_readfirstlane(ctrl[C_DEAD])) break;
    }
    if (SLEEP) __builtin_amdgcn_s_sleep(SLEEP);
  }
  asm volatile("" ::: "memory");
  return v;
}
__device__ __forceinline__ bool glb_give_up(ctrl_t ctrl, int& n) {
  if ((++n & 63) != 0) return false;
  if (n > GLB_SPIN_CAP) ctrl[C_DEAD] = 1u;
  return __builtin_amdgcn_readfirstlane(ctrl[C_DEAD]) != 0;
}

#define STAMP(i) do {} while (0)
#define STAMP2(i) do {} while (0)
#define FLUSH() do {} while (0)

__global__ void __launch_bounds__(384) fused_decode(const char* __restrict__ x, const char* __restrict__ kc, const char* __restrict__ vc,
                                                    const char* __restrict__ wq, const char* __restrict__ wo, const char* __restrict__ w1,
                                                    const char* __restrict__ w2, char* __restrict__ out, char* ws) {
  extern __shared__ __attribute__((aligned(16))) char smem[];
  LAS char* L = (LAS char*)smem;
  ctrl_t ctrl = (ctrl_t)(L + L_CTRL);
  const int lane = threadIdx.x & 63, wave = __builtin_amdgcn_readfirstlane(threadIdx.x >> 6), bid = blockIdx.x;
  const int xg = bid & 7, tg = bid >> 3, kvh = tg >> 2, gi = 4 * xg + (tg & 3), wid = kvh * 32 + gi;
  const int bb = gi >> 4, split = gi & 15, qc = COLROT ? 4 * ((xg + kvh) & 7) + (tg & 3) : gi;
  const int g = lane >> 4, col = lane & 15, q4 = (lane >> 2) & 3, p4 = lane & 3;
  const int ffn = xg * 28 + tg;
  const bool has_ffn = tg < 28 && !NOFFN;
  const __amdgpu_buffer_rsrc_t rs = __builtin_amdgcn_make_buffer_rsrc((void*)ws, 0, WS_END, 0x00020000);

  if (wave == 0) {
    __builtin_amdgcn_s_setprio(3);
    STAMP2(0);
    const unsigned lbase = (unsigned)(uintptr_t)L;
    if (lane < 16) ctrl[lane] = 0u;
    wait_lgkm();
    if (WQFIRST) __builtin_amdgcn_s_barrier();
    int issued = 4, slot = SEQ_KV, pub = 0;
    auto throttle = [&]() {
      ++issued;
      wait_vm<WIN>();
      pub = max(pub, issued - WIN / 4);
      if (lane == 0) ctrl[C_LANDED] = pub;
    };
    auto wait_free = [&](int seq) {
      if (seq < NSLOT || NOCONS) return;
      const unsigned need = seq - NSLOT + 1;
      for (int n = 0;; ++n) {
        const v4u f = *(volatile LAS v4u*)(ctrl + C_FREED);
        if ((unsigned)__builtin_amdgcn_readfirstlane(min(min(f[0], f[1]), min(f[2], f[3]))) >= need) break;
        if (n == 0) {
          wait_vm<0>(); pub = issued; if (lane == 0) ctrl[C_LANDED] = pub;
        }
        if ((n & 255) == 255) {
          if (n > LDS_SPIN_CAP) ctrl[C_DEAD] = 1u;
          if (__builtin_amdgcn_readfirstlane(ctrl[C_DEAD])) break;
        }
        __builtin_amdgcn_s_sleep(1);
      }
      asm volatile("" ::: "memory");
    };
    const size_t kvoff = ((size_t)(bb * 8 + kvh) * SK + split * 2048) * 256;
    const char* kb = kc + kvoff;
    const char* vb = vc + kvoff;
    unsigned vk[4], vv[2];
#pragma unroll
    for (int j = 0; j < 4; ++j) vk[j] = g * 256 + ((col ^ (4 * j + g)) * 16);
#pragma unroll
    for (int j = 0; j < 2; ++j) vv[j] = g * 256 + ((col ^ ((4 * j + g) << 1)) * 16);
    for (int t = 0; t < NTILE; ++t) {
      wait_free(SEQ_KV + 2 * t);
      unsigned la = lbase + slot * SLOT;
#pragma unroll
      for (int j = 0; j < 4; ++j) {
        if (KPOL == 1 || (KPOL == 2 && bb == 0) || (KPOL == 3 && bb == 1)) dma4(vk[0], vk[1], vk[2], vk[3], kb + t * SLOT + j * 4096, la + j * 4096);
        else dma4c(vk[0], vk[1], vk[2], vk[3], kb + t * SLOT + j * 4096, la + j * 4096);
        if (!WQFIRST && t == 0 && j == 3) __builtin_amdgcn_s_barrier();
        throttle();
      }
      slot = slot + 1 == NSLOT ? 0 : slot + 1;
      wait_free(SEQ_KV + 2 * t + 1);
      la = lbase + slot * SLOT;
#pragma unroll
      for (int j = 0; j < 4; ++j) {
        if (VPOL == 1 || (VPOL == 2 && bb == 0) || (VPOL == 3 && bb == 1)) dma4(vv[0], vv[1], vv[0], vv[1], vb + t * SLOT + j * 4096, la + j * 4096);
        else dma4c(vv[0], vv[1], vv[0], vv[1], vb + t * SLOT + j * 4096, la + j * 4096);
        throttle();
      }
      slot = slot + 1 == NSLOT ? 0 : slot + 1;
    }
    STAMP(1);
#define DRAIN_KV(N) if (WIN > (N) && (N) >= TAILW) { wait_vm<(N)>(); pub = max(pub, G_KV - (N) / 4); if (lane == 0) ctrl[C_LANDED] = pub; }
    DRAIN_KV(28) DRAIN_KV(24) DRAIN_KV(20) DRAIN_KV(16) DRAIN_KV(12) DRAIN_KV(8) DRAIN_KV(4) DRAIN_KV(0)
#undef DRAIN_KV
    wait_free(SEQ_WO);
    {
      const int dq = gi >> 2;
      const char* src = wo + (size_t)(kvh * 512 + 32 * (dq >> 1) + 8 * (dq & 1)) * 1024 + (gi & 3) * 256;
      const unsigned v0 = vv[0] + g * 768, v1 = vv[1] + g * 768;
#pragma unroll
      for (int j = 0; j < 16; ++j)
        dma1((j & 1) ? v1 : v0, src + ((j & 3) * 128 + ((j >> 2) & 1) * 16 + (j >> 3) * 4) * 1024, lbase + SL_WO + j * 1024);
    }
#define DRAIN_TO(N) if ((TAILW < WIN ? TAILW : WIN) + 16 > (N)) { wait_vm<(N)>(); pub = max(pub, G_WO - (N) / 4); if (lane == 0) ctrl[C_LANDED] = pub; }
    DRAIN_TO(44) DRAIN_TO(40) DRAIN_TO(36) DRAIN_TO(32) DRAIN_TO(28) DRAIN_TO(24) DRAIN_TO(20) DRAIN_TO(16)
    DRAIN_TO(0)
#undef DRAIN_TO
    STAMP(2);
    FLUSH();
    return;
  }

  if (wave == 5) {
    const unsigned lbase = (unsigned)(uintptr_t)L;
    u64 tag;
    v4u x0, x1;
    asm volatile("global_load_dwordx2 %0, %3, %4 sc1\n\tglobal_load_dwordx4 %1, %5, %6\n\tglobal_load_dwordx4 %2, %5, %6 offset:1024"
                 : "=&v"(tag), "=&v"(x0), "=&v"(x1) : "v"(WS_EPOCH), "s"(ws), "v"(lane * 16), "s"(x) : "memory");
    {
      const unsigned vo = (lane >> 1) * 8192 + (lane & 1) * 16;
      const char* src = wq + (kvh * 512 + qc * 16) * 2;
#pragma unroll
      for (int j = 0; j < 16; ++j) dma1(NOWQ ? lane * 16 : vo, NOWQ ? wq + bid * 16384 + j * 1024 : src + (size_t)j * (32 * 8192), lbase + j * 1024);
    }
    auto load_w1 = [&]() {
      const unsigned vo = NOW1 ? lane * 16 : lane * (FF * 2);
#pragma unroll
      for (int j = 0; j < 8; ++j) {
        const char* src = NOW1 ? w1 + ffn * 8192 + j * 1024 : w1 + ffn * 16 + (size_t)j * (64 * FF * 2);
        if (W1NT) dma1(vo, src, lbase + L_W + j * 1024); else dma1c(vo, src, lbase + L_W + j * 1024);
      }
    };
    __builtin_amdgcn_s_barrier();
    asm volatile("s_waitcnt vmcnt(16)" : "+v"(x0), "+v"(x1), "+v"(tag) :: "memory");
    tag += 1;
    if (FFNPOS == 0 && has_ffn) load_w1();
    asm volatile("" ::: "memory");
#pragma unroll
    for (int b = 0; b < 2; ++b) {
      const v4u xv = b ? x1 : x0;
      float v[8];
#pragma unroll
      for (int e = 0; e < 4; ++e) { v[2 * e] = bf_lo(xv[e]); v[2 * e + 1] = bf_hi(xv[e]); }
      const float mean = wave_sum(((v[0] + v[1]) + (v[2] + v[3])) + ((v[4] + v[5]) + (v[6] + v[7]))) * (1.f / D);
      float sq = 0.f;
#pragma unroll
      for (int e = 0; e < 8; ++e) sq = fmaf(v[e] - mean, v[e] - mean, sq);
      const float var = wave_sum(sq) * (1.f / D);
      const float mb = rbf(mean), den = rbf(sqrtf(rbf(rbf(var) + 1e-5f))), rden = 1.f / den;
      v4u o;
#pragma unroll
      for (int e = 0; e < 4; ++e) {
        const unsigned dp = pk_bf16(v[2 * e] - mb, v[2 * e + 1] - mb);
        const float d0 = bf_lo(dp), d1 = bf_hi(dp), q0 = d0 * rden, q1 = d1 * rden;
        o[e] = pk_bf16(fmaf(fmaf(-q0, den, d0), rden, q0), fmaf(fmaf(-q1, den, d1), rden, q1));
      }
      *(LAS v4u*)(L + L_XN + b * 1024 + lane * 16) = o;
    }
    v4u xb[16];
    {
      LAS char* xa = L + L_XN + (((col >> 1) & 1) * 512 + 4 * g) * 2;
#pragma unroll
      for (int ks = 0; ks < 16; ++ks) xb[ks] = cat(*(LAS v2u*)(xa + ks * 64), *(LAS v2u*)(xa + ks * 64 + 32));
    }
    __builtin_amdgcn_sched_barrier(0);
    STAMP(0);
    if (FFNPOS == 0 && has_ffn) wait_vm<8>(); else wait_vm<0>();
    STAMP(1);
    {
      f32x4 qp[2] = {{0.f, 0.f, 0.f, 0.f}, {0.f, 0.f, 0.f, 0.f}};
      LAS char* wa = L + 8 * lane;
      v4u fa[16];
#pragma unroll
      for (int ks = 0; ks < 16; ++ks) fa[ks] = cat(lds_tr(wa + ks * 1024), lds_tr(wa + ks * 1024 + 512));
      __builtin_amdgcn_sched_barrier(0);
#pragma unroll
      for (int ks = 0; ks < 16; ++ks) qp[ks & 1] = mfma32(fa[ks], xb[ks], qp[ks & 1]);
      const f32x4 qa = qp[0] + qp[1];
      const int r = (col & 1) | ((col >> 1) & 2);
      const float qv = r == 0 ? qa[0] : r == 1 ? qa[1] : r == 2 ? qa[2] : qa[3];
      const u64 gran = (tag << 16) | (pk_bf16(qv, qv) & 0xffffu);
      if (col < 8) __hip_atomic_store((GAS u64*)(ws + WS_QG) + (kvh * 2 + ((col >> 1) & 1)) * 512 + qc * 16 + 4 * g + r, gran, RLX_AGENT);
    }
    if (lane < 4) ctrl[C_FREED + lane] = 1u;
    STAMP(2);
    f32x4 fo[4];
    auto ffn_slice = [&]() {
      if (FFNPOS != 0) load_w1();
      wait_vm<0>();
      f32x4 hp[2] = {{0.f, 0.f, 0.f, 0.f}, {0.f, 0.f, 0.f, 0.f}};
      LAS char* wa = L + L_W + (4 * g + q4) * 16 + 8 * (p4 & 1);
      v4u fa[16];
#pragma unroll
      for (int ks = 0; ks < 16; ++ks) fa[ks] = cat(lds_tr(wa + ks * 512), lds_tr(wa + ks * 512 + 256));
      __builtin_amdgcn_sched_barrier(0);
      wait_lgkm();
      dma4(lane * 16, (lane ^ 2) * 16, (lane ^ 4) * 16, (lane ^ 6) * 16, w2 + (size_t)ffn * 8192, lbase + L_W);
      dma4((lane ^ 8) * 16, (lane ^ 10) * 16, (lane ^ 12) * 16, (lane ^ 14) * 16, w2 + (size_t)ffn * 8192 + 4096, lbase + L_W + 4096);
#pragma unroll
      for (int ks = 0; ks < 16; ++ks) hp[ks & 1] = mfma32(fa[ks], xb[ks], hp[ks & 1]);
      const f32x4 h = hp[0] + hp[1];
      const v2u hb = {pk_bf16(silu(rbf(h[0])), silu(rbf(h[1]))), pk_bf16(silu(rbf(h[2])), silu(rbf(h[3])))};
      wait_vm<0>();
      const int row = 4 * (g & 1) + q4;
      LAS char* w2a = L + L_W + row * 1024 + 8 * (p4 & 1);
      v2u wf[32];
#pragma unroll
      for (int n = 0; n < 32; ++n) wf[n] = lds_tr(w2a + (((2 * n + (p4 >> 1)) ^ (2 * row)) * 16));
      __builtin_amdgcn_sched_barrier(0);
#pragma unroll
      for (int n = 0; n < 4; ++n) {
        const bool on = g < 2 && (col >> 2) == n, on0 = on && !(col & 1), on1 = on && (col & 1);
        const v4u bm = {on0 ? hb[0] : 0u, on0 ? hb[1] : 0u, on1 ? hb[0] : 0u, on1 ? hb[1] : 0u};
#pragma unroll
        for (int c = 0; c < 4; ++c) fo[c] = mfma32(cat(wf[8 * c + 2 * n], wf[8 * c + 2 * n + 1]), bm, n ? fo[c] : f32x4{0.f, 0.f, 0.f, 0.f});
      }
    };
    {
      const int qo = WS_QG + (kvh * 2 + bb) * 4096 + lane * 8;
      const unsigned thi = (unsigned)(tag >> 16), tlo = (unsigned)tag << 16;
      v2u a[8];
#pragma unroll
      for (int k = 0; k < 8; ++k) a[k] = v2u{~tlo, ~thi};
      for (int n = 0; !NOQ;) {
        asm volatile("" ::: "memory");
        bool all = true;
        v2u nv[8];
#pragma unroll
        for (int k = 0; k < 8; ++k) {
          const bool ok = a[k][1] == thi && ((a[k][0] ^ tlo) >> 16) == 0;
          nv[k] = __builtin_amdgcn_raw_buffer_load_b64(rs, ok ? OOB : qo + 512 * k, 0, AUX_SC1);
        }
#pragma unroll
        for (int k = 0; k < 8; ++k) {
          const bool ok = a[k][1] == thi && ((a[k][0] ^ tlo) >> 16) == 0;
          a[k] = ok ? a[k] : nv[k];
          all = all && a[k][1] == thi && ((a[k][0] ^ tlo) >> 16) == 0;
        }
        if (__all(all) || glb_give_up(ctrl, n)) break;
        if (QSLEEP) __builtin_amdgcn_s_sleep(QSLEEP);
      }
#pragma unroll
      for (int k = 0; k < 8; ++k) *(LAS unsigned short*)(L + L_Q + (64 * k + lane) * 2) = (unsigned short)a[k][0];
      wait_lgkm();
      if (lane == 0) ctrl[C_Q] = 1u;
    }
    STAMP(3);
    {
      const v4u sv = {SENT, SENT, SENT, SENT};
      if (has_ffn) {
        if (FFNPOS == 2) lds_wait_ge<8>(ctrl, C_LANDED, FFNAT);
        ffn_slice();
#pragma unroll
        for (int c = 0; c < 4; ++c)
          __builtin_amdgcn_raw_buffer_store_b128(__builtin_bit_cast(v4u, fo[c]), rs, NOPFST ? OOB : WS_PF + ffn * 4096 + c * 1024 + lane * 16, 0, AUX_SC1);
      }
      __builtin_amdgcn_raw_buffer_store_b128(sv, rs, WS_P1 + wid * 2048 + lane * 16, 0, AUX_SC1);
      __builtin_amdgcn_raw_buffer_store_b128(sv, rs, WS_P1 + wid * 2048 + 1024 + lane * 16, 0, AUX_SC1);
      __builtin_amdgcn_raw_buffer_store_b128(sv, rs, WS_P2 + wid * 1024 + lane * 16, 0, AUX_SC1);
      wait_vm<0>();
      if (lane == 0) __hip_atomic_store((GAS u64*)(ws + WS_FLAG) + bid, tag, RLX_AGENT);
    }
    STAMP(4);
    lds_wait_ge<8>(ctrl, C_LANDED, 4 * 32);
    for (int n = 0; !NOFLAG;) {
      u64 f[4];
#pragma unroll
      for (int k = 0; k < 4; ++k) f[k] = __hip_atomic_load((GAS u64*)(ws + WS_FLAG) + k * 64 + lane, RLX_AGENT);
      if (__all(((f[0] ^ tag) | (f[1] ^ tag) | (f[2] ^ tag) | (f[3] ^ tag)) == 0) || glb_give_up(ctrl, n)) break;
      __builtin_amdgcn_s_sleep(8);
    }
    asm volatile("" ::: "memory");
    if (lane == 0) ctrl[C_FLAG] = 1u;
    if (bid == 0 && lane == 0) __hip_atomic_store((GAS u64*)(ws + WS_EPOCH), tag, RLX_AGENT);
    if (bid >= 64) { FLUSH(); return; }

    const int rho = xg * 8 + tg, cb = rho >> 4, cw = (rho >> 2) & 3, gw = rho & 3;
    const int oidx = (p4 >> 1) * 512 + cb * 128 + cw * 32 + (p4 & 1) * 16 + gw * 4;
    const v2u xin = *(const GAS v2u*)(x + oidx * 2);
    f32x4 gate = {0.f, 0.f, 0.f, 0.f};
    {
      v4u fv[NFFN / 16];
#pragma unroll
      for (int k = 0; k < NFFN / 16; ++k)
        fv[k] = __builtin_amdgcn_raw_buffer_load_b128(rs, NOPF ? OOB : WS_PF + ((lane >> 2) + 16 * k) * 4096 + cb * 1024 + gw * 256 + cw * 64 + p4 * 16, 0, AUX_SC1);
#pragma unroll
      for (int k = 0; k < NFFN / 16; ++k) gate += __builtin_bit_cast(f32x4, fv[k]);
#pragma unroll
      for (int j = 0; j < 4; ++j) gate[j] = rbf(silu(rbf(quad_stride_sum(gate[j]))));
    }
    asm volatile("" : "+v"(gate));
    lds_wait_ge<8>(ctrl, C_LANDED, G_KV - 8);
    const int po = WS_P2 + (4 * (lane >> 2) + cb) * 1024 + cw * 256 + gw * 64 + p4 * 16;
    v4u sv[4];
#pragma unroll
    for (int k = 0; k < 4; ++k) sv[k] = v4u{SENT, SENT, SENT, SENT};
    for (int n = 0;;) {
      asm volatile("" ::: "memory");
#pragma unroll
      for (int k = 0; k < 4; ++k) sv[k] = refill(sv[k], rs, po + k * (64 * 1024));
      if (__all(ready(sv[0]) && ready(sv[1]) && ready(sv[2]) && ready(sv[3])) || glb_give_up(ctrl, n)) break;
      if (PSLEEP) __builtin_amdgcn_s_sleep(PSLEEP);
    }
    STAMP(5);
    const f32x4 ss = (__builtin_bit_cast(f32x4, sv[0]) + __builtin_bit_cast(f32x4, sv[1])) + (__builtin_bit_cast(f32x4, sv[2]) + __builtin_bit_cast(f32x4, sv[3]));
    float y[4];
#pragma unroll
    for (int j = 0; j < 4; ++j)
      y[j] = ((j & 1) ? bf_hi(xin[j >> 1]) : bf_lo(xin[j >> 1])) + rbf(rbf(quad_stride_sum(ss[j])) * gate[j]);
    v2u yo = {pk_bf16(y[0], y[1]), pk_bf16(y[2], y[3])};
    if (ctrl[C_DEAD]) yo = v2u{0x7fc07fc0u, 0x7fc07fc0u};
    if (lane < 4) *(GAS v2u*)(out + oidx * 2) = yo;
    STAMP2(6);
    FLUSH();
    return;
  }

  const int c = wave - 1;
  __builtin_amdgcn_s_barrier();
  asm volatile("" ::: "memory");
  lds_wait_ge<2>(ctrl, C_Q, 1u);
  const int blk = col >> 2;
  v4u qf[4][4];
#pragma unroll
  for (int k = 0; k < 4; ++k) {
    const v4u qv = *(LAS v4u*)(L + L_Q + ((col & 3) * 128 + 32 * k + 8 * g) * 2);
#pragma unroll
    for (int a = 0; a < 4; ++a) qf[a][k] = blk == a ? qv : v4u{0u, 0u, 0u, 0u};
  }
  unsigned ka[4], va[2];
#pragma unroll
  for (int k = 0; k < 4; ++k) ka[k] = col * 256 + (((4 * k + g) ^ col) * 16);
#pragma unroll
  for (int n = 0; n < 2; ++n)
    va[n] = (4 * g + q4) * 256 + (((4 * c + 2 * n + (p4 >> 1)) ^ ((4 * (g & 1) + q4) << 1)) * 16) + 8 * (p4 & 1);

  constexpr float SCALE = 0.08838834764831845f, LOG2E = 1.4426950408889634f;
  float ms = -3.0e38f, lsum = 0.f;
  f32x4 acc0 = {0.f, 0.f, 0.f, 0.f}, acc1 = {0.f, 0.f, 0.f, 0.f};
  int slot = SEQ_KV;
  unsigned seen = 0;
  auto need = [&](unsigned want) {
    if (seen < want) seen = lds_wait_ge(ctrl, C_LANDED, want);
    asm volatile("" ::: "memory");
  };
  if (NOCONS) lds_wait_ge<32>(ctrl, C_LANDED, G_KV - 8);
  for (int t = NOCONS ? NTILE - 1 : 0; t < NTILE; ++t) {
    const int seq = SEQ_KV + 2 * t;
    LAS char* sb = L + slot * SLOT;
    slot = slot + 1 == NSLOT ? 0 : slot + 1;
    need(4 * seq + 4);
    v4u kf[4][4];
#pragma unroll
    for (int k = 0; k < 4; ++k)
#pragma unroll
      for (int a = 0; a < 4; ++a) kf[a][k] = *(LAS v4u*)(sb + ka[k] + a * 4096);
    __builtin_amdgcn_sched_barrier(0);
    f32x4 s0 = {0.f, 0.f, 0.f, 0.f}, s1 = {0.f, 0.f, 0.f, 0.f};
#pragma unroll
    for (int k = 0; k < 4; ++k) {
      s0 = mfma32(kf[0][k], qf[0][k], s0); s1 = mfma32(kf[1][k], qf[1][k], s1);
      s0 = mfma32(kf[2][k], qf[2][k], s0); s1 = mfma32(kf[3][k], qf[3][k], s1);
    }
    __builtin_amdgcn_sched_barrier(0);
    wait_lgkm();
    if (lane == 0) ctrl[C_FREED + c] = seq + 1;
    s0 += s1;
    float s[4];
#pragma unroll
    for (int j = 0; j < 2; ++j) {
      const unsigned a = pk_bf16(s0[2 * j], s0[2 * j + 1]), b = pk_bf16(bf_lo(a) * SCALE, bf_hi(a) * SCALE);
      s[2 * j] = bf_lo(b); s[2 * j + 1] = bf_hi(b);
    }
    float mx = fmaxf(fmaxf(s[0], s[1]), fmaxf(s[2], s[3]));
    mx = fmaxf(mx, dpp<ROR8>(mx)); mx = fmaxf(mx, dpp<ROR4>(mx)); mx = xrow_max(mx);
    const float mn = fmaxf(ms, mx * LOG2E), alpha = __builtin_amdgcn_exp2f(ms - mn);
    ms = mn;
#pragma unroll
    for (int j = 0; j < 4; ++j) s[j] = __builtin_amdgcn_exp2f(fmaf(s[j], LOG2E, -mn));
    lsum = fmaf(lsum, alpha, (s[0] + s[1]) + (s[2] + s[3]));
    const unsigned p0 = pk_bf16(s[0], s[1]), p1 = pk_bf16(s[2], s[3]);
    v4u pA = {blk == 0 ? p0 : 0u, blk == 0 ? p1 : 0u, blk == 1 ? p0 : 0u, blk == 1 ? p1 : 0u};
    v4u pB = {blk == 2 ? p0 : 0u, blk == 2 ? p1 : 0u, blk == 3 ? p0 : 0u, blk == 3 ? p1 : 0u};
    acc0 *= alpha; acc1 *= alpha;
    asm volatile("" : "+v"(pA), "+v"(pB), "+v"(acc0), "+v"(acc1), "+v"(lsum));
    sb = L + slot * SLOT;
    slot = slot + 1 == NSLOT ? 0 : slot + 1;
    need(4 * seq + 6);
    acc0 = mfma32(cat(lds_tr(sb + va[0]), lds_tr(sb + va[0] + 4096)), pA, acc0);
    acc1 = mfma32(cat(lds_tr(sb + va[1]), lds_tr(sb + va[1] + 4096)), pA, acc1);
    need(4 * seq + 8);
    v4u v0 = cat(lds_tr(sb + va[0] + 8192), lds_tr(sb + va[0] + 12288)), v1 = cat(lds_tr(sb + va[1] + 8192), lds_tr(sb + va[1] + 12288));
    wait_lgkm();
    if (lane == 0) ctrl[C_FREED + c] = seq + 2;
    asm volatile("" : "+v"(v0), "+v"(v1) :: "memory");
    acc0 = mfma32(v0, pB, acc0);
    acc1 = mfma32(v1, pB, acc1);
  }
  STAMP(0);

  lds_wait_ge(ctrl, C_FLAG, 1u);
  {
    const float lt = quad_stride_sum(lsum), inv = 1.f / lt;
    const unsigned lam = __float_as_uint(ms + __builtin_amdgcn_logf(lt));
    v4u pk;
#pragma unroll
    for (int j = 0; j < 4; ++j) {
      float a0 = acc0[j], a1 = acc1[j];
      a0 += dpp<ROR8>(a0); a0 += dpp<ROR4>(a0);
      a1 += dpp<ROR8>(a1); a1 += dpp<ROR4>(a1);
      const unsigned u = __float_as_uint(((col & 4) ? a1 : a0) * inv);
      pk[j] = ((u + 0x80u) & 0xffffff00u) | ((lam >> (8 * j)) & 0xffu);
    }
    if (col < 8) __builtin_amdgcn_raw_buffer_store_b128(not_sent(pk), rs, WS_P1 + wid * 2048 + c * 512 + g * 128 + col * 16, 0, AUX_SC1);
  }
  STAMP(1);
  {
    const int rec = WS_P1 + (kvh * 32 + (c >> 1) * 16 + (lane >> 3)) * 2048 + (gi >> 2) * 256 + (c & 1) * 128 + (lane & 7) * 16;
    v4u a0 = {SENT, SENT, SENT, SENT}, a1 = a0, wa[4];
    a0 = refill(a0, rs, rec); a1 = refill(a1, rs, rec + 8 * 2048);
    __builtin_amdgcn_sched_barrier(0);
    need(G_WO);
    LAS char* wp = L + SL_WO;
#pragma unroll
    for (int n = 0; n < 4; ++n) wa[n] = cat(lds_tr(wp + va[n & 1] + (n >> 1) * 8192), lds_tr(wp + va[n & 1] + (n >> 1) * 8192 + 4096));
    __builtin_amdgcn_sched_barrier(0);
    STAMP(6);
    for (int n = 0; !__all(ready(a0) && ready(a1)) && !glb_give_up(ctrl, n);) {
      if (PSLEEP) __builtin_amdgcn_s_sleep(PSLEEP);
      asm volatile("" ::: "memory");
      a0 = refill(a0, rs, rec); a1 = refill(a1, rs, rec + 8 * 2048);
    }
    STAMP(2);
    const float lA = __uint_as_float((a0[0] & 0xffu) | ((a0[1] & 0xffu) << 8) | ((a0[2] & 0xffu) << 16) | (a0[3] << 24));
    const float lB = __uint_as_float((a1[0] & 0xffu) | ((a1[1] & 0xffu) << 8) | ((a1[2] & 0xffu) << 16) | (a1[3] << 24));
    float M = fmaxf(lA, lB);
    M = xrow_max(fmaxf(M, dpp<ROR8>(M)));
    const float wA = __builtin_amdgcn_exp2f(lA - M), wB = __builtin_amdgcn_exp2f(lB - M);
    float r[5];
    r[4] = wA + wB;
#pragma unroll
    for (int j = 0; j < 4; ++j) r[j] = fmaf(wA, __uint_as_float(a0[j] & 0xffffff00u), wB * __uint_as_float(a1[j] & 0xffffff00u));
#pragma unroll
    for (int j = 0; j < 5; ++j) r[j] = xrow_sum(r[j] + dpp<ROR8>(r[j]));
    const float inv = 1.f / r[4];
    if (lane < 8) *(LAS v2u*)(L + L_O + c * 64 + lane * 8) = v2u{pk_bf16(r[0] * inv, r[1] * inv), pk_bf16(r[2] * inv, r[3] * inv)};
    wait_lgkm();
    if (lane == 0) __hip_atomic_fetch_add((LAS unsigned*)(ctrl + C_O), 1u, __ATOMIC_RELAXED, __HIP_MEMORY_SCOPE_WORKGROUP);
    lds_wait_ge(ctrl, C_O, 4u);
    LAS char* op = L + L_O + ((col >> 1) & 1) * 128 + g * 8;
    const v4u o0 = cat(*(LAS v2u*)op, *(LAS v2u*)(op + 32)), o1 = cat(*(LAS v2u*)(op + 64), *(LAS v2u*)(op + 96));
    const v4u z = {0u, 0u, 0u, 0u};
    f32x4 sp = {0.f, 0.f, 0.f, 0.f};
    sp = mfma32(wa[0], (col & 1) ? z : o0, sp);
    sp = mfma32(wa[1], (col & 1) ? o0 : z, sp);
    sp = mfma32(wa[2], (col & 1) ? z : o1, sp);
    sp = mfma32(wa[3], (col & 1) ? o1 : z, sp);
    if (col < 4) __builtin_amdgcn_raw_buffer_store_b128(not_sent(__builtin_bit_cast(v4u, sp)), rs, WS_P2 + wid * 1024 + c * 256 + g * 64 + col * 16, 0, AUX_SC1);
  }
  STAMP(3);
  FLUSH();
}

extern "C" void kernel_launch(void* const* d_in, const int* in_sizes, int n_in, void* d_out, int out_size, void* d_ws, size_t ws_size,
                              hipStream_t stream) {
  (void)hipFuncSetAttribute((const void*)fused_decode, hipFuncAttributeMaxDynamicSharedMemorySize, L_BYTES);
  hipLaunchKernelGGL(fused_decode, dim3(NWG), dim3(384), L_BYTES, stream, (const char*)d_in[0], (const char*)d_in[1], (const char*)d_in[2],
                     (const char*)d_in[3], (const char*)d_in[4], (const char*)d_in[6], (const char*)d_in[7], (char*)d_out, (char*)d_ws);
}
```

```cpp
#include <hip/hip_runtime.h>
#include <hip/hip_bf16.h>
#include <stdint.h>
#include <stdio.h>

#define WIN 20
#define DBG 0
#define TAILW 16
#define FFNPOS 1
#define W1NT 1
#define NOW1 0
#define WQFIRST 0
#define FFNAT 40
#define COLROT 0
#define NOCONS 0
#define NOFFN 0
#define NOQ 0
#define NOFLAG 0
#define NOPF 0
#define NOPFST 0
#define NOWQ 0
#define KPOL 1
#define VPOL 1
#define QSLEEP 4
#define PSLEEP 1

#define GAS __attribute__((address_space(1)))
#define LAS __attribute__((address_space(3)))
#define RLX_AGENT __ATOMIC_RELAXED, __HIP_MEMORY_SCOPE_AGENT
typedef unsigned long long u64;
typedef unsigned v4u __attribute__((ext_vector_type(4)));
typedef unsigned v2u __attribute__((ext_vector_type(2)));
typedef float f32x4 __attribute__((ext_vector_type(4)));
typedef float f32x2 __attribute__((ext_vector_type(2)));
typedef __bf16 bf16x8 __attribute__((ext_vector_type(8)));
typedef __bf16 bf16x4 __attribute__((ext_vector_type(4)));
typedef __bf16 bf16x2 __attribute__((ext_vector_type(2)));

constexpr int D = 512, SK = 32768, FF = 1792, NWG = 256, NFFN = FF / 8, NTILE = 32, NSLOT = 9, SLOT = 16384;
constexpr int SEQ_KV = 1, SEQ_WO = SEQ_KV + 2 * NTILE, SL_WO = (SEQ_WO % NSLOT) * SLOT;
constexpr int G_KV = 4 * SEQ_WO, G_WO = G_KV + 4;
constexpr unsigned SENT = 0xFFFFFFFFu;
constexpr int AUX_SC1 = 16, OOB = 0x7ffffff0;

constexpr int WS_EPOCH = 0, WS_FLAG = 4096, WS_QG = 8192, WS_P1 = 131072, WS_P2 = WS_P1 + NWG * 2048,
              WS_PF = WS_P2 + NWG * 1024, WS_END = WS_PF + NWG * 4096, WS_DBG = 131072 + 3 * 256 * 4096;

constexpr int L_CTRL = NSLOT * SLOT, L_XN = L_CTRL + 256, L_Q = L_XN + 2048, L_O = L_Q + 1024, L_W = L_O + 256, L_TR = L_W + 8192, L_BYTES = L_TR + (DBG == 1 ? 256 : 0);
constexpr int C_LANDED = 0, C_DEAD = 1, C_Q = 2, C_FLAG = 3, C_FREED = 4, C_O = 8;
constexpr int LDS_SPIN_CAP = 1 << 22, GLB_SPIN_CAP = 1 << 18;

__device__ __forceinline__ unsigned pk_bf16(float lo, float hi) {
  return __builtin_bit_cast(unsigned, __builtin_convertvector(f32x2{lo, hi}, bf16x2));
}
__device__ __forceinline__ float bf_lo(unsigned u) { return __uint_as_float(u << 16); }
__device__ __forceinline__ float bf_hi(unsigned u) { return __uint_as_float(u & 0xffff0000u); }
__device__ __forceinline__ float rbf(float f) { return bf_lo(pk_bf16(f, f)); }
template <int CTRL> __device__ __forceinline__ float dpp(float x) {
  return __builtin_bit_cast(float, __builtin_amdgcn_mov_dpp(__builtin_bit_cast(int, x), CTRL, 0xf, 0xf, true));
}
constexpr int XOR1 = 0xB1, XOR2 = 0x4E, ROR4 = 0x124, ROR8 = 0x128;
__device__ __forceinline__ float xrow_max(float x) {
  auto s = __builtin_amdgcn_permlane16_swap(__float_as_uint(x), __float_as_uint(x), false, false);
  x = fmaxf(__uint_as_float(s[0]), __uint_as_float(s[1]));
  auto t = __builtin_amdgcn_permlane32_swap(__float_as_uint(x), __float_as_uint(x), false, false);
  return fmaxf(__uint_as_float(t[0]), __uint_as_float(t[1]));
}
__device__ __forceinline__ float xrow_sum(float x) {
  auto s = __builtin_amdgcn_permlane16_swap(__float_as_uint(x), __float_as_uint(x), false, false);
  x = __uint_as_float(s[0]) + __uint_as_float(s[1]);
  auto t = __builtin_amdgcn_permlane32_swap(__float_as_uint(x), __float_as_uint(x), false, false);
  return __uint_as_float(t[0]) + __uint_as_float(t[1]);
}
__device__ __forceinline__ float quad_stride_sum(float x) {
  x += dpp<ROR8>(x); x += dpp<ROR4>(x);
  return xrow_sum(x);
}
__device__ __forceinline__ float wave_sum(float x) {
  x += dpp<XOR1>(x); x += dpp<XOR2>(x);
  return quad_stride_sum(x);
}
__device__ __forceinline__ f32x4 mfma32(v4u a, v4u b, f32x4 c) {
  return __builtin_amdgcn_mfma_f32_16x16x32_bf16(__builtin_bit_cast(bf16x8, a), __builtin_bit_cast(bf16x8, b), c, 0, 0, 0);
}
__device__ __forceinline__ v2u lds_tr(LAS char* p) {
  return __builtin_bit_cast(v2u, __builtin_amdgcn_ds_read_tr16_b64_v4bf16((LAS bf16x4*)p));
}
__device__ __forceinline__ v4u cat(v2u a, v2u b) { return v4u{a[0], a[1], b[0], b[1]}; }
__device__ __forceinline__ v4u not_sent(v4u u) {
  return v4u{min(u[0], SENT - 1), min(u[1], SENT - 1), min(u[2], SENT - 1), min(u[3], SENT - 1)};
}
__device__ __forceinline__ bool ready(v4u u) { return max(max(u[0], u[1]), max(u[2], u[3])) != SENT; }
__device__ __forceinline__ v4u refill(v4u cur, __amdgpu_buffer_rsrc_t rs, int off) {
  const bool ok = ready(cur);
  const v4u nv = __builtin_amdgcn_raw_buffer_load_b128(rs, ok ? OOB : off, 0, AUX_SC1);
  return ok ? cur : nv;
}
__device__ __forceinline__ float silu(float v) { return v / (1.f + __expf(-v)); }

__device__ __forceinline__ void dma1(unsigned voff, const char* sbase, unsigned ldsaddr) {
  asm volatile("s_mov_b32 m0, %2\n\ts_nop 0\n\tglobal_load_lds_dwordx4 %0, %1 nt" :: "v"(voff), "s"(sbase), "s"(ldsaddr) : "memory");
}
__device__ __forceinline__ void dma1c(unsigned voff, const char* sbase, unsigned ldsaddr) {
  asm volatile("s_mov_b32 m0, %2\n\ts_nop 0\n\tglobal_load_lds_dwordx4 %0, %1" :: "v"(voff), "s"(sbase), "s"(ldsaddr) : "memory");
}
__device__ __forceinline__ void dma4(unsigned v0, unsigned v1, unsigned v2, unsigned v3, const char* sbase, unsigned ldsaddr) {
  asm volatile("s_mov_b32 m0, %5\n\ts_nop 0\n\t"
               "global_load_lds_dwordx4 %0, %4 nt\n\tglobal_load_lds_dwordx4 %1, %4 offset:1024 nt\n\t"
               "global_load_lds_dwordx4 %2, %4 offset:2048 nt\n\tglobal_load_lds_dwordx4 %3, %4 offset:3072 nt"
               :: "v"(v0), "v"(v1), "v"(v2), "v"(v3), "s"(sbase), "s"(ldsaddr) : "memory");
}
__device__ __forceinline__ void dma4c(unsigned v0, unsigned v1, unsigned v2, unsigned v3, const char* sbase, unsigned ldsaddr) {
  asm volatile("s_mov_b32 m0, %5\n\ts_nop 0\n\t"
               "global_load_lds_dwordx4 %0, %4\n\tglobal_load_lds_dwordx4 %1, %4 offset:1024\n\t"
               "global_load_lds_dwordx4 %2, %4 offset:2048\n\tglobal_load_lds_dwordx4 %3, %4 offset:3072"
               :: "v"(v0), "v"(v1), "v"(v2), "v"(v3), "s"(sbase), "s"(ldsaddr) : "memory");
}
template <int N> __device__ __forceinline__ void wait_vm() { asm volatile("s_waitcnt vmcnt(%0)" :: "i"(N) : "memory"); }
__device__ __forceinline__ void wait_lgkm() { asm volatile("s_waitcnt lgkmcnt(0)" ::: "memory"); }

typedef volatile LAS unsigned* ctrl_t;
template <int SLEEP = 0> __device__ __forceinline__ unsigned lds_wait_ge(ctrl_t ctrl, int word, unsigned want) {
  unsigned v;
  for (int n = 0; (v = __builtin_amdgcn_readfirstlane(ctrl[word])) < want; ++n) {
    if ((n & 255) == 255) {
      if (n > LDS_SPIN_CAP) ctrl[C_DEAD] = 1u;
      if (__builtin_amdgcn_readfirstlane(ctrl[C_DEAD])) break;
    }
    if (SLEEP) __builtin_amdgcn_s_sleep(SLEEP);
  }
  asm volatile("" ::: "memory");
  return v;
}
__device__ __forceinline__ bool glb_give_up(ctrl_t ctrl, int& n) {
  if ((++n & 63) != 0) return false;
  if (n > GLB_SPIN_CAP) ctrl[C_DEAD] = 1u;
  return __builtin_amdgcn_readfirstlane(ctrl[C_DEAD]) != 0;
}

#define STAMP(i) do {} while (0)
#define STAMP2(i) do {} while (0)
#define FLUSH() do {} while (0)

__global__ void __launch_bounds__(384) fused_decode(const char* __restrict__ x, const char* __restrict__ kc, const char* __restrict__ vc,
                                                    const char* __restrict__ wq, const char* __restrict__ wo, const char* __restrict__ w1,
                                                    const char* __restrict__ w2, char* __restrict__ out, char* ws) {
  extern __shared__ __attribute__((aligned(16))) char smem[];
  LAS char* L = (LAS char*)smem;
  ctrl_t ctrl = (ctrl_t)(L + L_CTRL);
  const int lane = threadIdx.x & 63, wave = __builtin_amdgcn_readfirstlane(threadIdx.x >> 6), bid = blockIdx.x;
  const int xg = bid & 7, tg = bid >> 3, kvh = tg >> 2, gi = 4 * xg + (tg & 3), wid = kvh * 32 + gi;
  const int bb = gi >> 4, split = gi & 15, qc = COLROT ? 4 * ((xg + kvh) & 7) + (tg & 3) : gi;
  const int g = lane >> 4, col = lane & 15, q4 = (lane >> 2) & 3, p4 = lane & 3;
  const int ffn = xg * 28 + tg;
  const bool has_ffn = tg < 28 && !NOFFN;
  const __amdgpu_buffer_rsrc_t rs = __builtin_amdgcn_make_buffer_rsrc((void*)ws, 0, WS_END, 0x00020000);

  if (wave == 0) {
    __builtin_amdgcn_s_setprio(3);
    STAMP2(0);
    const unsigned lbase = (unsigned)(uintptr_t)L;
    if (lane < 16) ctrl[lane] = 0u;
    wait_lgkm();
    if (WQFIRST) __builtin_amdgcn_s_barrier();
    int issued = 4, slot = SEQ_KV, pub = 0;
    auto throttle = [&]() {
      ++issued;
      wait_vm<WIN>();
      pub = max(pub, issued - WIN / 4);
      if (lane == 0) ctrl[C_LANDED] = pub;
    };
    auto wait_free = [&](int seq) {
      if (seq < NSLOT || NOCONS) return;
      const unsigned need = seq - NSLOT + 1;
      for (int n = 0;; ++n) {
        const v4u f = *(volatile LAS v4u*)(ctrl + C_FREED);
        if ((unsigned)__builtin_amdgcn_readfirstlane(min(min(f[0], f[1]), min(f[2], f[3]))) >= need) break;
        if (n == 0) {
          wait_vm<0>(); pub = issued; if (lane == 0) ctrl[C_LANDED] = pub;
        }
        if ((n & 255) == 255) {
          if (n > LDS_SPIN_CAP) ctrl[C_DEAD] = 1u;
          if (__builtin_amdgcn_readfirstlane(ctrl[C_DEAD])) break;
        }
        __builtin_amdgcn_s_sleep(1);
      }
      asm volatile("" ::: "memory");
    };
    const size_t kvoff = ((size_t)(bb * 8 + kvh) * SK + split * 2048) * 256;
    const char* kb = kc + kvoff;
    const char* vb = vc + kvoff;
    unsigned vk[4], vv[2];
#pragma unroll
    for (int j = 0; j < 4; ++j) vk[j] = g * 256 + ((col ^ (4 * j + g)) * 16);
#pragma unroll
    for (int j = 0; j < 2; ++j) vv[j] = g * 256 + ((col ^ ((4 * j + g) << 1)) * 16);
    for (int t = 0; t < NTILE; ++t) {
      wait_free(SEQ_KV + 2 * t);
      unsigned la = lbase + slot * SLOT;
#pragma unroll
      for (int j = 0; j < 4; ++j) {
        if (KPOL == 1 || (KPOL == 2 && bb == 0) || (KPOL == 3 && bb == 1)) dma4(vk[0], vk[1], vk[2], vk[3], kb + t * SLOT + j * 4096, la + j * 4096);
        else dma4c(vk[0], vk[1], vk[2], vk[3], kb + t * SLOT + j * 4096, la + j * 4096);
        if (!WQFIRST && t == 0 && j == 3) __builtin_amdgcn_s_barrier();
        throttle();
      }
      slot = slot + 1 == NSLOT ? 0 : slot + 1;
      wait_free(SEQ_KV + 2 * t + 1);
      la = lbase + slot * SLOT;
#pragma unroll
      for (int j = 0; j < 4; ++j) {
        if (VPOL == 1 || (VPOL == 2 && bb == 0) || (VPOL == 3 && bb == 1)) dma4(vv[0], vv[1], vv[0], vv[1], vb + t * SLOT + j * 4096, la + j * 4096);
        else dma4c(vv[0], vv[1], vv[0], vv[1], vb + t * SLOT + j * 4096, la + j * 4096);
        throttle();
      }
      slot = slot + 1 == NSLOT ? 0 : slot + 1;
    }
    STAMP(1);
#define DRAIN_KV(N) if (WIN > (N) && (N) >= TAILW) { wait_vm<(N)>(); pub = max(pub, G_KV - (N) / 4); if (lane == 0) ctrl[C_LANDED] = pub; }
    DRAIN_KV(28) DRAIN_KV(24) DRAIN_KV(20) DRAIN_KV(16) DRAIN_KV(12) DRAIN_KV(8) DRAIN_KV(4) DRAIN_KV(0)
#undef DRAIN_KV
    wait_free(SEQ_WO);
    {
      const int dq = gi >> 2;
      const char* src = wo + (size_t)(kvh * 512 + 32 * (dq >> 1) + 8 * (dq & 1)) * 1024 + (gi & 3) * 256;
      const unsigned v0 = vv[0] + g * 768, v1 = vv[1] + g * 768;
#pragma unroll
      for (int j = 0; j < 16; ++j)
        dma1((j & 1) ? v1 : v0, src + ((j & 3) * 128 + ((j >> 2) & 1) * 16 + (j >> 3) * 4) * 1024, lbase + SL_WO + j * 1024);
    }
#define DRAIN_TO(N) if ((TAILW < WIN ? TAILW : WIN) + 16 > (N)) { wait_vm<(N)>(); pub = max(pub, G_WO - (N) / 4); if (lane == 0) ctrl[C_LANDED] = pub; }
    DRAIN_TO(44) DRAIN_TO(40) DRAIN_TO(36) DRAIN_TO(32) DRAIN_TO(28) DRAIN_TO(24) DRAIN_TO(20) DRAIN_TO(16)
    DRAIN_TO(0)
#undef DRAIN_TO
    STAMP(2);
    FLUSH();
    return;
  }

  if (wave == 5) {
    const unsigned lbase = (unsigned)(uintptr_t)L;
    u64 tag;
    v4u x0, x1;
    asm volatile("global_load_dwordx2 %0, %3, %4 sc1\n\tglobal_load_dwordx4 %1, %5, %6\n\tglobal_load_dwordx4 %2, %5, %6 offset:1024"
                 : "=&v"(tag), "=&v"(x0), "=&v"(x1) : "v"(WS_EPOCH), "s"(ws), "v"(lane * 16), "s"(x) : "memory");
    {
      const unsigned vo = (lane >> 1) * 8192 + (lane & 1) * 16;
      const char* src = wq + (kvh * 512 + qc * 16) * 2;
#pragma unroll
      for (int j = 0; j < 16; ++j) dma1(NOWQ ? lane * 16 : vo, NOWQ ? wq + bid * 16384 + j * 1024 : src + (size_t)j * (32 * 8192), lbase + j * 1024);
    }
    auto load_w1 = [&]() {
      const unsigned vo = NOW1 ? lane * 16 : lane * (FF * 2);
#pragma unroll
      for (int j = 0; j < 8; ++j) {
        const char* src = NOW1 ? w1 + ffn * 8192 + j * 1024 : w1 + ffn * 16 + (size_t)j * (64 * FF * 2);
        if (W1NT) dma1(vo, src, lbase + L_W + j * 1024); else dma1c(vo, src, lbase + L_W + j * 1024);
      }
    };
    __builtin_amdgcn_s_barrier();
    asm volatile("s_waitcnt vmcnt(16)" : "+v"(x0), "+v"(x1), "+v"(tag) :: "memory");
    tag += 1;
    if (FFNPOS == 0 && has_ffn) load_w1();
    asm volatile("" ::: "memory");
#pragma unroll
    for (int b = 0; b < 2; ++b) {
      const v4u xv = b ? x1 : x0;
      float v[8];
#pragma unroll
      for (int e = 0; e < 4; ++e) { v[2 * e] = bf_lo(xv[e]); v[2 * e + 1] = bf_hi(xv[e]); }
      const float mean = wave_sum(((v[0] + v[1]) + (v[2] + v[3])) + ((v[4] + v[5]) + (v[6] + v[7]))) * (1.f / D);
      float sq = 0.f;
#pragma unroll
      for (int e = 0; e < 8; ++e) sq = fmaf(v[e] - mean, v[e] - mean, sq);
      const float var = wave_sum(sq) * (1.f / D);
      const float mb = rbf(mean), den = rbf(sqrtf(rbf(rbf(var) + 1e-5f))), rden = 1.f / den;
      v4u o;
#pragma unroll
      for (int e = 0; e < 4; ++e) {
        const unsigned dp = pk_bf16(v[2 * e] - mb, v[2 * e + 1] - mb);
        const float d0 = bf_lo(dp), d1 = bf_hi(dp), q0 = d0 * rden, q1 = d1 * rden;
        o[e] = pk_bf16(fmaf(fmaf(-q0, den, d0), rden, q0), fmaf(fmaf(-q1, den, d1), rden, q1));
      }
      *(LAS v4u*)(L + L_XN + b * 1024 + lane * 16) = o;
    }
    v4u xb[16];
    {
      LAS char* xa = L + L_XN + (((col >> 1) & 1) * 512 + 4 * g) * 2;
#pragma unroll
      for (int ks = 0; ks < 16; ++ks) xb[ks] = cat(*(LAS v2u*)(xa + ks * 64), *(LAS v2u*)(xa + ks * 64 + 32));
    }
    __builtin_amdgcn_sched_barrier(0);
    STAMP(0);
    if (FFNPOS == 0 && has_ffn) wait_vm<8>(); else wait_vm<0>();
    STAMP(1);
    {
      f32x4 qp[2] = {{0.f, 0.f, 0.f, 0.f}, {0.f, 0.f, 0.f, 0.f}};
      LAS char* wa = L + 8 * lane;
      v4u fa[16];
#pragma unroll
      for (int ks = 0; ks < 16; ++ks) fa[ks] = cat(lds_tr(wa + ks * 1024), lds_tr(wa + ks * 1024 + 512));
      __builtin_amdgcn_sched_barrier(0);
#pragma unroll
      for (int ks = 0; ks < 16; ++ks) qp[ks & 1] = mfma32(fa[ks], xb[ks], qp[ks & 1]);
      const f32x4 qa = qp[0] + qp[1];
      const int r = (col & 1) | ((col >> 1) & 2);
      const float qv = r == 0 ? qa[0] : r == 1 ? qa[1] : r == 2 ? qa[2] : qa[3];
      const u64 gran = (tag << 16) | (pk_bf16(qv, qv) & 0xffffu);
      if (col < 8) __hip_atomic_store((GAS u64*)(ws + WS_QG) + (kvh * 2 + ((col >> 1) & 1)) * 512 + qc * 16 + 4 * g + r, gran, RLX_AGENT);
    }
    if (lane < 4) ctrl[C_FREED + lane] = 1u;
    STAMP(2);
    f32x4 fo[4];
    auto ffn_slice = [&]() {
      if (FFNPOS != 0) load_w1();
      wait_vm<0>();
      f32x4 hp[2] = {{0.f, 0.f, 0.f, 0.f}, {0.f, 0.f, 0.f, 0.f}};
      LAS char* wa = L + L_W + (4 * g + q4) * 16 + 8 * (p4 & 1);
      v4u fa[16];
#pragma unroll
      for (int ks = 0; ks < 16; ++ks) fa[ks] = cat(lds_tr(wa + ks * 512), lds_tr(wa + ks * 512 + 256));
      __builtin_amdgcn_sched_barrier(0);
      wait_lgkm();
      dma4(lane * 16, (lane ^ 2) * 16, (lane ^ 4) * 16, (lane ^ 6) * 16, w2 + (size_t)ffn * 8192, lbase + L_W);
      dma4((lane ^ 8) * 16, (lane ^ 10) * 16, (lane ^ 12) * 16, (lane ^ 14) * 16, w2 + (size_t)ffn * 8192 + 4096, lbase + L_W + 4096);
#pragma unroll
      for (int ks = 0; ks < 16; ++ks) hp[ks & 1] = mfma32(fa[ks], xb[ks], hp[ks & 1]);
      const f32x4 h = hp[0] + hp[1];
      const v2u hb = {pk_bf16(silu(rbf(h[0])), silu(rbf(h[1]))), pk_bf16(silu(rbf(h[2])), silu(rbf(h[3])))};
      wait_vm<0>();
      const int row = 4 * (g & 1) + q4;
      LAS char* w2a = L + L_W + row * 1024 + 8 * (p4 & 1);
      v2u wf[32];
#pragma unroll
      for (int n = 0; n < 32; ++n) wf[n] = lds_tr(w2a + (((2 * n + (p4 >> 1)) ^ (2 * row)) * 16));
      __builtin_amdgcn_sched_barrier(0);
#pragma unroll
      for (int n = 0; n < 4; ++n) {
        const bool on = g < 2 && (col >> 2) == n, on0 = on && !(col & 1), on1 = on && (col & 1);
        const v4u bm = {on0 ? hb[0] : 0u, on0 ? hb[1] : 0u, on1 ? hb[0] : 0u, on1 ? hb[1] : 0u};
#pragma unroll
        for (int c = 0; c < 4; ++c) fo[c] = mfma32(cat(wf[8 * c + 2 * n], wf[8 * c + 2 * n + 1]), bm, n ? fo[c] : f32x4{0.f, 0.f, 0.f, 0.f});
      }
    };
    {
      const int qo = WS_QG + (kvh * 2 + bb) * 4096 + lane * 8;
      const unsigned thi = (unsigned)(tag >> 16), tlo = (unsigned)tag << 16;
      v2u a[8];
#pragma unroll
      for (int k = 0; k < 8; ++k) a[k] = v2u{~tlo, ~thi};
      for (int n = 0; !NOQ;) {
        asm volatile("" ::: "memory");
        bool all = true;
        v2u nv[8];
#pragma unroll
        for (int k = 0; k < 8; ++k) {
          const bool ok = a[k][1] == thi && ((a[k][0] ^ tlo) >> 16) == 0;
          nv[k] = __builtin_amdgcn_raw_buffer_load_b64(rs, ok ? OOB : qo + 512 * k, 0, AUX_SC1);
        }
#pragma unroll
        for (int k = 0; k < 8; ++k) {
          const bool ok = a[k][1] == thi && ((a[k][0] ^ tlo) >> 16) == 0;
          a[k] = ok ? a[k] : nv[k];
          all = all && a[k][1] == thi && ((a[k][0] ^ tlo) >> 16) == 0;
        }
        if (__all(all) || glb_give_up(ctrl, n)) break;
        if (QSLEEP) __builtin_amdgcn_s_sleep(QSLEEP);
      }
#pragma unroll
      for (int k = 0; k < 8; ++k) *(LAS unsigned short*)(L + L_Q + (64 * k + lane) * 2) = (unsigned short)a[k][0];
      wait_lgkm();
      if (lane == 0) ctrl[C_Q] = 1u;
    }
    STAMP(3);
    {
      const v4u sv = {SENT, SENT, SENT, SENT};
      if (has_ffn) {
        if (FFNPOS == 2) lds_wait_ge<8>(ctrl, C_LANDED, FFNAT);
        ffn_slice();
#pragma unroll
        for (int c = 0; c < 4; ++c)
          __builtin_amdgcn_raw_buffer_store_b128(__builtin_bit_cast(v4u, fo[c]), rs, NOPFST ? OOB : WS_PF + ffn * 4096 + c * 1024 + lane * 16, 0, AUX_SC1);
      }
      __builtin_amdgcn_raw_buffer_store_b128(sv, rs, WS_P1 + wid * 2048 + lane * 16, 0, AUX_SC1);
      __builtin_amdgcn_raw_buffer_store_b128(sv, rs, WS_P1 + wid * 2048 + 1024 + lane * 16, 0, AUX_SC1);
      __builtin_amdgcn_raw_buffer_store_b128(sv, rs, WS_P2 + wid * 1024 + lane * 16, 0, AUX_SC1);
      wait_vm<0>();
      if (lane == 0) __hip_atomic_store((GAS u64*)(ws + WS_FLAG) + bid, tag, RLX_AGENT);
    }
    STAMP(4);
    lds_wait_ge<8>(ctrl, C_LANDED, 4 * 32);
    for (int n = 0; !NOFLAG;) {
      u64 f[4];
#pragma unroll
      for (int k = 0; k < 4; ++k) f[k] = __hip_atomic_load((GAS u64*)(ws + WS_FLAG) + k * 64 + lane, RLX_AGENT);
      if (__all(((f[0] ^ tag) | (f[1] ^ tag) | (f[2] ^ tag) | (f[3] ^ tag)) == 0) || glb_give_up(ctrl, n)) break;
      __builtin_amdgcn_s_sleep(8);
    }
    asm volatile("" ::: "memory");
    if (lane == 0) ctrl[C_FLAG] = 1u;
    if (bid == 0 && lane == 0) __hip_atomic_store((GAS u64*)(ws + WS_EPOCH), tag, RLX_AGENT);
    if (bid >= 64) { FLUSH(); return; }

    const int rho = xg * 8 + tg, cb = rho >> 4, cw = (rho >> 2) & 3, gw = rho & 3;
    const int oidx = (p4 >> 1) * 512 + cb * 128 + cw * 32 + (p4 & 1) * 16 + gw * 4;
    const v2u xin = *(const GAS v2u*)(x + oidx * 2);
    f32x4 gate = {0.f, 0.f, 0.f, 0.f};
    {
      v4u fv[NFFN / 16];
#pragma unroll
      for (int k = 0; k < NFFN / 16; ++k)
        fv[k] = __builtin_amdgcn_raw_buffer_load_b128(rs, NOPF ? OOB : WS_PF + ((lane >> 2) + 16 * k) * 4096 + cb * 1024 + gw * 256 + cw * 64 + p4 * 16, 0, AUX_SC1);
#pragma unroll
      for (int k = 0; k < NFFN / 16; ++k) gate += __builtin_bit_cast(f32x4, fv[k]);
#pragma unroll
      for (int j = 0; j < 4; ++j) gate[j] = rbf(silu(rbf(quad_stride_sum(gate[j]))));
    }
    asm volatile("" : "+v"(gate));
    lds_wait_ge<8>(ctrl, C_LANDED, G_KV - 8);
    const int po = WS_P2 + (4 * (lane >> 2) + cb) * 1024 + cw * 256 + gw * 64 + p4 * 16;
    v4u sv[4];
#pragma unroll
    for (int k = 0; k < 4; ++k) sv[k] = v4u{SENT, SENT, SENT, SENT};
    for (int n = 0;;) {
      asm volatile("" ::: "memory");
#pragma unroll
      for (int k = 0; k < 4; ++k) sv[k] = refill(sv[k], rs, po + k * (64 * 1024));
      if (__all(ready(sv[0]) && ready(sv[1]) && ready(sv[2]) && ready(sv[3])) || glb_give_up(ctrl, n)) break;
      if (PSLEEP) __builtin_amdgcn_s_sleep(PSLEEP);
    }
    STAMP(5);
    const f32x4 ss = (__builtin_bit_cast(f32x4, sv[0]) + __builtin_bit_cast(f32x4, sv[1])) + (__builtin_bit_cast(f32x4, sv[2]) + __builtin_bit_cast(f32x4, sv[3]));
    float y[4];
#pragma unroll
    for (int j = 0; j < 4; ++j)
      y[j] = ((j & 1) ? bf_hi(xin[j >> 1]) : bf_lo(xin[j >> 1])) + rbf(rbf(quad_stride_sum(ss[j])) * gate[j]);
    v2u yo = {pk_bf16(y[0], y[1]), pk_bf16(y[2], y[3])};
    if (ctrl[C_DEAD]) yo = v2u{0x7fc07fc0u, 0x7fc07fc0u};
    if (lane < 4) *(GAS v2u*)(out + oidx * 2) = yo;
    STAMP2(6);
    FLUSH();
    return;
  }

  const int c = wave - 1;
  __builtin_amdgcn_s_barrier();
  asm volatile("" ::: "memory");
  lds_wait_ge<2>(ctrl, C_Q, 1u);
  const int blk = col >> 2;
  v4u qf[4][4];
#pragma unroll
  for (int k = 0; k < 4; ++k) {
    const v4u qv = *(LAS v4u*)(L + L_Q + ((col & 3) * 128 + 32 * k + 8 * g) * 2);
#pragma unroll
    for (int a = 0; a < 4; ++a) qf[a][k] = blk == a ? qv : v4u{0u, 0u, 0u, 0u};
  }
  unsigned ka[4], va[2];
#pragma unroll
  for (int k = 0; k < 4; ++k) ka[k] = col * 256 + (((4 * k + g) ^ col) * 16);
#pragma unroll
  for (int n = 0; n < 2; ++n)
    va[n] = (4 * g + q4) * 256 + (((4 * c + 2 * n + (p4 >> 1)) ^ ((4 * (g & 1) + q4) << 1)) * 16) + 8 * (p4 & 1);

  constexpr float SCALE = 0.08838834764831845f, LOG2E = 1.4426950408889634f;
  float ms = -3.0e38f, lsum = 0.f;
  f32x4 acc0 = {0.f, 0.f, 0.f, 0.f}, acc1 = {0.f, 0.f, 0.f, 0.f};
  int slot = SEQ_KV;
  unsigned seen = 0;
  auto need = [&](unsigned want) {
    if (seen < want) seen = lds_wait_ge(ctrl, C_LANDED, want);
    asm volatile("" ::: "memory");
  };
  if (NOCONS) lds_wait_ge<32>(ctrl, C_LANDED, G_KV - 8);
  for (int t = NOCONS ? NTILE - 1 : 0; t < NTILE; ++t) {
    const int seq = SEQ_KV + 2 * t;
    LAS char* sb = L + slot * SLOT;
    slot = slot + 1 == NSLOT ? 0 : slot + 1;
    need(4 * seq + 4);
    v4u kf[4][4];
#pragma unroll
    for (int k = 0; k < 4; ++k)
#pragma unroll
      for (int a = 0; a < 4; ++a) kf[a][k] = *(LAS v4u*)(sb + ka[k] + a * 4096);
    __builtin_amdgcn_sched_barrier(0);
    f32x4 s0 = {0.f, 0.f, 0.f, 0.f}, s1 = {0.f, 0.f, 0.f, 0.f};
#pragma unroll
    for (int k = 0; k < 4; ++k) {
      s0 = mfma32(kf[0][k], qf[0][k], s0); s1 = mfma32(kf[1][k], qf[1][k], s1);
      s0 = mfma32(kf[2][k], qf[2][k], s0); s1 = mfma32(kf[3][k], qf[3][k], s1);
    }
    __builtin_amdgcn_sched_barrier(0);
    wait_lgkm();
    if (lane == 0) ctrl[C_FREED + c] = seq + 1;
    s0 += s1;
    float s[4];
#pragma unroll
    for (int j = 0; j < 2; ++j) {
      const unsigned a = pk_bf16(s0[2 * j], s0[2 * j + 1]), b = pk_bf16(bf_lo(a) * SCALE, bf_hi(a) * SCALE);
      s[2 * j] = bf_lo(b); s[2 * j + 1] = bf_hi(b);
    }
    float mx = fmaxf(fmaxf(s[0], s[1]), fmaxf(s[2], s[3]));
    mx = fmaxf(mx, dpp<ROR8>(mx)); mx = fmaxf(mx, dpp<ROR4>(mx)); mx = xrow_max(mx);
    const float mn = fmaxf(ms, mx * LOG2E), alpha = __builtin_amdgcn_exp2f(ms - mn);
    ms = mn;
#pragma unroll
    for (int j = 0; j < 4; ++j) s[j] = __builtin_amdgcn_exp2f(fmaf(s[j], LOG2E, -mn));
    lsum = fmaf(lsum, alpha, (s[0] + s[1]) + (s[2] + s[3]));
    const unsigned p0 = pk_bf16(s[0], s[1]), p1 = pk_bf16(s[2], s[3]);
    v4u pA = {blk == 0 ? p0 : 0u, blk == 0 ? p1 : 0u, blk == 1 ? p0 : 0u, blk == 1 ? p1 : 0u};
    v4u pB = {blk == 2 ? p0 : 0u, blk == 2 ? p1 : 0u, blk == 3 ? p0 : 0u, blk == 3 ? p1 : 0u};
    acc0 *= alpha; acc1 *= alpha;
    asm volatile("" : "+v"(pA), "+v"(pB), "+v"(acc0), "+v"(acc1), "+v"(lsum));
    sb = L + slot * SLOT;
    slot = slot + 1 == NSLOT ? 0 : slot + 1;
    need(4 * seq + 6);
    acc0 = mfma32(cat(lds_tr(sb + va[0]), lds_tr(sb + va[0] + 4096)), pA, acc0);
    acc1 = mfma32(cat(lds_tr(sb + va[1]), lds_tr(sb + va[1] + 4096)), pA, acc1);
    need(4 * seq + 8);
    v4u v0 = cat(lds_tr(sb + va[0] + 8192), lds_tr(sb + va[0] + 12288)), v1 = cat(lds_tr(sb + va[1] + 8192), lds_tr(sb + va[1] + 12288));
    wait_lgkm();
    if (lane == 0) ctrl[C_FREED + c] = seq + 2;
    asm volatile("" : "+v"(v0), "+v"(v1) :: "memory");
    acc0 = mfma32(v0, pB, acc0);
    acc1 = mfma32(v1, pB, acc1);
  }
  STAMP(0);

  lds_wait_ge(ctrl, C_FLAG, 1u);
  {
    const float lt = quad_stride_sum(lsum), inv = 1.f / lt;
    const unsigned lam = __float_as_uint(ms + __builtin_amdgcn_logf(lt));
    v4u pk;
#pragma unroll
    for (int j = 0; j < 4; ++j) {
      float a0 = acc0[j], a1 = acc1[j];
      a0 += dpp<ROR8>(a0); a0 += dpp<ROR4>(a0);
      a1 += dpp<ROR8>(a1); a1 += dpp<ROR4>(a1);
      const unsigned u = __float_as_uint(((col & 4) ? a1 : a0) * inv);
      pk[j] = ((u + 0x80u) & 0xffffff00u) | ((lam >> (8 * j)) & 0xffu);
    }
    if (col < 8) __builtin_amdgcn_raw_buffer_store_b128(not_sent(pk), rs, WS_P1 + wid * 2048 + c * 512 + g * 128 + col * 16, 0, AUX_SC1);
  }
  STAMP(1);
  {
    const int rec = WS_P1 + (kvh * 32 + (c >> 1) * 16 + (lane >> 3)) * 2048 + (gi >> 2) * 256 + (c & 1) * 128 + (lane & 7) * 16;
    v4u a0 = {SENT, SENT, SENT, SENT}, a1 = a0, wa[4];
    a0 = refill(a0, rs, rec); a1 = refill(a1, rs, rec + 8 * 2048);
    __builtin_amdgcn_sched_barrier(0);
    need(G_WO);
    LAS char* wp = L + SL_WO;
#pragma unroll
    for (int n = 0; n < 4; ++n) wa[n] = cat(lds_tr(wp + va[n & 1] + (n >> 1) * 8192), lds_tr(wp + va[n & 1] + (n >> 1) * 8192 + 4096));
    __builtin_amdgcn_sched_barrier(0);
    STAMP(6);
    for (int n = 0; !__all(ready(a0) && ready(a1)) && !glb_give_up(ctrl, n);) {
      if (PSLEEP) __builtin_amdgcn_s_sleep(PSLEEP);
      asm volatile("" ::: "memory");
      a0 = refill(a0, rs, rec); a1 = refill(a1, rs, rec + 8 * 2048);
    }
    STAMP(2);
    const float lA = __uint_as_float((a0[0] & 0xffu) | ((a0[1] & 0xffu) << 8) | ((a0[2] & 0xffu) << 16) | (a0[3] << 24));
    const float lB = __uint_as_float((a1[0] & 0xffu) | ((a1[1] & 0xffu) << 8) | ((a1[2] & 0xffu) << 16) | (a1[3] << 24));
    float M = fmaxf(lA, lB);
    M = xrow_max(fmaxf(M, dpp<ROR8>(M)));
    const float wA = __builtin_amdgcn_exp2f(lA - M), wB = __builtin_amdgcn_exp2f(lB - M);
    float r[5];
    r[4] = wA + wB;
#pragma unroll
    for (int j = 0; j < 4; ++j) r[j] = fmaf(wA, __uint_as_float(a0[j] & 0xffffff00u), wB * __uint_as_float(a1[j] & 0xffffff00u));
#pragma unroll
    for (int j = 0; j < 5; ++j) r[j] = xrow_sum(r[j] + dpp<ROR8>(r[j]));
    const float inv = 1.f / r[4];
    if (lane < 8) *(LAS v2u*)(L + L_O + c * 64 + lane * 8) = v2u{pk_bf16(r[0] * inv, r[1] * inv), pk_bf16(r[2] * inv, r[3] * inv)};
    wait_lgkm();
    if (lane == 0) __hip_atomic_fetch_add((LAS unsigned*)(ctrl + C_O), 1u, __ATOMIC_RELAXED, __HIP_MEMORY_SCOPE_WORKGROUP);
    lds_wait_ge(ctrl, C_O, 4u);
    LAS char* op = L + L_O + ((col >> 1) & 1) * 128 + g * 8;
    const v4u o0 = cat(*(LAS v2u*)op, *(LAS v2u*)(op + 32)), o1 = cat(*(LAS v2u*)(op + 64), *(LAS v2u*)(op + 96));
    const v4u z = {0u, 0u, 0u, 0u};
    f32x4 sp = {0.f, 0.f, 0.f, 0.f};
    sp = mfma32(wa[0], (col & 1) ? z : o0, sp);
    sp = mfma32(wa[1], (col & 1) ? o0 : z, sp);
    sp = mfma32(wa[2], (col & 1) ? z : o1, sp);
    sp = mfma32(wa[3], (col & 1) ? o1 : z, sp);
    if (col < 4) __builtin_amdgcn_raw_buffer_store_b128(not_sent(__builtin_bit_cast(v4u, sp)), rs, WS_P2 + wid * 1024 + c * 256 + g * 64 + col * 16, 0, AUX_SC1);
  }
  STAMP(3);
  FLUSH();
}

extern "C" void kernel_launch(void* const* d_in, const int* in_sizes, int n_in, void* d_out, int out_size, void* d_ws, size_t ws_size,
                              hipStream_t stream) {
  for (int i = 0; i < n_in; ++i) {
    hipDeviceptr_t base = 0; size_t sz = 0; (void)hipMemGetAddressRange(&base, &sz, d_in[i]);
    fprintf(stderr, "PTR in%d %p n=%d base %p size %zu\n", i, d_in[i], in_sizes[i], base, sz);
  }
  fprintf(stderr, "PTR out %p ws %p\n", d_out, d_ws);
  (void)hipFuncSetAttribute((const void*)fused_decode, hipFuncAttributeMaxDynamicSharedMemorySize, L_BYTES);
  hipLaunchKernelGGL(fused_decode, dim3(NWG), dim3(384), L_BYTES, stream, (const char*)d_in[0], (const char*)d_in[1], (const char*)d_in[2],
                     (const char*)d_in[3], (const char*)d_in[4], (const char*)d_in[6], (const char*)d_in[7], (char*)d_out, (char*)d_ws);
}
```

```cpp
#include <hip/hip_runtime.h>
#include <hip/hip_bf16.h>
#include <stdint.h>

#define WIN 20
#define DBG 0
#define TAILW 16
#define FFNPOS 1
#define W1NT 1
#define NOW1 0
#define WQFIRST 0
#define FFNAT 40
#define COLROT 0
#define NOCONS 0
#define NOFFN 0
#define NOQ 0
#define NOFLAG 0
#define NOPF 0
#define NOPFST 0
#define NOWQ 0
#define KPOL 1
#define VPOL 1
#define PROBE 1
#define QSLEEP 4
#define PSLEEP 1

#define GAS __attribute__((address_space(1)))
#define LAS __attribute__((address_space(3)))
#define RLX_AGENT __ATOMIC_RELAXED, __HIP_MEMORY_SCOPE_AGENT
typedef unsigned long long u64;
typedef unsigned v4u __attribute__((ext_vector_type(4)));
typedef unsigned v2u __attribute__((ext_vector_type(2)));
typedef float f32x4 __attribute__((ext_vector_type(4)));
typedef float f32x2 __attribute__((ext_vector_type(2)));
typedef __bf16 bf16x8 __attribute__((ext_vector_type(8)));
typedef __bf16 bf16x4 __attribute__((ext_vector_type(4)));
typedef __bf16 bf16x2 __attribute__((ext_vector_type(2)));

constexpr int D = 512, SK = 32768, FF = 1792, NWG = 256, NFFN = FF / 8, NTILE = 32, NSLOT = 9, SLOT = 16384;
constexpr int SEQ_KV = 1, SEQ_WO = SEQ_KV + 2 * NTILE, SL_WO = (SEQ_WO % NSLOT) * SLOT;
constexpr int G_KV = 4 * SEQ_WO, G_WO = G_KV + 4;
constexpr unsigned SENT = 0xFFFFFFFFu;
constexpr int AUX_SC1 = 16, OOB = 0x7ffffff0;

constexpr int WS_EPOCH = 0, WS_FLAG = 4096, WS_QG = 8192, WS_P1 = 131072, WS_P2 = WS_P1 + NWG * 2048,
              WS_PF = WS_P2 + NWG * 1024, WS_END = WS_PF + NWG * 4096, WS_DBG = 131072 + 3 * 256 * 4096;

constexpr int L_CTRL = NSLOT * SLOT, L_XN = L_CTRL + 256, L_Q = L_XN + 2048, L_O = L_Q + 1024, L_W = L_O + 256, L_TR = L_W + 8192, L_BYTES = L_TR + (DBG == 1 ? 256 : 0);
constexpr int C_LANDED = 0, C_DEAD = 1, C_Q = 2, C_FLAG = 3, C_FREED = 4, C_O = 8;
constexpr int LDS_SPIN_CAP = 1 << 22, GLB_SPIN_CAP = 1 << 18;

__device__ __forceinline__ unsigned pk_bf16(float lo, float hi) {
  return __builtin_bit_cast(unsigned, __builtin_convertvector(f32x2{lo, hi}, bf16x2));
}
__device__ __forceinline__ float bf_lo(unsigned u) { return __uint_as_float(u << 16); }
__device__ __forceinline__ float bf_hi(unsigned u) { return __uint_as_float(u & 0xffff0000u); }
__device__ __forceinline__ float rbf(float f) { return bf_lo(pk_bf16(f, f)); }
template <int CTRL> __device__ __forceinline__ float dpp(float x) {
  return __builtin_bit_cast(float, __builtin_amdgcn_mov_dpp(__builtin_bit_cast(int, x), CTRL, 0xf, 0xf, true));
}
constexpr int XOR1 = 0xB1, XOR2 = 0x4E, ROR4 = 0x124, ROR8 = 0x128;
__device__ __forceinline__ float xrow_max(float x) {
  auto s = __builtin_amdgcn_permlane16_swap(__float_as_uint(x), __float_as_uint(x), false, false);
  x = fmaxf(__uint_as_float(s[0]), __uint_as_float(s[1]));
  auto t = __builtin_amdgcn_permlane32_swap(__float_as_uint(x), __float_as_uint(x), false, false);
  return fmaxf(__uint_as_float(t[0]), __uint_as_float(t[1]));
}
__device__ __forceinline__ float xrow_sum(float x) {
  auto s = __builtin_amdgcn_permlane16_swap(__float_as_uint(x), __float_as_uint(x), false, false);
  x = __uint_as_float(s[0]) + __uint_as_float(s[1]);
  auto t = __builtin_amdgcn_permlane32_swap(__float_as_uint(x), __float_as_uint(x), false, false);
  return __uint_as_float(t[0]) + __uint_as_float(t[1]);
}
__device__ __forceinline__ float quad_stride_sum(float x) {
  x += dpp<ROR8>(x); x += dpp<ROR4>(x);
  return xrow_sum(x);
}
__device__ __forceinline__ float wave_sum(float x) {
  x += dpp<XOR1>(x); x += dpp<XOR2>(x);
  return quad_stride_sum(x);
}
__device__ __forceinline__ f32x4 mfma32(v4u a, v4u b, f32x4 c) {
  return __builtin_amdgcn_mfma_f32_16x16x32_bf16(__builtin_bit_cast(bf16x8, a), __builtin_bit_cast(bf16x8, b), c, 0, 0, 0);
}
__device__ __forceinline__ v2u lds_tr(LAS char* p) {
  return __builtin_bit_cast(v2u, __builtin_amdgcn_ds_read_tr16_b64_v4bf16((LAS bf16x4*)p));
}
__device__ __forceinline__ v4u cat(v2u a, v2u b) { return v4u{a[0], a[1], b[0], b[1]}; }
__device__ __forceinline__ v4u not_sent(v4u u) {
  return v4u{min(u[0], SENT - 1), min(u[1], SENT - 1), min(u[2], SENT - 1), min(u[3], SENT - 1)};
}
__device__ __forceinline__ bool ready(v4u u) { return max(max(u[0], u[1]), max(u[2], u[3])) != SENT; }
__device__ __forceinline__ v4u refill(v4u cur, __amdgpu_buffer_rsrc_t rs, int off) {
  const bool ok = ready(cur);
  const v4u nv = __builtin_amdgcn_raw_buffer_load_b128(rs, ok ? OOB : off, 0, AUX_SC1);
  return ok ? cur : nv;
}
__device__ __forceinline__ float silu(float v) { return v / (1.f + __expf(-v)); }

__device__ __forceinline__ void dma1(unsigned voff, const char* sbase, unsigned ldsaddr) {
  asm volatile("s_mov_b32 m0, %2\n\ts_nop 0\n\tglobal_load_lds_dwordx4 %0, %1 nt" :: "v"(voff), "s"(sbase), "s"(ldsaddr) : "memory");
}
__device__ __forceinline__ void dma1c(unsigned voff, const char* sbase, unsigned ldsaddr) {
  asm volatile("s_mov_b32 m0, %2\n\ts_nop 0\n\tglobal_load_lds_dwordx4 %0, %1" :: "v"(voff), "s"(sbase), "s"(ldsaddr) : "memory");
}
__device__ __forceinline__ void dma4(unsigned v0, unsigned v1, unsigned v2, unsigned v3, const char* sbase, unsigned ldsaddr) {
  asm volatile("s_mov_b32 m0, %5\n\ts_nop 0\n\t"
               "global_load_lds_dwordx4 %0, %4 nt\n\tglobal_load_lds_dwordx4 %1, %4 offset:1024 nt\n\t"
               "global_load_lds_dwordx4 %2, %4 offset:2048 nt\n\tglobal_load_lds_dwordx4 %3, %4 offset:3072 nt"
               :: "v"(v0), "v"(v1), "v"(v2), "v"(v3), "s"(sbase), "s"(ldsaddr) : "memory");
}
__device__ __forceinline__ void dma4c(unsigned v0, unsigned v1, unsigned v2, unsigned v3, const char* sbase, unsigned ldsaddr) {
  asm volatile("s_mov_b32 m0, %5\n\ts_nop 0\n\t"
               "global_load_lds_dwordx4 %0, %4\n\tglobal_load_lds_dwordx4 %1, %4 offset:1024\n\t"
               "global_load_lds_dwordx4 %2, %4 offset:2048\n\tglobal_load_lds_dwordx4 %3, %4 offset:3072"
               :: "v"(v0), "v"(v1), "v"(v2), "v"(v3), "s"(sbase), "s"(ldsaddr) : "memory");
}
template <int N> __device__ __forceinline__ void wait_vm() { asm volatile("s_waitcnt vmcnt(%0)" :: "i"(N) : "memory"); }
__device__ __forceinline__ void wait_lgkm() { asm volatile("s_waitcnt lgkmcnt(0)" ::: "memory"); }

typedef volatile LAS unsigned* ctrl_t;
template <int SLEEP = 0> __device__ __forceinline__ unsigned lds_wait_ge(ctrl_t ctrl, int word, unsigned want) {
  unsigned v;
  for (int n = 0; (v = __builtin_amdgcn_readfirstlane(ctrl[word])) < want; ++n) {
    if ((n & 255) == 255) {
      if (n > LDS_SPIN_CAP) ctrl[C_DEAD] = 1u;
      if (__builtin_amdgcn_readfirstlane(ctrl[C_DEAD])) break;
    }
    if (SLEEP) __builtin_amdgcn_s_sleep(SLEEP);
  }
  asm volatile("" ::: "memory");
  return v;
}
__device__ __forceinline__ bool glb_give_up(ctrl_t ctrl, int& n) {
  if ((++n & 63) != 0) return false;
  if (n > GLB_SPIN_CAP) ctrl[C_DEAD] = 1u;
  return __builtin_amdgcn_readfirstlane(ctrl[C_DEAD]) != 0;
}

#define STAMP(i) do {} while (0)
#define STAMP2(i) do {} while (0)
#define FLUSH() do {} while (0)

__global__ void __launch_bounds__(384) fused_decode(const char* __restrict__ x, const char* __restrict__ kc, const char* __restrict__ vc,
                                                    const char* __restrict__ wq, const char* __restrict__ wo, const char* __restrict__ w1,
                                                    const char* __restrict__ w2, char* __restrict__ out, char* ws) {
  extern __shared__ __attribute__((aligned(16))) char smem[];
  LAS char* L = (LAS char*)smem;
  ctrl_t ctrl = (ctrl_t)(L + L_CTRL);
  const int lane = threadIdx.x & 63, wave = __builtin_amdgcn_readfirstlane(threadIdx.x >> 6), bid = blockIdx.x;
  const int xg = bid & 7, tg = bid >> 3, kvh = tg >> 2, gi = 4 * xg + (tg & 3), wid = kvh * 32 + gi;
  const int bb = gi >> 4, split = gi & 15, qc = COLROT ? 4 * ((xg + kvh) & 7) + (tg & 3) : gi;
  const int g = lane >> 4, col = lane & 15, q4 = (lane >> 2) & 3, p4 = lane & 3;
  const int ffn = xg * 28 + tg;
  const bool has_ffn = tg < 28 && !NOFFN;
  const __amdgpu_buffer_rsrc_t rs = __builtin_amdgcn_make_buffer_rsrc((void*)ws, 0, WS_END, 0x00020000);

  if (wave == 0) {
    __builtin_amdgcn_s_setprio(3);
    STAMP2(0);
    const unsigned lbase = (unsigned)(uintptr_t)L;
    if (lane < 16) ctrl[lane] = 0u;
    wait_lgkm();
    if (WQFIRST) __builtin_amdgcn_s_barrier();
    int issued = 4, slot = SEQ_KV, pub = 0;
    auto throttle = [&]() {
      ++issued;
      wait_vm<WIN>();
      pub = max(pub, issued - WIN / 4);
      if (lane == 0) ctrl[C_LANDED] = pub;
    };
    auto wait_free = [&](int seq) {
      if (seq < NSLOT || NOCONS) return;
      const unsigned need = seq - NSLOT + 1;
      for (int n = 0;; ++n) {
        const v4u f = *(volatile LAS v4u*)(ctrl + C_FREED);
        if ((unsigned)__builtin_amdgcn_readfirstlane(min(min(f[0], f[1]), min(f[2], f[3]))) >= need) break;
        if (n == 0) {
          wait_vm<0>(); pub = issued; if (lane == 0) ctrl[C_LANDED] = pub;
        }
        if ((n & 255) == 255) {
          if (n > LDS_SPIN_CAP) ctrl[C_DEAD] = 1u;
          if (__builtin_amdgcn_readfirstlane(ctrl[C_DEAD])) break;
        }
        __builtin_amdgcn_s_sleep(1);
      }
      asm volatile("" ::: "memory");
    };
    const size_t kvoff = ((size_t)(bb * 8 + kvh) * SK + split * 2048) * 256;
    const char* kb = kc + kvoff;
    const char* vb = vc + kvoff;
    unsigned vk[4], vv[2];
#pragma unroll
    for (int j = 0; j < 4; ++j) vk[j] = g * 256 + ((col ^ (4 * j + g)) * 16);
#pragma unroll
    for (int j = 0; j < 2; ++j) vv[j] = g * 256 + ((col ^ ((4 * j + g) << 1)) * 16);
    for (int t = 0; t < NTILE; ++t) {
      wait_free(SEQ_KV + 2 * t);
      unsigned la = lbase + slot * SLOT;
#pragma unroll
      for (int j = 0; j < 4; ++j) {
        if (KPOL == 1 || (KPOL == 2 && bb == 0) || (KPOL == 3 && bb == 1)) dma4(vk[0], vk[1], vk[2], vk[3], kb + t * SLOT + j * 4096, la + j * 4096);
        else dma4c(vk[0], vk[1], vk[2], vk[3], kb + t * SLOT + j * 4096, la + j * 4096);
        if (!WQFIRST && t == 0 && j == 3) __builtin_amdgcn_s_barrier();
        throttle();
      }
      slot = slot + 1 == NSLOT ? 0 : slot + 1;
      wait_free(SEQ_KV + 2 * t + 1);
      la = lbase + slot * SLOT;
#pragma unroll
      for (int j = 0; j < 4; ++j) {
        if (VPOL == 1 || (VPOL == 2 && bb == 0) || (VPOL == 3 && bb == 1)) dma4(vv[0], vv[1], vv[0], vv[1], vb + t * SLOT + j * 4096, la + j * 4096);
        else dma4c(vv[0], vv[1], vv[0], vv[1], vb + t * SLOT + j * 4096, la + j * 4096);
        throttle();
      }
      slot = slot + 1 == NSLOT ? 0 : slot + 1;
    }
    STAMP(1);
#define DRAIN_KV(N) if (WIN > (N) && (N) >= TAILW) { wait_vm<(N)>(); pub = max(pub, G_KV - (N) / 4); if (lane == 0) ctrl[C_LANDED] = pub; }
    DRAIN_KV(28) DRAIN_KV(24) DRAIN_KV(20) DRAIN_KV(16) DRAIN_KV(12) DRAIN_KV(8) DRAIN_KV(4) DRAIN_KV(0)
#undef DRAIN_KV
    wait_free(SEQ_WO);
    {
      const int dq = gi >> 2;
      const char* src = wo + (size_t)(kvh * 512 + 32 * (dq >> 1) + 8 * (dq & 1)) * 1024 + (gi & 3) * 256;
      const unsigned v0 = vv[0] + g * 768, v1 = vv[1] + g * 768;
#pragma unroll
      for (int j = 0; j < 16; ++j)
        dma1((j & 1) ? v1 : v0, src + ((j & 3) * 128 + ((j >> 2) & 1) * 16 + (j >> 3) * 4) * 1024, lbase + SL_WO + j * 1024);
    }
#define DRAIN_TO(N) if ((TAILW < WIN ? TAILW : WIN) + 16 > (N)) { wait_vm<(N)>(); pub = max(pub, G_WO - (N) / 4); if (lane == 0) ctrl[C_LANDED] = pub; }
    DRAIN_TO(44) DRAIN_TO(40) DRAIN_TO(36) DRAIN_TO(32) DRAIN_TO(28) DRAIN_TO(24) DRAIN_TO(20) DRAIN_TO(16)
    DRAIN_TO(0)
#undef DRAIN_TO
    STAMP(2);
    FLUSH();
    return;
  }

  if (wave == 5) {
    const u64 pt0 = __hip_atomic_load((GAS u64*)(ws + 1024), RLX_AGENT);
    u64 ptx = pt0;
    const unsigned lbase = (unsigned)(uintptr_t)L;
    u64 tag;
    v4u x0, x1;
    asm volatile("global_load_dwordx2 %0, %3, %4 sc1\n\tglobal_load_dwordx4 %1, %5, %6\n\tglobal_load_dwordx4 %2, %5, %6 offset:1024"
                 : "=&v"(tag), "=&v"(x0), "=&v"(x1) : "v"(WS_EPOCH), "s"(ws), "v"(lane * 16), "s"(x) : "memory");
    {
      const unsigned vo = (lane >> 1) * 8192 + (lane & 1) * 16;
      const char* src = wq + (kvh * 512 + qc * 16) * 2;
#pragma unroll
      for (int j = 0; j < 16; ++j) dma1(NOWQ ? lane * 16 : vo, NOWQ ? wq + bid * 16384 + j * 1024 : src + (size_t)j * (32 * 8192), lbase + j * 1024);
    }
    auto load_w1 = [&]() {
      const unsigned vo = NOW1 ? lane * 16 : lane * (FF * 2);
#pragma unroll
      for (int j = 0; j < 8; ++j) {
        const char* src = NOW1 ? w1 + ffn * 8192 + j * 1024 : w1 + ffn * 16 + (size_t)j * (64 * FF * 2);
        if (W1NT) dma1(vo, src, lbase + L_W + j * 1024); else dma1c(vo, src, lbase + L_W + j * 1024);
      }
    };
    __builtin_amdgcn_s_barrier();
    asm volatile("s_waitcnt vmcnt(16)" : "+v"(x0), "+v"(x1), "+v"(tag) :: "memory");
    tag += 1;
    if (FFNPOS == 0 && has_ffn) load_w1();
    asm volatile("" ::: "memory");
#pragma unroll
    for (int b = 0; b < 2; ++b) {
      const v4u xv = b ? x1 : x0;
      float v[8];
#pragma unroll
      for (int e = 0; e < 4; ++e) { v[2 * e] = bf_lo(xv[e]); v[2 * e + 1] = bf_hi(xv[e]); }
      const float mean = wave_sum(((v[0] + v[1]) + (v[2] + v[3])) + ((v[4] + v[5]) + (v[6] + v[7]))) * (1.f / D);
      float sq = 0.f;
#pragma unroll
      for (int e = 0; e < 8; ++e) sq = fmaf(v[e] - mean, v[e] - mean, sq);
      const float var = wave_sum(sq) * (1.f / D);
      const float mb = rbf(mean), den = rbf(sqrtf(rbf(rbf(var) + 1e-5f))), rden = 1.f / den;
      v4u o;
#pragma unroll
      for (int e = 0; e < 4; ++e) {
        const unsigned dp = pk_bf16(v[2 * e] - mb, v[2 * e + 1] - mb);
        const float d0 = bf_lo(dp), d1 = bf_hi(dp), q0 = d0 * rden, q1 = d1 * rden;
        o[e] = pk_bf16(fmaf(fmaf(-q0, den, d0), rden, q0), fmaf(fmaf(-q1, den, d1), rden, q1));
      }
      *(LAS v4u*)(L + L_XN + b * 1024 + lane * 16) = o;
    }
    v4u xb[16];
    {
      LAS char* xa = L + L_XN + (((col >> 1) & 1) * 512 + 4 * g) * 2;
#pragma unroll
      for (int ks = 0; ks < 16; ++ks) xb[ks] = cat(*(LAS v2u*)(xa + ks * 64), *(LAS v2u*)(xa + ks * 64 + 32));
    }
    __builtin_amdgcn_sched_barrier(0);
    if (PROBE == 2) ptx = __builtin_amdgcn_s_memrealtime();
    if (FFNPOS == 0 && has_ffn) wait_vm<8>(); else wait_vm<0>();
    if (PROBE == 3) ptx = __builtin_amdgcn_s_memrealtime();
    {
      f32x4 qp[2] = {{0.f, 0.f, 0.f, 0.f}, {0.f, 0.f, 0.f, 0.f}};
      LAS char* wa = L + 8 * lane;
      v4u fa[16];
#pragma unroll
      for (int ks = 0; ks < 16; ++ks) fa[ks] = cat(lds_tr(wa + ks * 1024), lds_tr(wa + ks * 1024 + 512));
      __builtin_amdgcn_sched_barrier(0);
#pragma unroll
      for (int ks = 0; ks < 16; ++ks) qp[ks & 1] = mfma32(fa[ks], xb[ks], qp[ks & 1]);
      const f32x4 qa = qp[0] + qp[1];
      const int r = (col & 1) | ((col >> 1) & 2);
      const float qv = r == 0 ? qa[0] : r == 1 ? qa[1] : r == 2 ? qa[2] : qa[3];
      const u64 gran = (tag << 16) | (pk_bf16(qv, qv) & 0xffffu);
      if (col < 8) __hip_atomic_store((GAS u64*)(ws + WS_QG) + (kvh * 2 + ((col >> 1) & 1)) * 512 + qc * 16 + 4 * g + r, gran, RLX_AGENT);
    }
    if (lane < 4) ctrl[C_FREED + lane] = 1u;
    STAMP(2);
    f32x4 fo[4];
    auto ffn_slice = [&]() {
      if (FFNPOS != 0) load_w1();
      wait_vm<0>();
      f32x4 hp[2] = {{0.f, 0.f, 0.f, 0.f}, {0.f, 0.f, 0.f, 0.f}};
      LAS char* wa = L + L_W + (4 * g + q4) * 16 + 8 * (p4 & 1);
      v4u fa[16];
#pragma unroll
      for (int ks = 0; ks < 16; ++ks) fa[ks] = cat(lds_tr(wa + ks * 512), lds_tr(wa + ks * 512 + 256));
      __builtin_amdgcn_sched_barrier(0);
      wait_lgkm();
      dma4(lane * 16, (lane ^ 2) * 16, (lane ^ 4) * 16, (lane ^ 6) * 16, w2 + (size_t)ffn * 8192, lbase + L_W);
      dma4((lane ^ 8) * 16, (lane ^ 10) * 16, (lane ^ 12) * 16, (lane ^ 14) * 16, w2 + (size_t)ffn * 8192 + 4096, lbase + L_W + 4096);
#pragma unroll
      for (int ks = 0; ks < 16; ++ks) hp[ks & 1] = mfma32(fa[ks], xb[ks], hp[ks & 1]);
      const f32x4 h = hp[0] + hp[1];
      const v2u hb = {pk_bf16(silu(rbf(h[0])), silu(rbf(h[1]))), pk_bf16(silu(rbf(h[2])), silu(rbf(h[3])))};
      wait_vm<0>();
      const int row = 4 * (g & 1) + q4;
      LAS char* w2a = L + L_W + row * 1024 + 8 * (p4 & 1);
      v2u wf[32];
#pragma unroll
      for (int n = 0; n < 32; ++n) wf[n] = lds_tr(w2a + (((2 * n + (p4 >> 1)) ^ (2 * row)) * 16));
      __builtin_amdgcn_sched_barrier(0);
#pragma unroll
      for (int n = 0; n < 4; ++n) {
        const bool on = g < 2 && (col >> 2) == n, on0 = on && !(col & 1), on1 = on && (col & 1);
        const v4u bm = {on0 ? hb[0] : 0u, on0 ? hb[1] : 0u, on1 ? hb[0] : 0u, on1 ? hb[1] : 0u};
#pragma unroll
        for (int c = 0; c < 4; ++c) fo[c] = mfma32(cat(wf[8 * c + 2 * n], wf[8 * c + 2 * n + 1]), bm, n ? fo[c] : f32x4{0.f, 0.f, 0.f, 0.f});
      }
    };
    {
      const int qo = WS_QG + (kvh * 2 + bb) * 4096 + lane * 8;
      const unsigned thi = (unsigned)(tag >> 16), tlo = (unsigned)tag << 16;
      v2u a[8];
#pragma unroll
      for (int k = 0; k < 8; ++k) a[k] = v2u{~tlo, ~thi};
      for (int n = 0; !NOQ;) {
        asm volatile("" ::: "memory");
        bool all = true;
        v2u nv[8];
#pragma unroll
        for (int k = 0; k < 8; ++k) {
          const bool ok = a[k][1] == thi && ((a[k][0] ^ tlo) >> 16) == 0;
          nv[k] = __builtin_amdgcn_raw_buffer_load_b64(rs, ok ? OOB : qo + 512 * k, 0, AUX_SC1);
        }
#pragma unroll
        for (int k = 0; k < 8; ++k) {
          const bool ok = a[k][1] == thi && ((a[k][0] ^ tlo) >> 16) == 0;
          a[k] = ok ? a[k] : nv[k];
          all = all && a[k][1] == thi && ((a[k][0] ^ tlo) >> 16) == 0;
        }
        if (__all(all) || glb_give_up(ctrl, n)) break;
        if (QSLEEP) __builtin_amdgcn_s_sleep(QSLEEP);
      }
#pragma unroll
      for (int k = 0; k < 8; ++k) *(LAS unsigned short*)(L + L_Q + (64 * k + lane) * 2) = (unsigned short)a[k][0];
      wait_lgkm();
      if (lane == 0) ctrl[C_Q] = 1u;
    }
    if (PROBE == 4) ptx = __builtin_amdgcn_s_memrealtime();
    {
      const v4u sv = {SENT, SENT, SENT, SENT};
      if (has_ffn) {
        if (FFNPOS == 2) lds_wait_ge<8>(ctrl, C_LANDED, FFNAT);
        ffn_slice();
#pragma unroll
        for (int c = 0; c < 4; ++c)
          __builtin_amdgcn_raw_buffer_store_b128(__builtin_bit_cast(v4u, fo[c]), rs, NOPFST ? OOB : WS_PF + ffn * 4096 + c * 1024 + lane * 16, 0, AUX_SC1);
      }
      __builtin_amdgcn_raw_buffer_store_b128(sv, rs, WS_P1 + wid * 2048 + lane * 16, 0, AUX_SC1);
      __builtin_amdgcn_raw_buffer_store_b128(sv, rs, WS_P1 + wid * 2048 + 1024 + lane * 16, 0, AUX_SC1);
      __builtin_amdgcn_raw_buffer_store_b128(sv, rs, WS_P2 + wid * 1024 + lane * 16, 0, AUX_SC1);
      wait_vm<0>();
      if (lane == 0) __hip_atomic_store((GAS u64*)(ws + WS_FLAG) + bid, tag, RLX_AGENT);
    }
    STAMP(4);
    lds_wait_ge<8>(ctrl, C_LANDED, 4 * 32);
    if (PROBE == 5) ptx = __builtin_amdgcn_s_memrealtime();
    for (int n = 0; !NOFLAG;) {
      u64 f[4];
#pragma unroll
      for (int k = 0; k < 4; ++k) f[k] = __hip_atomic_load((GAS u64*)(ws + WS_FLAG) + k * 64 + lane, RLX_AGENT);
      if (__all(((f[0] ^ tag) | (f[1] ^ tag) | (f[2] ^ tag) | (f[3] ^ tag)) == 0) || glb_give_up(ctrl, n)) break;
      __builtin_amdgcn_s_sleep(8);
    }
    asm volatile("" ::: "memory");
    if (lane == 0) ctrl[C_FLAG] = 1u;
    if (bid == 0 && lane == 0) __hip_atomic_store((GAS u64*)(ws + WS_EPOCH), tag, RLX_AGENT);
    if (bid >= 64) { FLUSH(); return; }

    const int rho = xg * 8 + tg, cb = rho >> 4, cw = (rho >> 2) & 3, gw = rho & 3;
    const int oidx = (p4 >> 1) * 512 + cb * 128 + cw * 32 + (p4 & 1) * 16 + gw * 4;
    const v2u xin = *(const GAS v2u*)(x + oidx * 2);
    f32x4 gate = {0.f, 0.f, 0.f, 0.f};
    {
      v4u fv[NFFN / 16];
#pragma unroll
      for (int k = 0; k < NFFN / 16; ++k)
        fv[k] = __builtin_amdgcn_raw_buffer_load_b128(rs, NOPF ? OOB : WS_PF + ((lane >> 2) + 16 * k) * 4096 + cb * 1024 + gw * 256 + cw * 64 + p4 * 16, 0, AUX_SC1);
#pragma unroll
      for (int k = 0; k < NFFN / 16; ++k) gate += __builtin_bit_cast(f32x4, fv[k]);
#pragma unroll
      for (int j = 0; j < 4; ++j) gate[j] = rbf(silu(rbf(quad_stride_sum(gate[j]))));
    }
    asm volatile("" : "+v"(gate));
    lds_wait_ge<8>(ctrl, C_LANDED, G_KV - 8);
    if (PROBE == 6) ptx = __builtin_amdgcn_s_memrealtime();
    const int po = WS_P2 + (4 * (lane >> 2) + cb) * 1024 + cw * 256 + gw * 64 + p4 * 16;
    v4u sv[4];
#pragma unroll
    for (int k = 0; k < 4; ++k) sv[k] = v4u{SENT, SENT, SENT, SENT};
    for (int n = 0;;) {
      asm volatile("" ::: "memory");
#pragma unroll
      for (int k = 0; k < 4; ++k) sv[k] = refill(sv[k], rs, po + k * (64 * 1024));
      if (__all(ready(sv[0]) && ready(sv[1]) && ready(sv[2]) && ready(sv[3])) || glb_give_up(ctrl, n)) break;
      if (PSLEEP) __builtin_amdgcn_s_sleep(PSLEEP);
    }
    STAMP(5);
    const f32x4 ss = (__builtin_bit_cast(f32x4, sv[0]) + __builtin_bit_cast(f32x4, sv[1])) + (__builtin_bit_cast(f32x4, sv[2]) + __builtin_bit_cast(f32x4, sv[3]));
    float y[4];
#pragma unroll
    for (int j = 0; j < 4; ++j)
      y[j] = ((j & 1) ? bf_hi(xin[j >> 1]) : bf_lo(xin[j >> 1])) + rbf(rbf(quad_stride_sum(ss[j])) * gate[j]);
    v2u yo = {pk_bf16(y[0], y[1]), pk_bf16(y[2], y[3])};
    if (ctrl[C_DEAD]) yo = v2u{0x7fc07fc0u, 0x7fc07fc0u};
    if (lane < 4) *(GAS v2u*)(out + oidx * 2) = yo;
    if (PROBE == 1) { while (__builtin_amdgcn_s_memrealtime() - pt0 < 8000) {} }
    if (PROBE >= 2 && bid == 0) { const u64 lim = 8000 + (PROBE >= 5 ? 2 : 10) * (ptx - pt0); while (__builtin_amdgcn_s_memrealtime() - pt0 < lim) {} }
    STAMP2(6);
    FLUSH();
    return;
  }

  const int c = wave - 1;
  __builtin_amdgcn_s_barrier();
  asm volatile("" ::: "memory");
  lds_wait_ge<2>(ctrl, C_Q, 1u);
  const int blk = col >> 2;
  v4u qf[4][4];
#pragma unroll
  for (int k = 0; k < 4; ++k) {
    const v4u qv = *(LAS v4u*)(L + L_Q + ((col & 3) * 128 + 32 * k + 8 * g) * 2);
#pragma unroll
    for (int a = 0; a < 4; ++a) qf[a][k] = blk == a ? qv : v4u{0u, 0u, 0u, 0u};
  }
  unsigned ka[4], va[2];
#pragma unroll
  for (int k = 0; k < 4; ++k) ka[k] = col * 256 + (((4 * k + g) ^ col) * 16);
#pragma unroll
  for (int n = 0; n < 2; ++n)
    va[n] = (4 * g + q4) * 256 + (((4 * c + 2 * n + (p4 >> 1)) ^ ((4 * (g & 1) + q4) << 1)) * 16) + 8 * (p4 & 1);

  constexpr float SCALE = 0.08838834764831845f, LOG2E = 1.4426950408889634f;
  float ms = -3.0e38f, lsum = 0.f;
  f32x4 acc0 = {0.f, 0.f, 0.f, 0.f}, acc1 = {0.f, 0.f, 0.f, 0.f};
  int slot = SEQ_KV;
  unsigned seen = 0;
  auto need = [&](unsigned want) {
    if (seen < want) seen = lds_wait_ge(ctrl, C_LANDED, want);
    asm volatile("" ::: "memory");
  };
  if (NOCONS) lds_wait_ge<32>(ctrl, C_LANDED, G_KV - 8);
  for (int t = NOCONS ? NTILE - 1 : 0; t < NTILE; ++t) {
    const int seq = SEQ_KV + 2 * t;
    LAS char* sb = L + slot * SLOT;
    slot = slot + 1 == NSLOT ? 0 : slot + 1;
    need(4 * seq + 4);
    v4u kf[4][4];
#pragma unroll
    for (int k = 0; k < 4; ++k)
#pragma unroll
      for (int a = 0; a < 4; ++a) kf[a][k] = *(LAS v4u*)(sb + ka[k] + a * 4096);
    __builtin_amdgcn_sched_barrier(0);
    f32x4 s0 = {0.f, 0.f, 0.f, 0.f}, s1 = {0.f, 0.f, 0.f, 0.f};
#pragma unroll
    for (int k = 0; k < 4; ++k) {
      s0 = mfma32(kf[0][k], qf[0][k], s0); s1 = mfma32(kf[1][k], qf[1][k], s1);
      s0 = mfma32(kf[2][k], qf[2][k], s0); s1 = mfma32(kf[3][k], qf[3][k], s1);
    }
    __builtin_amdgcn_sched_barrier(0);
    wait_lgkm();
    if (lane == 0) ctrl[C_FREED + c] = seq + 1;
    s0 += s1;
    float s[4];
#pragma unroll
    for (int j = 0; j < 2; ++j) {
      const unsigned a = pk_bf16(s0[2 * j], s0[2 * j + 1]), b = pk_bf16(bf_lo(a) * SCALE, bf_hi(a) * SCALE);
      s[2 * j] = bf_lo(b); s[2 * j + 1] = bf_hi(b);
    }
    float mx = fmaxf(fmaxf(s[0], s[1]), fmaxf(s[2], s[3]));
    mx = fmaxf(mx, dpp<ROR8>(mx)); mx = fmaxf(mx, dpp<ROR4>(mx)); mx = xrow_max(mx);
    const float mn = fmaxf(ms, mx * LOG2E), alpha = __builtin_amdgcn_exp2f(ms - mn);
    ms = mn;
#pragma unroll
    for (int j = 0; j < 4; ++j) s[j] = __builtin_amdgcn_exp2f(fmaf(s[j], LOG2E, -mn));
    lsum = fmaf(lsum, alpha, (s[0] + s[1]) + (s[2] + s[3]));
    const unsigned p0 = pk_bf16(s[0], s[1]), p1 = pk_bf16(s[2], s[3]);
    v4u pA = {blk == 0 ? p0 : 0u, blk == 0 ? p1 : 0u, blk == 1 ? p0 : 0u, blk == 1 ? p1 : 0u};
    v4u pB = {blk == 2 ? p0 : 0u, blk == 2 ? p1 : 0u, blk == 3 ? p0 : 0u, blk == 3 ? p1 : 0u};
    acc0 *= alpha; acc1 *= alpha;
    asm volatile("" : "+v"(pA), "+v"(pB), "+v"(acc0), "+v"(acc1), "+v"(lsum));
    sb = L + slot * SLOT;
    slot = slot + 1 == NSLOT ? 0 : slot + 1;
    need(4 * seq + 6);
    acc0 = mfma32(cat(lds_tr(sb + va[0]), lds_tr(sb + va[0] + 4096)), pA, acc0);
    acc1 = mfma32(cat(lds_tr(sb + va[1]), lds_tr(sb + va[1] + 4096)), pA, acc1);
    need(4 * seq + 8);
    v4u v0 = cat(lds_tr(sb + va[0] + 8192), lds_tr(sb + va[0] + 12288)), v1 = cat(lds_tr(sb + va[1] + 8192), lds_tr(sb + va[1] + 12288));
    wait_lgkm();
    if (lane == 0) ctrl[C_FREED + c] = seq + 2;
    asm volatile("" : "+v"(v0), "+v"(v1) :: "memory");
    acc0 = mfma32(v0, pB, acc0);
    acc1 = mfma32(v1, pB, acc1);
  }
  STAMP(0);

  lds_wait_ge(ctrl, C_FLAG, 1u);
  {
    const float lt = quad_stride_sum(lsum), inv = 1.f / lt;
    const unsigned lam = __float_as_uint(ms + __builtin_amdgcn_logf(lt));
    v4u pk;
#pragma unroll
    for (int j = 0; j < 4; ++j) {
      float a0 = acc0[j], a1 = acc1[j];
      a0 += dpp<ROR8>(a0); a0 += dpp<ROR4>(a0);
      a1 += dpp<ROR8>(a1); a1 += dpp<ROR4>(a1);
      const unsigned u = __float_as_uint(((col & 4) ? a1 : a0) * inv);
      pk[j] = ((u + 0x80u) & 0xffffff00u) | ((lam >> (8 * j)) & 0xffu);
    }
    if (col < 8) __builtin_amdgcn_raw_buffer_store_b128(not_sent(pk), rs, WS_P1 + wid * 2048 + c * 512 + g * 128 + col * 16, 0, AUX_SC1);
  }
  STAMP(1);
  {
    const int rec = WS_P1 + (kvh * 32 + (c >> 1) * 16 + (lane >> 3)) * 2048 + (gi >> 2) * 256 + (c & 1) * 128 + (lane & 7) * 16;
    v4u a0 = {SENT, SENT, SENT, SENT}, a1 = a0, wa[4];
    a0 = refill(a0, rs, rec); a1 = refill(a1, rs, rec + 8 * 2048);
    __builtin_amdgcn_sched_barrier(0);
    need(G_WO);
    LAS char* wp = L + SL_WO;
#pragma unroll
    for (int n = 0; n < 4; ++n) wa[n] = cat(lds_tr(wp + va[n & 1] + (n >> 1) * 8192), lds_tr(wp + va[n & 1] + (n >> 1) * 8192 + 4096));
    __builtin_amdgcn_sched_barrier(0);
    STAMP(6);
    for (int n = 0; !__all(ready(a0) && ready(a1)) && !glb_give_up(ctrl, n);) {
      if (PSLEEP) __builtin_amdgcn_s_sleep(PSLEEP);
      asm volatile("" ::: "memory");
      a0 = refill(a0, rs, rec); a1 = refill(a1, rs, rec + 8 * 2048);
    }
    STAMP(2);
    const float lA = __uint_as_float((a0[0] & 0xffu) | ((a0[1] & 0xffu) << 8) | ((a0[2] & 0xffu) << 16) | (a0[3] << 24));
    const float lB = __uint_as_float((a1[0] & 0xffu) | ((a1[1] & 0xffu) << 8) | ((a1[2] & 0xffu) << 16) | (a1[3] << 24));
    float M = fmaxf(lA, lB);
    M = xrow_max(fmaxf(M, dpp<ROR8>(M)));
    const float wA = __builtin_amdgcn_exp2f(lA - M), wB = __builtin_amdgcn_exp2f(lB - M);
    float r[5];
    r[4] = wA + wB;
#pragma unroll
    for (int j = 0; j < 4; ++j) r[j] = fmaf(wA, __uint_as_float(a0[j] & 0xffffff00u), wB * __uint_as_float(a1[j] & 0xffffff00u));
#pragma unroll
    for (int j = 0; j < 5; ++j) r[j] = xrow_sum(r[j] + dpp<ROR8>(r[j]));
    const float inv = 1.f / r[4];
    if (lane < 8) *(LAS v2u*)(L + L_O + c * 64 + lane * 8) = v2u{pk_bf16(r[0] * inv, r[1] * inv), pk_bf16(r[2] * inv, r[3] * inv)};
    wait_lgkm();
    if (lane == 0) __hip_atomic_fetch_add((LAS unsigned*)(ctrl + C_O), 1u, __ATOMIC_RELAXED, __HIP_MEMORY_SCOPE_WORKGROUP);
    lds_wait_ge(ctrl, C_O, 4u);
    LAS char* op = L + L_O + ((col >> 1) & 1) * 128 + g * 8;
    const v4u o0 = cat(*(LAS v2u*)op, *(LAS v2u*)(op + 32)), o1 = cat(*(LAS v2u*)(op + 64), *(LAS v2u*)(op + 96));
    const v4u z = {0u, 0u, 0u, 0u};
    f32x4 sp = {0.f, 0.f, 0.f, 0.f};
    sp = mfma32(wa[0], (col & 1) ? z : o0, sp);
    sp = mfma32(wa[1], (col & 1) ? o0 : z, sp);
    sp = mfma32(wa[2], (col & 1) ? z : o1, sp);
    sp = mfma32(wa[3], (col & 1) ? o1 : z, sp);
    if (col < 4) __builtin_amdgcn_raw_buffer_store_b128(not_sent(__builtin_bit_cast(v4u, sp)), rs, WS_P2 + wid * 1024 + c * 256 + g * 64 + col * 16, 0, AUX_SC1);
  }
  STAMP(3);
  FLUSH();
}

__global__ void k_pre(char* ws) { if (threadIdx.x == 0 && blockIdx.x == 0) *(GAS u64*)(ws + 1024) = __builtin_amdgcn_s_memrealtime(); }
extern "C" void kernel_launch(void* const* d_in, const int* in_sizes, int n_in, void* d_out, int out_size, void* d_ws, size_t ws_size,
                              hipStream_t stream) {
  hipLaunchKernelGGL(k_pre, dim3(256), dim3(64), 0, stream, (char*)d_ws);
  (void)hipFuncSetAttribute((const void*)fused_decode, hipFuncAttributeMaxDynamicSharedMemorySize, L_BYTES);
  hipLaunchKernelGGL(fused_decode, dim3(NWG), dim3(384), L_BYTES, stream, (const char*)d_in[0], (const char*)d_in[1], (const char*)d_in[2],
                     (const char*)d_in[3], (const char*)d_in[4], (const char*)d_in[6], (const char*)d_in[7], (char*)d_out, (char*)d_ws);
}
```

```cpp
#include <hip/hip_runtime.h>
#include <hip/hip_bf16.h>
#include <stdint.h>

#define DBG 0
#define W0 20
#define WMIN 12
#define WMAX 32
#define KN 1
#define KD 1
#define TG16 220
#define NROUND 1
#define MARK 64
#define MDEL 16
#define QDELAY 250
#define QSLEEP 4
#define PSLEEP 0
#define PSLEEP2 0
#define FULLLINE 0
#define OPT 266
#define PFAT 176
#define REBAL 1

#define GAS __attribute__((address_space(1)))
#define LAS __attribute__((address_space(3)))
#define RLX_AGENT __ATOMIC_RELAXED, __HIP_MEMORY_SCOPE_AGENT
typedef unsigned long long u64;
typedef unsigned v4u __attribute__((ext_vector_type(4)));
typedef unsigned v2u __attribute__((ext_vector_type(2)));
typedef float f32x4 __attribute__((ext_vector_type(4)));
typedef float f32x2 __attribute__((ext_vector_type(2)));
typedef __bf16 bf16x8 __attribute__((ext_vector_type(8)));
typedef __bf16 bf16x4 __attribute__((ext_vector_type(4)));
typedef __bf16 bf16x2 __attribute__((ext_vector_type(2)));

constexpr int D = 512, SK = 32768, FF = 1792, NWG = 256, NSLOT = 9, SLOT = 16384, SEQ_KV = 1;
constexpr unsigned SENT = 0xFFFFFFFFu;
constexpr int AUX_SC1 = 16, OOB = 0x7ffffff0;

constexpr int WS_EPOCH = 0, WS_GOK = 4096, WS_TAU = 8192, WS_QG = 32768, WS_P1 = WS_QG + 65536, WS_P2 = WS_P1 + NWG * 2048,
              WS_PF = WS_P2 + NWG * 1024, WS_END = WS_PF + 64 * 4096, WS_DBG = 131072 + 3 * 256 * 4096;

constexpr int L_CTRL = NSLOT * SLOT, L_XN = L_CTRL + 512, L_Q = L_XN + 2048, L_O = L_Q + 1024, L_W = L_O + 256, L_TR = L_W + 8192, L_BYTES = L_TR + (DBG == 1 ? 256 : 0);
constexpr int C_LANDED = 64, C_ALL = 0, C_Q = 2, C_CLR = 3, C_FREED = 4, C_PUB = 8, C_FLAG = 9, C_TAG = 10, C_SCHED = 12;
constexpr int SPIN_BUDGET = 1 << 20;

__device__ __forceinline__ unsigned pk_bf16(float lo, float hi) {
  return __builtin_bit_cast(unsigned, __builtin_convertvector(f32x2{lo, hi}, bf16x2));
}
__device__ __forceinline__ float bf_lo(unsigned u) { return __uint_as_float(u << 16); }
__device__ __forceinline__ float bf_hi(unsigned u) { return __uint_as_float(u & 0xffff0000u); }
__device__ __forceinline__ float rbf(float f) { return bf_lo(pk_bf16(f, f)); }
template <int CTRL> __device__ __forceinline__ float dpp(float x) {
  return __builtin_bit_cast(float, __builtin_amdgcn_mov_dpp(__builtin_bit_cast(int, x), CTRL, 0xf, 0xf, true));
}
constexpr int XOR1 = 0xB1, XOR2 = 0x4E, ROR4 = 0x124, ROR8 = 0x128;
__device__ __forceinline__ float xrow_sum(float x) {
  auto s = __builtin_amdgcn_permlane16_swap(__float_as_uint(x), __float_as_uint(x), false, false);
  x = __uint_as_float(s[0]) + __uint_as_float(s[1]);
  auto t = __builtin_amdgcn_permlane32_swap(__float_as_uint(x), __float_as_uint(x), false, false);
  return __uint_as_float(t[0]) + __uint_as_float(t[1]);
}
__device__ __forceinline__ float quad_stride_sum(float x) {
  x += dpp<ROR8>(x); x += dpp<ROR4>(x);
  return xrow_sum(x);
}
__device__ __forceinline__ float rows_sum4(float a, float b, float c, float d) {
  const auto s = __builtin_amdgcn_permlane32_swap(__float_as_uint(a), __float_as_uint(b), false, false);
  const auto t = __builtin_amdgcn_permlane32_swap(__float_as_uint(c), __float_as_uint(d), false, false);
  const float ab = __uint_as_float(s[0]) + __uint_as_float(s[1]), cd = __uint_as_float(t[0]) + __uint_as_float(t[1]);
  const auto u = __builtin_amdgcn_permlane16_swap(__float_as_uint(ab), __float_as_uint(cd), false, false);
  return __uint_as_float(u[0]) + __uint_as_float(u[1]);
}
__device__ __forceinline__ float max2(float a, float b) {
  float top = 3.4028235e38f;
  asm("" : "+s"(top));
  return __builtin_amdgcn_fmed3f(a, b, top);
}
__device__ __forceinline__ float xrow_max(float x) {
  const auto s = __builtin_amdgcn_permlane16_swap(__float_as_uint(x), __float_as_uint(x), false, false);
  x = max2(__uint_as_float(s[0]), __uint_as_float(s[1]));
  const auto t = __builtin_amdgcn_permlane32_swap(__float_as_uint(x), __float_as_uint(x), false, false);
  return max2(__uint_as_float(t[0]), __uint_as_float(t[1]));
}
__device__ __forceinline__ float wave_sum(float x) {
  x += dpp<XOR1>(x); x += dpp<XOR2>(x);
  return quad_stride_sum(x);
}
__device__ __forceinline__ f32x4 mfma32(v4u a, v4u b, f32x4 c) {
  return __builtin_amdgcn_mfma_f32_16x16x32_bf16(__builtin_bit_cast(bf16x8, a), __builtin_bit_cast(bf16x8, b), c, 0, 0, 0);
}
__device__ __forceinline__ v2u lds_tr(LAS char* p) {
  return __builtin_bit_cast(v2u, __builtin_amdgcn_ds_read_tr16_b64_v4bf16((LAS bf16x4*)p));
}
__device__ __forceinline__ v4u cat(v2u a, v2u b) { return v4u{a[0], a[1], b[0], b[1]}; }
__device__ __forceinline__ v4u not_sent(v4u u) {
  return v4u{min(u[0], SENT - 1), min(u[1], SENT - 1), min(u[2], SENT - 1), min(u[3], SENT - 1)};
}
__device__ __forceinline__ float silu(float v) { return v / (1.f + __expf(-v)); }

__device__ __forceinline__ void dma1(unsigned voff, const char* sbase, unsigned ldsaddr) {
  asm volatile("s_mov_b32 m0, %2\n\ts_nop 0\n\tglobal_load_lds_dwordx4 %0, %1 nt" :: "v"(voff), "s"(sbase), "s"(ldsaddr) : "memory");
}
template <int N> __device__ __forceinline__ void wait_vm() { asm volatile("s_waitcnt vmcnt(%0)" :: "i"(N) : "memory"); }
#define SLOT_GROUP(W, STEP) \
  "s_mov_b32 m0, %[lds]\n\ts_add_u32 %[lds], %[lds], 0x1000\n\t" \
  "global_load_lds_dwordx4 %[v0], vcc nt\n\tglobal_load_lds_dwordx4 %[v1], vcc offset:1024 nt\n\t" \
  "global_load_lds_dwordx4 %[v2], vcc offset:2048 nt\n\tglobal_load_lds_dwordx4 %[v3], vcc offset:3072 nt\n\t" \
  "s_add_u32 vcc_lo, vcc_lo, " STEP "\n\ts_addc_u32 vcc_hi, vcc_hi, 0\n\t" \
  "s_add_i32 %[cand], %[cand], 1\n\ts_max_i32 %[pub], %[pub], %[cand]\n\tv_mov_b32 %[tmp], %[pub]\n\t" \
  "s_waitcnt vmcnt(" #W ")\n\tds_write_b32 %[va], %[tmp]\n\t"
#define SLOT_LEVEL(W) \
  "s_mov_b64 vcc, %[src]\n\t" SLOT_GROUP(W, "0x1000") SLOT_GROUP(W, "%[mid]") SLOT_GROUP(W, "0x1000") SLOT_GROUP(W, "0") "s_branch 9f\n\t"
__device__ __forceinline__ void issue_slot(unsigned v0, unsigned v1, unsigned v2, unsigned v3, const char* src, int mid, unsigned lds,
                                           int w, int cand, int& pub, unsigned va) {
  unsigned tmp;
  int jump;
  asm volatile("s_mul_i32 %[jmp], %[lvl], 8f-7f\n\ts_getpc_b64 vcc\n6:\n\ts_add_u32 %[jmp], %[jmp], 7f-6b\n\t"
               "s_add_u32 vcc_lo, vcc_lo, %[jmp]\n\ts_addc_u32 vcc_hi, vcc_hi, 0\n\ts_setpc_b64 vcc\n7:\n\t"
               SLOT_LEVEL(12) "8:\n\t" SLOT_LEVEL(16) SLOT_LEVEL(20) SLOT_LEVEL(24) SLOT_LEVEL(28) SLOT_LEVEL(32) "9:"
               : [lds] "+&s"(lds), [cand] "+&s"(cand), [pub] "+&s"(pub), [tmp] "=&v"(tmp), [jmp] "=&s"(jump)
               : [v0] "v"(v0), [v1] "v"(v1), [v2] "v"(v2), [v3] "v"(v3), [src] "s"(src), [mid] "s"(mid), [lvl] "s"(w - 3), [va] "v"(va)
               : "vcc", "scc", "memory");
}
__device__ __forceinline__ int opaque(int v) { asm volatile("" : "+s"(v)); return v; }
__device__ __forceinline__ void wait_lgkm() { asm volatile("s_waitcnt lgkmcnt(0)" ::: "memory"); }

typedef volatile LAS unsigned* ctrl_t;
template <int SLEEP = 0> __device__ __forceinline__ unsigned lds_wait_ge(ctrl_t ctrl, int word, unsigned want, int& budget) {
  unsigned v, tmp;
  int go;
  budget = __builtin_amdgcn_readfirstlane(budget);
  want = __builtin_amdgcn_readfirstlane(want);
  asm volatile("1:\n\tds_read_b32 %[t], %[a]\n\t.if %[slp]\n\ts_sleep %[slp]\n\t.endif\n\ts_waitcnt lgkmcnt(0)\n\tv_readfirstlane_b32 %[v], %[t]\n\t"
               "s_cmp_lt_u32 %[v], %[w]\n\ts_cselect_b32 %[g], %[b], 0\n\ts_subb_u32 %[b], %[b], 0\n\ts_cmp_gt_i32 %[g], 0\n\ts_cbranch_scc1 1b"
               : [v] "=&s"(v), [t] "=&v"(tmp), [g] "=&s"(go), [b] "+&s"(budget)
               : [a] "v"((unsigned)(uintptr_t)(ctrl + word)), [w] "s"(want), [slp] "i"(SLEEP) : "scc", "memory");
  return v;
}
#define BOX_LOOP_END \
  "v_cmpx_eq_u32 vcc, -1, %[t]\n\ts_sub_i32 %[b], %[b], 1\n\ts_cmp_gt_i32 %[b], 0\n\ts_cselect_b64 vcc, exec, 0\n\t" \
  "s_cmp_lg_u64 vcc, 0\n\ts_cbranch_scc0 2f\n\t.if %[slp]\n\ts_sleep %[slp]\n\t.endif\n\ts_branch 1b\n2:\n\ts_mov_b64 exec, -1"
template <int SLEEP> __device__ __forceinline__ v4u poll_box(__amdgpu_buffer_rsrc_t rs, int off, int& budget) {
  v4u a;
  unsigned tmp;
  budget = __builtin_amdgcn_readfirstlane(budget);
  asm volatile("1:\n\tbuffer_load_dwordx4 v[100:103], %[o], %[rs], 0 offen sc1\n\ts_waitcnt vmcnt(0)\n\t"
               "v_max_u32 %[t], v100, v101\n\tv_max3_u32 %[t], %[t], v102, v103\n\t" BOX_LOOP_END
               : "=&{v[100:103]}"(a), [t] "=&v"(tmp), [b] "+&s"(budget) : [o] "v"(off), [rs] "s"(rs), [slp] "i"(SLEEP) : "vcc", "scc", "memory");
  return a;
}
template <int SLEEP> __device__ __forceinline__ void poll_box2(__amdgpu_buffer_rsrc_t rs, int off0, int off1, v4u& a, v4u& b, int& budget) {
  unsigned tmp;
  budget = __builtin_amdgcn_readfirstlane(budget);
  asm volatile("1:\n\tbuffer_load_dwordx4 v[100:103], %[o0], %[rs], 0 offen sc1\n\tbuffer_load_dwordx4 v[104:107], %[o1], %[rs], 0 offen sc1\n\t"
               "s_waitcnt vmcnt(0)\n\tv_max_u32 %[t], v100, v101\n\tv_max3_u32 %[t], %[t], v102, v103\n\t"
               "v_max3_u32 %[t], %[t], v104, v105\n\tv_max3_u32 %[t], %[t], v106, v107\n\t" BOX_LOOP_END
               : "=&{v[100:103]}"(a), "=&{v[104:107]}"(b), [t] "=&v"(tmp), [b] "+&s"(budget)
               : [o0] "v"(off0), [o1] "v"(off1), [rs] "s"(rs), [slp] "i"(SLEEP) : "vcc", "scc", "memory");
}
__device__ __forceinline__ void poll_lds_o(unsigned addr, v4u& a, v4u& b, int& budget) {
  unsigned tmp;
  budget = __builtin_amdgcn_readfirstlane(budget);
  asm volatile("1:\n\tds_read_b64 v[100:101], %[o]\n\tds_read_b64 v[102:103], %[o] offset:32\n\tds_read_b64 v[104:105], %[o] offset:64\n\t"
               "ds_read_b64 v[106:107], %[o] offset:96\n\ts_waitcnt lgkmcnt(0)\n\tv_max_u32 %[t], v100, v101\n\tv_max3_u32 %[t], %[t], v102, v103\n\t"
               "v_max3_u32 %[t], %[t], v104, v105\n\tv_max3_u32 %[t], %[t], v106, v107\n\t" BOX_LOOP_END
               : "=&{v[100:103]}"(a), "=&{v[104:107]}"(b), [t] "=&v"(tmp), [b] "+&s"(budget) : [o] "v"(addr), [slp] "i"(0) : "vcc", "scc", "memory");
}

#define STAMP(i) do {} while (0)
#define STAMP2(i) do {} while (0)
#define FLUSH() do {} while (0)

typedef unsigned v16u __attribute__((ext_vector_type(16)));
__device__ __forceinline__ char* as_ptr(unsigned lo, unsigned hi) { return (char*)(((u64)hi << 32) | lo); }

__global__ void __launch_bounds__(448) fused_decode(const char*, const char*, const char*, const char*, const char*, const char*, const char*,
                                                    char*, char*) {
  v16u ka16;
  v2u ka2;
  asm volatile("s_load_dwordx16 %0, %2, 0x0\n\ts_load_dwordx2 %1, %2, 0x40\n\ts_waitcnt lgkmcnt(0)"
               : "=&s"(ka16), "=&s"(ka2) : "s"(__builtin_amdgcn_kernarg_segment_ptr()));
  const char *x = as_ptr(ka16[0], ka16[1]), *kc = as_ptr(ka16[2], ka16[3]), *vc = as_ptr(ka16[4], ka16[5]), *wq = as_ptr(ka16[6], ka16[7]),
             *wo = as_ptr(ka16[8], ka16[9]), *w1 = as_ptr(ka16[10], ka16[11]), *w2 = as_ptr(ka16[12], ka16[13]);
  char *out = as_ptr(ka16[14], ka16[15]), *ws = as_ptr(ka2[0], ka2[1]);
  const __amdgpu_buffer_rsrc_t rs = __builtin_amdgcn_make_buffer_rsrc((void*)ws, 0, WS_END, 0x00020000);
  extern __shared__ __attribute__((aligned(16))) char smem[];
  LAS char* L = (LAS char*)smem;
  const unsigned lbase = (unsigned)(uintptr_t)L;
  ctrl_t ctrl = (ctrl_t)(L + L_CTRL);
  const int lane = threadIdx.x & 63, wave = __builtin_amdgcn_readfirstlane(threadIdx.x >> 6), bid = blockIdx.x;
  const int xg = bid & 7, tg = bid >> 3, kvh = tg >> 2, ii = tg & 3, gi = 4 * xg + ii, wid = kvh * 32 + gi;
  if (wave == 0) {
    STAMP2(7);
    asm volatile("s_mov_b32 m0, %4\n\ts_nop 0\n\tglobal_load_lds_dwordx4 %0, %1\n\tglobal_load_lds_dwordx4 %0, %1 offset:1024\n\t"
                 "s_mov_b32 m0, %5\n\ts_nop 0\n\tglobal_load_lds_dwordx4 %2, %3 sc1"
                 :: "v"(lane * 16), "s"(x), "v"(WS_EPOCH), "s"(ws), "s"(lbase + L_W), "s"(lbase + L_W + 2048) : "memory");
    const unsigned vo = (lane >> 1) * 8192 + (lane & 1) * 16;
    const char* src = wq + (kvh * 512 + gi * 16) * 2;
#pragma nounroll
    for (int j = 0; j < 16; ++j) dma1(vo, src + (size_t)j * (32 * 8192), lbase + j * 1024);
  }
  const int bb = gi >> 4, nt = REBAL ? (ii == 0 ? 33 : ii == 1 ? 31 : 32) : 32, tile0 = (xg & 3) * 128 + (REBAL && ii == 1 ? 33 : 32 * ii);
  const int seq_wo = SEQ_KV + 2 * nt, g_kv = 4 * seq_wo, g_wo = g_kv + 4;
  const int g = lane >> 4, col = lane & 15, q4 = (lane >> 2) & 3, p4 = lane & 3;
  const int ffn = xg * 8 + kvh;
  const bool has_ffn = ii == 1;
  const int role = opaque(wave);
  int budget = SPIN_BUDGET;

  if (__builtin_expect(role == 1, 1)) {
    __builtin_amdgcn_s_setprio(3);
    STAMP2(0);
    ctrl[lane] = 0u;
    ctrl[C_LANDED + lane] = 0u;
    *(LAS unsigned*)(L + L_O + lane * 4) = SENT;
    unsigned vk[4], vv[2];
#pragma unroll
    for (int j = 0; j < 4; ++j) vk[j] = g * 256 + ((col ^ (4 * j + g)) * 16);
#pragma unroll
    for (int j = 0; j < 2; ++j) vv[j] = g * 256 + ((col ^ ((4 * j + g) << 1)) * 16);
    const size_t kvoff = ((size_t)(bb * 8 + kvh) * SK + tile0 * 64) * 256;
    const char* kb = kc + kvoff;
    const char* vb = vc + kvoff;
    const char* wb = wo + (size_t)(kvh * 512 + 32 * (gi >> 3) + 8 * ((gi >> 2) & 1)) * 1024 + ii * 256;
    const unsigned va_pub = lbase + L_CTRL + (C_LANDED + lane) * 4;
    int issued = 4, slot = SEQ_KV, pub = 0, wl = W0 / 4, dith = 0;
#pragma nounroll
    for (int seq = SEQ_KV; seq <= seq_wo; ++seq) {
      if (seq != SEQ_KV) {
        const v4u sc = *(volatile LAS v4u*)(ctrl + C_SCHED);
        v4u f = *(volatile LAS v4u*)(ctrl + C_FREED);
        const int el = (int)((unsigned)__builtin_amdgcn_s_memrealtime() - __builtin_amdgcn_readfirstlane(sc[0]));
        const int rate = __builtin_amdgcn_readfirstlane(sc[2]);
        const int lag16 = rate ? ((((int)__builtin_amdgcn_readfirstlane(sc[1]) + ((el * rate) >> 8)) * nt) >> 5) - (pub - 4) * 16 : 0;
        const int wk = W0 * 16 + lag16 * KN / KD + dith;
        wl = __builtin_amdgcn_readfirstlane(min(max((wk + 32) >> 6, WMIN / 4), WMAX / 4));
        dith = min(max(wk - wl * 64, -32), 32);
        if ((int)__builtin_amdgcn_readfirstlane(min(min(f[0], f[1]), min(f[2], f[3]))) <= seq - NSLOT) {
          wait_vm<0>(); pub = issued; ctrl[C_LANDED + lane] = pub;
          do {
            __builtin_amdgcn_s_sleep(1);
            f = *(volatile LAS v4u*)(ctrl + C_FREED);
          } while ((int)__builtin_amdgcn_readfirstlane(min(min(f[0], f[1]), min(f[2], f[3]))) <= seq - NSLOT && --budget > 0);
        }
      }
      const bool is_wo = seq == seq_wo, is_k = (seq & 1) != 0 && !is_wo;
      unsigned v[4];
#pragma unroll
      for (int j = 0; j < 4; ++j) v[j] = is_k ? vk[j] : is_wo ? vv[j & 1] + g * 768 + j * (127 * 1024) : vv[j & 1];
      const char* src = is_wo ? wb : (is_k ? kb : vb) + ((seq - SEQ_KV) >> 1) * SLOT;
      issue_slot(v[0], v[1], v[2], v[3], src, is_wo ? 12288 : 4096, lbase + slot * SLOT, wl, issued - wl, pub, va_pub);
      issued += 4;
      if (seq == SEQ_KV) { wait_lgkm(); __builtin_amdgcn_s_barrier(); }
      slot = slot + 1 == NSLOT ? 0 : slot + 1;
    }
#define DRAIN_TO(N) { wait_vm<4 * (N)>(); pub = max(pub, issued - (N)); ctrl[C_LANDED + lane] = pub; }
    DRAIN_TO(7) DRAIN_TO(6) DRAIN_TO(5) DRAIN_TO(4) DRAIN_TO(0)
#undef DRAIN_TO
    STAMP(2);
    FLUSH();
    return;
  }

  if (role == 0) {
    const unsigned t_start = (unsigned)__builtin_amdgcn_s_memrealtime();
    __builtin_amdgcn_s_barrier();
    asm volatile("" ::: "memory");
    u64 tag;
    v4u xb[16];
    {
      wait_vm<16>();
      tag = *(volatile LAS u64*)(L + L_W + 2048) + 1;
#pragma unroll
      for (int b = 0; b < 2; ++b) {
        const v4u xv = *(volatile LAS v4u*)(L + L_W + b * 1024 + lane * 16);
        float v[8];
#pragma unroll
        for (int e = 0; e < 4; ++e) { v[2 * e] = bf_lo(xv[e]); v[2 * e + 1] = bf_hi(xv[e]); }
        const float mean = wave_sum(((v[0] + v[1]) + (v[2] + v[3])) + ((v[4] + v[5]) + (v[6] + v[7]))) * (1.f / D);
        float sq = 0.f;
#pragma unroll
        for (int e = 0; e < 8; ++e) sq = fmaf(v[e] - mean, v[e] - mean, sq);
        const float var = wave_sum(sq) * (1.f / D);
        const float mb = rbf(mean), den = rbf(sqrtf(rbf(rbf(var) + 1e-5f))), rden = 1.f / den;
        v4u o;
#pragma unroll
        for (int e = 0; e < 4; ++e) {
          const unsigned dp = pk_bf16(v[2 * e] - mb, v[2 * e + 1] - mb);
          const float d0 = bf_lo(dp), d1 = bf_hi(dp), q0 = d0 * rden, q1 = d1 * rden;
          o[e] = pk_bf16(fmaf(fmaf(-q0, den, d0), rden, q0), fmaf(fmaf(-q1, den, d1), rden, q1));
        }
        *(LAS v4u*)(L + L_XN + b * 1024 + lane * 16) = o;
      }
      asm volatile("" ::: "memory");
      LAS char* xa = L + L_XN + (((col >> 1) & 1) * 512 + 4 * g) * 2;
#pragma unroll
      for (int ks = 0; ks < 16; ++ks) xb[ks] = cat(*(LAS v2u*)(xa + ks * 64), *(LAS v2u*)(xa + ks * 64 + 32));
      __builtin_amdgcn_sched_barrier(0);
      STAMP(0);
      wait_vm<0>();
      STAMP(1);
      f32x4 qp[2] = {{0.f, 0.f, 0.f, 0.f}, {0.f, 0.f, 0.f, 0.f}};
      LAS char* wa = L + 8 * lane;
      v4u fa[16];
#pragma unroll
      for (int ks = 0; ks < 16; ++ks) fa[ks] = cat(lds_tr(wa + ks * 1024), lds_tr(wa + ks * 1024 + 512));
      __builtin_amdgcn_sched_barrier(0);
#pragma unroll
      for (int ks = 0; ks < 16; ++ks) qp[ks & 1] = mfma32(fa[ks], xb[ks], qp[ks & 1]);
      const f32x4 qa = qp[0] + qp[1];
      if ((col & 13) == 0) *(LAS v2u*)(L + L_Q + (col >> 1) * 32 + g * 8) = v2u{pk_bf16(qa[0], qa[1]), pk_bf16(qa[2], qa[3])};
      asm volatile("" ::: "memory");
      const unsigned thi = (unsigned)(tag >> 16), tlo = (unsigned)tag << 16;
      const unsigned qv = *(LAS unsigned short*)(L + L_Q + (lane & 31) * 2);
      __builtin_amdgcn_raw_buffer_store_b64(v2u{tlo | qv, thi}, rs, lane < 32 ? WS_QG + ((kvh * 2 + g) * 512 + gi * 16 + col) * 8 : OOB, 0, AUX_SC1);
      if (lane < 4) ctrl[C_FREED + lane] = 1u;
      if (lane == 0) *(volatile LAS u64*)(ctrl + C_TAG) = tag;
      STAMP(2);
    }
    {
      const unsigned thi = (unsigned)(tag >> 16), tlo = (unsigned)tag << 16;
      while ((int)((unsigned)__builtin_amdgcn_s_memrealtime() - t_start) < QDELAY) __builtin_amdgcn_s_sleep(2);
      const int qo = WS_QG + (kvh * 2 + bb) * 4096 + lane * 8;
      v2u a[8];
#pragma unroll
      for (int k = 0; k < 8; ++k) a[k] = v2u{~tlo, ~thi};
      for (;;) {
        asm volatile("" ::: "memory");
        bool all = true;
        v2u nv[8];
#pragma unroll
        for (int k = 0; k < 8; ++k) {
          const bool ok = a[k][1] == thi && ((a[k][0] ^ tlo) >> 16) == 0;
          nv[k] = __builtin_amdgcn_raw_buffer_load_b64(rs, ok ? OOB : qo + 512 * k, 0, AUX_SC1);
        }
#pragma unroll
        for (int k = 0; k < 8; ++k) {
          const bool ok = a[k][1] == thi && ((a[k][0] ^ tlo) >> 16) == 0;
          a[k] = ok ? a[k] : nv[k];
          all = all && a[k][1] == thi && ((a[k][0] ^ tlo) >> 16) == 0;
        }
        if (__all(all)) break;
        if (--budget <= 0) break;
        if (QSLEEP) __builtin_amdgcn_s_sleep(QSLEEP);
      }
#pragma unroll
      for (int k = 0; k < 8; ++k) *(LAS unsigned short*)(L + L_Q + (64 * k + lane) * 2) = (unsigned short)a[k][0];
      wait_lgkm();
      if (lane == 0) ctrl[C_Q] = 1u;
    }
    STAMP(3);
    auto ffn_slice = [&]() {
      const int u0 = (28 * ffn) & ~7, lo = 28 * ffn - u0;
      const unsigned vo1 = (lane >> 2) * (FF * 2) + ((p4 ^ ((lane >> 3) & 2)) * 16);
      LAS char* wa = L + L_W + (4 * g + q4) * 64 + 8 * p4;
      f32x4 h0 = {0.f, 0.f, 0.f, 0.f}, h1 = h0;
#pragma unroll
      for (int rd = 0; rd < 4; ++rd) {
#pragma unroll
        for (int j = 0; j < 8; ++j) dma1(vo1, w1 + u0 * 2 + (size_t)(rd * 128 + j * 16) * (FF * 2), lbase + L_W + j * 1024);
        wait_vm<0>();
        v4u fa[4][2];
#pragma unroll
        for (int ks = 0; ks < 4; ++ks)
#pragma unroll
          for (int nb = 0; nb < 2; ++nb) {
            LAS char* p = wa + ks * 2048 + ((nb ^ (g & 1)) * 32);
            fa[ks][nb] = cat(lds_tr(p), lds_tr(p + 1024));
          }
        __builtin_amdgcn_sched_barrier(0);
        wait_lgkm();
#pragma unroll
        for (int ks = 0; ks < 4; ++ks) { h0 = mfma32(fa[ks][0], xb[4 * rd + ks], h0); h1 = mfma32(fa[ks][1], xb[4 * rd + ks], h1); }
      }
      float hv[8];
#pragma unroll
      for (int j = 0; j < 8; ++j) {
        const int u = (j >> 2) * 16 + 4 * g + (j & 3);
        hv[j] = (u >= lo && u < lo + 28) ? silu(rbf(j < 4 ? h0[j & 3] : h1[j & 3])) : 0.f;
      }
      const v4u hb = {pk_bf16(hv[0], hv[1]), pk_bf16(hv[2], hv[3]), pk_bf16(hv[4], hv[5]), pk_bf16(hv[6], hv[7])};
      const unsigned vo2[2] = {(unsigned)(g * 1024 + ((col ^ (g << 1)) * 16)), (unsigned)(g * 1024 + ((col ^ ((4 + g) << 1)) * 16))};
      const int sw = (4 * (g & 1) + q4) << 1;
      LAS char* w2a = L + L_W + (4 * g + q4) * 256 + 8 * (p4 & 1);
      const int mine = 2 * (col >> 2) + (col & 1);
#pragma unroll
      for (int cb = 0; cb < 4; ++cb) {
#pragma unroll
        for (int j = 0; j < 8; ++j) dma1(vo2[j & 1], w2 + (size_t)(u0 + 4 * j) * 1024 + cb * 256, lbase + L_W + j * 1024);
        wait_vm<0>();
        v4u wf[8];
#pragma unroll
        for (int nb = 0; nb < 8; ++nb) {
          LAS char* p = w2a + (((2 * nb + (p4 >> 1)) ^ sw) * 16);
          wf[nb] = cat(lds_tr(p), lds_tr(p + 4096));
        }
        __builtin_amdgcn_sched_barrier(0);
        wait_lgkm();
        f32x4 fo = {0.f, 0.f, 0.f, 0.f};
#pragma unroll
        for (int nb = 0; nb < 8; ++nb) fo = mfma32(wf[nb], mine == nb ? hb : v4u{0u, 0u, 0u, 0u}, fo);
        __builtin_amdgcn_raw_buffer_store_b128(not_sent(__builtin_bit_cast(v4u, fo)), rs, WS_PF + ffn * 4096 + cb * 1024 + lane * 16, 0, AUX_SC1);
      }
    };
    if (has_ffn) { lds_wait_ge<4>(ctrl, C_CLR, 4u, budget); ffn_slice(); }
    STAMP(4);

    lds_wait_ge<8>(ctrl, C_ALL, 1u, budget);
    const int cb = xg >> 1, pc = (xg & 1) * 32 + tg, cw = pc >> 4, gw = (pc >> 2) & 3;
    const int jj = (g >> 1) | ((g & 1) << 1), oidx = ((pc >> 1) & 1) * 512 + cb * 128 + cw * 32 + (pc & 1) * 16 + gw * 4 + jj;
    const float xin = __uint_as_float((unsigned)*(const GAS unsigned short*)(x + oidx * 2) << 16);
    float gate = 0.f;
#pragma nounroll
    for (int pass = 0; pass < 2; ++pass) {
      const bool last = opaque(pass) != 0;
      if (last) { lds_wait_ge<8>(ctrl, C_LANDED, g_kv - 4, budget); lds_wait_ge(ctrl, C_PUB, 1u, budget); }
      else lds_wait_ge<8>(ctrl, C_LANDED, PFAT, budget);
      const int po = (last ? WS_P2 + pc * 16 : WS_PF + gw * 256 + cw * 64 + (pc & 3) * 16) + lane * 4096 + cb * 1024;
      const v4u sv = poll_box<PSLEEP2>(rs, po, budget);
      STAMP(5);
      float t = rows_sum4(__uint_as_float(sv[0]), __uint_as_float(sv[1]), __uint_as_float(sv[2]), __uint_as_float(sv[3]));
      t += dpp<XOR1>(t); t += dpp<XOR2>(t); t += dpp<ROR8>(t); t += dpp<ROR4>(t);
      t = rbf(t);
      if (last) {
        const float y = xin + rbf(t * gate);
        if (col == 0) *(GAS unsigned short*)(out + oidx * 2) = budget <= 0 ? (unsigned short)0x7fc0 : (unsigned short)pk_bf16(y, y);
        break;
      }
      gate = rbf(silu(t));
    }
    STAMP2(6);
    FLUSH();
    return;
  }

  __builtin_amdgcn_s_barrier();
  asm volatile("" ::: "memory");
  if (role == 6) {
    lds_wait_ge<8>(ctrl, C_Q, 1u, budget);
    const u64 tag = *(volatile LAS u64*)(ctrl + C_TAG);
    unsigned prev = 0;
    for (int k = 1; k <= NROUND; ++k) {
      GAS u64* line = (GAS u64*)(ws + WS_TAU) + ((k - 1) * 8 + kvh) * 32;
      const int mark = 4 + ((k * MARK * nt) >> 5);
      if (k == 1) lds_wait_ge<8>(ctrl, C_CLR, 4u, budget);
      lds_wait_ge<1>(ctrl, C_LANDED, mark, budget);
      const unsigned tau = (unsigned)__builtin_amdgcn_s_memrealtime();
      if (lane == 0) __hip_atomic_store(line + gi, (tag << 16) | (tau & 0xffffu), RLX_AGENT);
      lds_wait_ge<4>(ctrl, C_LANDED, mark + MDEL, budget);
      const u64 v = __hip_atomic_load(line + (lane & 31), RLX_AGENT);
      const unsigned seen_t = ((v ^ (tag << 16)) >> 16) == 0 ? (unsigned)v : (unsigned)__builtin_amdgcn_s_memrealtime();
      const float d = wave_sum((float)(short)(seen_t - tau));
      const unsigned mean = tau + __builtin_amdgcn_readfirstlane((int)(d * (1.f / 64)));
      const int rate = k == 1 ? 65536 / TG16 : MARK * 16 * OPT / max((int)(mean - prev), 1);
      prev = mean;
      if (lane == 0) *(volatile LAS v4u*)(ctrl + C_SCHED) = v4u{mean, (unsigned)(k * MARK * 16), (unsigned)rate, 0u};
    }
    while (!__all(((__hip_atomic_load((GAS u64*)(ws + WS_TAU) + kvh * 32 + (lane & 31), RLX_AGENT) ^ (tag << 16)) >> 16) == 0) && --budget > 0)
      __builtin_amdgcn_s_sleep(8);
    if (lane == 0) ctrl[C_FLAG] = 1u;
    if (gi == 0 && lane == 0) __hip_atomic_store((GAS u64*)(ws + WS_GOK) + kvh, tag, RLX_AGENT);
    lds_wait_ge<8>(ctrl, C_LANDED, PFAT - 24, budget);
    while (!__all(__hip_atomic_load((GAS u64*)(ws + WS_GOK) + (lane & 7), RLX_AGENT) == tag) && --budget > 0) __builtin_amdgcn_s_sleep(8);
    if (lane == 0) ctrl[C_ALL] = 1u;
    if (bid == 0 && lane == 0) __hip_atomic_store((GAS u64*)(ws + WS_EPOCH), tag, RLX_AGENT);
    return;
  }

  const int c = role - 2;
  const int blk = col >> 2;
  unsigned ka[4], va[2];
#pragma unroll
  for (int k = 0; k < 4; ++k) ka[k] = col * 256 + (((4 * k + g) ^ col) * 16);
#pragma unroll
  for (int n = 0; n < 2; ++n)
    va[n] = (4 * g + q4) * 256 + (((4 * c + 2 * n + (p4 >> 1)) ^ ((4 * (g & 1) + q4) << 1)) * 16) + 8 * (p4 & 1);
  constexpr float SCALE = 0.08838834764831845f, LOG2E = 1.4426950408889634f;
  {
    const int ntp = nt, fw = C_FREED + c;
    {
      lds_wait_ge<4>(ctrl, C_FREED, 1u, budget);
      const v4u sv = {SENT, SENT, SENT, SENT};
      if (c < 3) __builtin_amdgcn_raw_buffer_store_b128(sv, rs, (c < 2 ? WS_P1 + wid * 2048 + c * 1024 : WS_P2 + wid * 1024) + lane * 16, 0, AUX_SC1);
      if (has_ffn) __builtin_amdgcn_raw_buffer_store_b128(sv, rs, WS_PF + ffn * 4096 + c * 1024 + lane * 16, 0, AUX_SC1);
      wait_vm<0>();
      if (lane == 0) __hip_atomic_fetch_add((LAS unsigned*)(ctrl + C_CLR), 1u, __ATOMIC_RELAXED, __HIP_MEMORY_SCOPE_WORKGROUP);
      lds_wait_ge<2>(ctrl, C_Q, 1u, budget);
    }
    v4u qf[4][4];
#pragma unroll
    for (int k = 0; k < 4; ++k) {
      const v4u qv = *(LAS v4u*)(L + L_Q + ((col & 3) * 128 + 32 * k + 8 * g) * 2);
#pragma unroll
      for (int a = 0; a < 4; ++a) qf[a][k] = blk == a ? qv : v4u{0u, 0u, 0u, 0u};
    }
    float ms = -3.0e38f, lsum = 0.f, linv = 0.f;
    unsigned lam = 0;
    f32x4 acc0 = {0.f, 0.f, 0.f, 0.f}, acc1 = {0.f, 0.f, 0.f, 0.f};
    int slot = SEQ_KV;
    unsigned seen = 0u;
    auto need = [&](unsigned want) {
      if (seen < want) seen = lds_wait_ge(ctrl, C_LANDED, want, budget);
      asm volatile("" ::: "memory");
    };
#pragma nounroll
    for (int t = 0; t < ntp; ++t) {
      const int seq = SEQ_KV + 2 * t;
      LAS char* sb = L + slot * SLOT;
      slot = slot + 1 == NSLOT ? 0 : slot + 1;
      need(4 * seq + 4);
      v4u kf[4][4];
#pragma unroll
      for (int k = 0; k < 4; ++k)
#pragma unroll
        for (int a = 0; a < 4; ++a) kf[a][k] = *(LAS v4u*)(sb + ka[k] + a * 4096);
      __builtin_amdgcn_sched_barrier(0);
      f32x4 s0 = {0.f, 0.f, 0.f, 0.f}, s1 = {0.f, 0.f, 0.f, 0.f};
#pragma unroll
      for (int k = 0; k < 4; ++k) {
        s0 = mfma32(kf[0][k], qf[0][k], s0); s1 = mfma32(kf[1][k], qf[1][k], s1);
        s0 = mfma32(kf[2][k], qf[2][k], s0); s1 = mfma32(kf[3][k], qf[3][k], s1);
      }
      __builtin_amdgcn_sched_barrier(0);
      wait_lgkm();
      if (lane == 0) ctrl[fw] = seq + 1;
      s0 += s1;
      float s[4];
#pragma unroll
      for (int j = 0; j < 2; ++j) {
        const unsigned a = pk_bf16(s0[2 * j], s0[2 * j + 1]), b = pk_bf16(bf_lo(a) * SCALE, bf_hi(a) * SCALE);
        s[2 * j] = bf_lo(b); s[2 * j + 1] = bf_hi(b);
      }
      float mx = max2(max2(s[0], s[1]), max2(s[2], s[3]));
      mx = max2(mx, dpp<ROR8>(mx)); mx = max2(mx, dpp<ROR4>(mx)); mx = xrow_max(mx);
      const float mn = max2(ms, mx * LOG2E), alpha = __builtin_amdgcn_exp2f(ms - mn);
      ms = mn;
#pragma unroll
      for (int j = 0; j < 4; ++j) s[j] = __builtin_amdgcn_exp2f(fmaf(s[j], LOG2E, -mn));
      lsum = fmaf(lsum, alpha, (s[0] + s[1]) + (s[2] + s[3]));
      const unsigned p0 = pk_bf16(s[0], s[1]), p1 = pk_bf16(s[2], s[3]);
      v4u pA = {blk == 0 ? p0 : 0u, blk == 0 ? p1 : 0u, blk == 1 ? p0 : 0u, blk == 1 ? p1 : 0u};
      v4u pB = {blk == 2 ? p0 : 0u, blk == 2 ? p1 : 0u, blk == 3 ? p0 : 0u, blk == 3 ? p1 : 0u};
      acc0 *= alpha; acc1 *= alpha;
      if (t == ntp - 1) {
        lds_wait_ge(ctrl, C_FLAG, 1u, budget);
        const float lt = quad_stride_sum(lsum);
        linv = __builtin_amdgcn_rcpf(lt);
        lam = __float_as_uint(ms + __builtin_amdgcn_logf(lt));
      }
      asm volatile("" : "+v"(pA), "+v"(pB), "+v"(acc0), "+v"(acc1), "+v"(lsum), "+v"(linv), "+v"(lam));
      sb = L + slot * SLOT;
      slot = slot + 1 == NSLOT ? 0 : slot + 1;
      need(4 * seq + 6);
      acc0 = mfma32(cat(lds_tr(sb + va[0]), lds_tr(sb + va[0] + 4096)), pA, acc0);
      acc1 = mfma32(cat(lds_tr(sb + va[1]), lds_tr(sb + va[1] + 4096)), pA, acc1);
      need(4 * seq + 8);
      v4u v0 = cat(lds_tr(sb + va[0] + 8192), lds_tr(sb + va[0] + 12288)), v1 = cat(lds_tr(sb + va[1] + 8192), lds_tr(sb + va[1] + 12288));
      wait_lgkm();
      if (lane == 0) ctrl[fw] = seq + 2;
      asm volatile("" : "+v"(v0), "+v"(v1) :: "memory");
      acc0 = mfma32(v0, pB, acc0);
      acc1 = mfma32(v1, pB, acc1);
    }

    STAMP(0);

    v4u pk;
#pragma unroll
    for (int j = 0; j < 4; ++j) {
      float a = ((col & 4) ? acc1[j] : acc0[j]) + dpp<ROR4>((col & 4) ? acc0[j] : acc1[j]);
      a += dpp<ROR8>(a);
      pk[j] = ((__float_as_uint(a * linv) + 0x80u) & 0xffffff00u) | ((lam >> (8 * j)) & 0xffu);
    }
    __builtin_amdgcn_raw_buffer_store_b128(not_sent(pk), rs, col < 8 ? WS_P1 + wid * 2048 + c * 512 + g * 128 + col * 16 : OOB, 0, AUX_SC1);
    STAMP(1);
    const int rec = WS_P1 + (kvh * 32 + (c >> 1) * 16 + (lane >> 3)) * 2048 + (gi >> 2) * 256 + (c & 1) * 128 + (lane & 7) * 16;
    v4u a0, a1, wa[4];
    need(g_wo);
    LAS char* wp = L + slot * SLOT;
#pragma unroll
    for (int n = 0; n < 4; ++n) wa[n] = cat(lds_tr(wp + va[n & 1] + (n >> 1) * 4096), lds_tr(wp + va[n & 1] + (n >> 1) * 4096 + 8192));
    __builtin_amdgcn_sched_barrier(0);
    STAMP(6);
    poll_box2<PSLEEP>(rs, rec, rec + 8 * 2048, a0, a1, budget);
    STAMP(2);
    const float lA = __uint_as_float(__builtin_amdgcn_perm(__builtin_amdgcn_perm(a0[3], a0[2], 0x04000c0cu), __builtin_amdgcn_perm(a0[1], a0[0], 0x0c0c0400u), 0x07060100u));
    const float lB = __uint_as_float(__builtin_amdgcn_perm(__builtin_amdgcn_perm(a1[3], a1[2], 0x04000c0cu), __builtin_amdgcn_perm(a1[1], a1[0], 0x0c0c0400u), 0x07060100u));
    float M = max2(lA, lB);
    M = xrow_max(max2(M, dpp<ROR8>(M)));
    const float wA = __builtin_amdgcn_exp2f(lA - M), wB = __builtin_amdgcn_exp2f(lB - M);
    float r[4];
#pragma unroll
    for (int j = 0; j < 4; ++j) r[j] = fmaf(wA, __uint_as_float(a0[j] & 0xffffff00u), wB * __uint_as_float(a1[j] & 0xffffff00u));
    float wsum = xrow_sum(wA + wB), ov = rows_sum4(r[0], r[1], r[2], r[3]);
    wsum += dpp<ROR8>(wsum); ov += dpp<ROR8>(ov);
    ov *= __builtin_amdgcn_rcpf(wsum);
    const auto pr = __builtin_amdgcn_permlane32_swap(__float_as_uint(ov), __float_as_uint(ov), false, false);
    if ((lane & 40) == 0) *(LAS unsigned*)(L + L_O + c * 64 + col * 8 + g * 4) = min(pk_bf16(__uint_as_float(pr[0]), __uint_as_float(pr[1])), SENT - 1);
    v4u o0, o1;
    poll_lds_o(lbase + L_O + ((col >> 1) & 1) * 128 + g * 8, o0, o1, budget);
    const f32x4 zero = {0.f, 0.f, 0.f, 0.f};
    const f32x4 sp0 = mfma32(wa[2], o1, mfma32(wa[0], o0, zero)), sp1 = mfma32(wa[3], o1, mfma32(wa[1], o0, zero));
    const f32x4 sp = (col & 1) ? sp1 : sp0;
    v4u so;
#pragma unroll
    for (int j = 0; j < 4; ++j) {
      const auto sw = __builtin_amdgcn_permlane16_swap(__float_as_uint(sp[j]), __float_as_uint(sp[j]), false, false);
      so[j] = !FULLLINE ? __float_as_uint(sp[j]) : (col & 4) ? sw[1] : sw[0];
    }
    __builtin_amdgcn_raw_buffer_store_b128(not_sent(so), rs, (FULLLINE ? (lane & 24) == 0 : col < 4) ? WS_P2 + wid * 1024 + c * 256 + g * 64 + col * 16 : OOB, 0, AUX_SC1);
    if (lane == 0) ctrl[C_PUB] = 1u;
    STAMP(3);
  }
  FLUSH();
}

extern "C" void kernel_launch(void* const* d_in, const int* in_sizes, int n_in, void* d_out, int out_size, void* d_ws, size_t ws_size,
                              hipStream_t stream) {
  (void)hipFuncSetAttribute((const void*)fused_decode, hipFuncAttributeMaxDynamicSharedMemorySize, L_BYTES);
  hipLaunchKernelGGL(fused_decode, dim3(NWG), dim3(448), L_BYTES, stream, (const char*)d_in[0], (const char*)d_in[1], (const char*)d_in[2],
                     (const char*)d_in[3], (const char*)d_in[4], (const char*)d_in[6], (const char*)d_in[7], (char*)d_out, (char*)d_ws);
}
```

```cpp
#include <hip/hip_runtime.h>
#include <hip/hip_bf16.h>
#include <stdint.h>

#define DBG 0
#define W0 23
#define WMIN 12
#define WMAX 32
#define KN 1
#define KD 1
#define TG16 220
#define NROUND 4
#define MARK 64
#define MDEL 16
#define QDELAY 300
#define QSLEEP 4
#define PSLEEP 0
#define PSLEEP2 0
#define FULLLINE 0
#define OPT 258
#define CSLEEP 0
#define TOUCH 1
#define ABL 0
#define OUTAUX 0
#define WI 20
#define FFNAT 0
#define FF0 32
#define FF1 -16
#define PFFLAG 1
#define KEND 0
#define MSTEP 48
#define HEAD 150
#define CLRAUX 16
#define NINIT 1
#define WQAFTER 0
#define PRETOUCH 1
#define WQLATE 1
#define AUXPRIO 1
#define PFAT 176
#define REBAL 1

#define GAS __attribute__((address_space(1)))
#define LAS __attribute__((address_space(3)))
#define RLX_AGENT __ATOMIC_RELAXED, __HIP_MEMORY_SCOPE_AGENT
typedef unsigned long long u64;
typedef unsigned v4u __attribute__((ext_vector_type(4)));
typedef unsigned v2u __attribute__((ext_vector_type(2)));
typedef float f32x4 __attribute__((ext_vector_type(4)));
typedef float f32x2 __attribute__((ext_vector_type(2)));
typedef __bf16 bf16x8 __attribute__((ext_vector_type(8)));
typedef __bf16 bf16x4 __attribute__((ext_vector_type(4)));
typedef __bf16 bf16x2 __attribute__((ext_vector_type(2)));

constexpr int D = 512, SK = 32768, FF = 1792, NWG = 256, NSLOT = 9, SLOT = 16384, SEQ_KV = 1;
constexpr unsigned SENT = 0xFFFFFFFFu;
constexpr int AUX_SC1 = 16, OOB = 0x7ffffff0;

constexpr int WS_EPOCH = 0, WS_GOK = 4096, WS_FOK = 4608, WS_TAU = 8192, WS_QG = 32768, WS_P1 = WS_QG + 65536, WS_P2 = WS_P1 + NWG * 2048,
              WS_PF = WS_P2 + NWG * 1024, WS_END = WS_PF + 64 * 4096, WS_DBG = 131072 + 3 * 256 * 4096;

constexpr int L_CTRL = NSLOT * SLOT, L_XN = L_CTRL + 512, L_Q = L_XN + 2048, L_O = L_Q + 1024, L_W = L_O + 256, L_TR = L_W + 8192, L_BYTES = L_TR + (DBG == 1 ? 256 : 0);
constexpr int C_LANDED = 64, C_ALL = 0, C_WQ = 1, C_Q = 2, C_CLR = 3, C_FREED = 4, C_PUB = 8, C_FLAG = 9, C_TAG = 10, C_SCHED = 12;
static_assert(NROUND >= 2 && WS_TAU + NROUND * 2048 <= WS_QG && MARK + (NROUND - 1) * MSTEP + MDEL <= 248, "");
constexpr int SPIN_BUDGET = 1 << 20;

__device__ __forceinline__ unsigned pk_bf16(float lo, float hi) {
  return __builtin_bit_cast(unsigned, __builtin_convertvector(f32x2{lo, hi}, bf16x2));
}
__device__ __forceinline__ float bf_lo(unsigned u) { return __uint_as_float(u << 16); }
__device__ __forceinline__ float bf_hi(unsigned u) { return __uint_as_float(u & 0xffff0000u); }
__device__ __forceinline__ float rbf(float f) { return bf_lo(pk_bf16(f, f)); }
template <int CTRL> __device__ __forceinline__ float dpp(float x) {
  return __builtin_bit_cast(float, __builtin_amdgcn_mov_dpp(__builtin_bit_cast(int, x), CTRL, 0xf, 0xf, true));
}
constexpr int XOR1 = 0xB1, XOR2 = 0x4E, ROR4 = 0x124, ROR8 = 0x128;
__device__ __forceinline__ float xrow_sum(float x) {
  auto s = __builtin_amdgcn_permlane16_swap(__float_as_uint(x), __float_as_uint(x), false, false);
  x = __uint_as_float(s[0]) + __uint_as_float(s[1]);
  auto t = __builtin_amdgcn_permlane32_swap(__float_as_uint(x), __float_as_uint(x), false, false);
  return __uint_as_float(t[0]) + __uint_as_float(t[1]);
}
__device__ __forceinline__ float quad_stride_sum(float x) {
  x += dpp<ROR8>(x); x += dpp<ROR4>(x);
  return xrow_sum(x);
}
__device__ __forceinline__ float rows_sum4(float a, float b, float c, float d) {
  const auto s = __builtin_amdgcn_permlane32_swap(__float_as_uint(a), __float_as_uint(b), false, false);
  const auto t = __builtin_amdgcn_permlane32_swap(__float_as_uint(c), __float_as_uint(d), false, false);
  const float ab = __uint_as_float(s[0]) + __uint_as_float(s[1]), cd = __uint_as_float(t[0]) + __uint_as_float(t[1]);
  const auto u = __builtin_amdgcn_permlane16_swap(__float_as_uint(ab), __float_as_uint(cd), false, false);
  return __uint_as_float(u[0]) + __uint_as_float(u[1]);
}
__device__ __forceinline__ float max2(float a, float b) {
  float top = 3.4028235e38f;
  asm("" : "+s"(top));
  return __builtin_amdgcn_fmed3f(a, b, top);
}
__device__ __forceinline__ float xrow_max(float x) {
  const auto s = __builtin_amdgcn_permlane16_swap(__float_as_uint(x), __float_as_uint(x), false, false);
  x = max2(__uint_as_float(s[0]), __uint_as_float(s[1]));
  const auto t = __builtin_amdgcn_permlane32_swap(__float_as_uint(x), __float_as_uint(x), false, false);
  return max2(__uint_as_float(t[0]), __uint_as_float(t[1]));
}
__device__ __forceinline__ float wave_sum(float x) {
  x += dpp<XOR1>(x); x += dpp<XOR2>(x);
  return quad_stride_sum(x);
}
__device__ __forceinline__ float half_sum(float x) {
  x += dpp<XOR1>(x); x += dpp<XOR2>(x); x += dpp<ROR8>(x); x += dpp<ROR4>(x);
  const auto s = __builtin_amdgcn_permlane16_swap(__float_as_uint(x), __float_as_uint(x), false, false);
  return __uint_as_float(s[0]) + __uint_as_float(s[1]);
}
__device__ __forceinline__ f32x4 mfma32(v4u a, v4u b, f32x4 c) {
  return __builtin_amdgcn_mfma_f32_16x16x32_bf16(__builtin_bit_cast(bf16x8, a), __builtin_bit_cast(bf16x8, b), c, 0, 0, 0);
}
__device__ __forceinline__ v2u lds_tr(LAS char* p) {
  return __builtin_bit_cast(v2u, __builtin_amdgcn_ds_read_tr16_b64_v4bf16((LAS bf16x4*)p));
}
__device__ __forceinline__ v4u cat(v2u a, v2u b) { return v4u{a[0], a[1], b[0], b[1]}; }
__device__ __forceinline__ v4u not_sent(v4u u) {
  return v4u{min(u[0], SENT - 1), min(u[1], SENT - 1), min(u[2], SENT - 1), min(u[3], SENT - 1)};
}
__device__ __forceinline__ float silu(float v) { return v / (1.f + __expf(-v)); }

__device__ __forceinline__ void dma1(unsigned voff, const char* sbase, unsigned ldsaddr) {
  asm volatile("s_mov_b32 m0, %2\n\ts_nop 0\n\tglobal_load_lds_dwordx4 %0, %1 nt" :: "v"(voff), "s"(sbase), "s"(ldsaddr) : "memory");
}
__device__ __forceinline__ void dma4(unsigned v0, unsigned v1, unsigned v2, unsigned v3, const char* sbase, unsigned ldsaddr) {
  asm volatile("s_mov_b32 m0, %5\n\ts_nop 0\n\t"
               "global_load_lds_dwordx4 %0, %4 nt\n\tglobal_load_lds_dwordx4 %1, %4 offset:1024 nt\n\t"
               "global_load_lds_dwordx4 %2, %4 offset:2048 nt\n\tglobal_load_lds_dwordx4 %3, %4 offset:3072 nt"
               :: "v"(v0), "v"(v1), "v"(v2), "v"(v3), "s"(sbase), "s"(ldsaddr) : "memory");
}
template <int N> __device__ __forceinline__ void wait_vm() { asm volatile("s_waitcnt vmcnt(%0)" :: "i"(N) : "memory"); }
#define SLOT_GROUP(W, STEP) \
  "s_mov_b32 m0, %[lds]\n\ts_add_u32 %[lds], %[lds], 0x1000\n\t" \
  "global_load_lds_dwordx4 %[v0], vcc nt\n\tglobal_load_lds_dwordx4 %[v1], vcc offset:1024 nt\n\t" \
  "global_load_lds_dwordx4 %[v2], vcc offset:2048 nt\n\tglobal_load_lds_dwordx4 %[v3], vcc offset:3072 nt\n\t" \
  "s_add_u32 vcc_lo, vcc_lo, " STEP "\n\ts_addc_u32 vcc_hi, vcc_hi, 0\n\t" \
  "s_add_i32 %[cand], %[cand], 1\n\ts_max_i32 %[pub], %[pub], %[cand]\n\tv_mov_b32 %[tmp], %[pub]\n\t" \
  "s_waitcnt vmcnt(" #W ")\n\tds_write_b32 %[va], %[tmp]\n\t"
#define SLOT_LEVEL(W) \
  "s_mov_b64 vcc, %[src]\n\t" SLOT_GROUP(W, "0x1000") SLOT_GROUP(W, "%[mid]") SLOT_GROUP(W, "0x1000") SLOT_GROUP(W, "0") "s_branch 9f\n\t"
__device__ __forceinline__ void issue_slot(unsigned v0, unsigned v1, unsigned v2, unsigned v3, const char* src, int mid, unsigned lds,
                                           int w, int cand, int& pub, unsigned va) {
  unsigned tmp;
  int jump;
  asm volatile("s_mul_i32 %[jmp], %[lvl], 8f-7f\n\ts_getpc_b64 vcc\n6:\n\ts_add_u32 %[jmp], %[jmp], 7f-6b\n\t"
               "s_add_u32 vcc_lo, vcc_lo, %[jmp]\n\ts_addc_u32 vcc_hi, vcc_hi, 0\n\ts_setpc_b64 vcc\n7:\n\t"
               SLOT_LEVEL(12) "8:\n\t" SLOT_LEVEL(16) SLOT_LEVEL(20) SLOT_LEVEL(24) SLOT_LEVEL(28) SLOT_LEVEL(32) "9:"
               : [lds] "+&s"(lds), [cand] "+&s"(cand), [pub] "+&s"(pub), [tmp] "=&v"(tmp), [jmp] "=&s"(jump)
               : [v0] "v"(v0), [v1] "v"(v1), [v2] "v"(v2), [v3] "v"(v3), [src] "s"(src), [mid] "s"(mid), [lvl] "s"(w - 3), [va] "v"(va)
               : "vcc", "scc", "memory");
}
__device__ __forceinline__ int opaque(int v) { asm volatile("" : "+s"(v)); return v; }
__device__ __forceinline__ void wait_lgkm() { asm volatile("s_waitcnt lgkmcnt(0)" ::: "memory"); }

typedef volatile LAS unsigned* ctrl_t;
template <int SLEEP = 0> __device__ __forceinline__ unsigned lds_wait_ge(ctrl_t ctrl, int word, unsigned want, int& budget) {
  unsigned v, tmp;
  int go;
  budget = __builtin_amdgcn_readfirstlane(budget);
  want = __builtin_amdgcn_readfirstlane(want);
  asm volatile("1:\n\tds_read_b32 %[t], %[a]\n\t.if %[slp]\n\ts_sleep %[slp]\n\t.endif\n\ts_waitcnt lgkmcnt(0)\n\tv_readfirstlane_b32 %[v], %[t]\n\t"
               "s_cmp_lt_u32 %[v], %[w]\n\ts_cselect_b32 %[g], %[b], 0\n\ts_subb_u32 %[b], %[b], 0\n\ts_cmp_gt_i32 %[g], 0\n\ts_cbranch_scc1 1b"
               : [v] "=&s"(v), [t] "=&v"(tmp), [g] "=&s"(go), [b] "+&s"(budget)
               : [a] "v"((unsigned)(uintptr_t)(ctrl + word)), [w] "s"(want), [slp] "i"(SLEEP) : "scc", "memory");
  return v;
}
#define BOX_LOOP_END \
  "v_cmpx_eq_u32 vcc, -1, %[t]\n\ts_sub_i32 %[b], %[b], 1\n\ts_cmp_gt_i32 %[b], 0\n\ts_cselect_b64 vcc, exec, 0\n\t" \
  "s_cmp_lg_u64 vcc, 0\n\ts_cbranch_scc0 2f\n\t.if %[slp]\n\ts_sleep %[slp]\n\t.endif\n\ts_branch 1b\n2:\n\ts_mov_b64 exec, -1"
template <int SLEEP> __device__ __forceinline__ v4u poll_box(__amdgpu_buffer_rsrc_t rs, int off, int& budget) {
  v4u a;
  unsigned tmp;
  budget = __builtin_amdgcn_readfirstlane(budget);
  asm volatile("1:\n\tbuffer_load_dwordx4 v[100:103], %[o], %[rs], 0 offen sc1\n\ts_waitcnt vmcnt(0)\n\t"
               "v_max_u32 %[t], v100, v101\n\tv_max3_u32 %[t], %[t], v102, v103\n\t" BOX_LOOP_END
               : "=&{v[100:103]}"(a), [t] "=&v"(tmp), [b] "+&s"(budget) : [o] "v"(off), [rs] "s"(rs), [slp] "i"(SLEEP) : "vcc", "scc", "memory");
  return a;
}
template <int SLEEP> __device__ __forceinline__ void poll_box2(__amdgpu_buffer_rsrc_t rs, int off0, int off1, v4u& a, v4u& b, int& budget) {
  unsigned tmp;
  budget = __builtin_amdgcn_readfirstlane(budget);
  asm volatile("1:\n\tbuffer_load_dwordx4 v[100:103], %[o0], %[rs], 0 offen sc1\n\tbuffer_load_dwordx4 v[104:107], %[o1], %[rs], 0 offen sc1\n\t"
               "s_waitcnt vmcnt(0)\n\tv_max_u32 %[t], v100, v101\n\tv_max3_u32 %[t], %[t], v102, v103\n\t"
               "v_max3_u32 %[t], %[t], v104, v105\n\tv_max3_u32 %[t], %[t], v106, v107\n\t" BOX_LOOP_END
               : "=&{v[100:103]}"(a), "=&{v[104:107]}"(b), [t] "=&v"(tmp), [b] "+&s"(budget)
               : [o0] "v"(off0), [o1] "v"(off1), [rs] "s"(rs), [slp] "i"(SLEEP) : "vcc", "scc", "memory");
}
__device__ __forceinline__ void poll_lds_o(unsigned addr, v4u& a, v4u& b, int& budget) {
  unsigned tmp;
  budget = __builtin_amdgcn_readfirstlane(budget);
  asm volatile("1:\n\tds_read_b64 v[100:101], %[o]\n\tds_read_b64 v[102:103], %[o] offset:32\n\tds_read_b64 v[104:105], %[o] offset:64\n\t"
               "ds_read_b64 v[106:107], %[o] offset:96\n\ts_waitcnt lgkmcnt(0)\n\tv_max_u32 %[t], v100, v101\n\tv_max3_u32 %[t], %[t], v102, v103\n\t"
               "v_max3_u32 %[t], %[t], v104, v105\n\tv_max3_u32 %[t], %[t], v106, v107\n\t" BOX_LOOP_END
               : "=&{v[100:103]}"(a), "=&{v[104:107]}"(b), [t] "=&v"(tmp), [b] "+&s"(budget) : [o] "v"(addr), [slp] "i"(0) : "vcc", "scc", "memory");
}

#define STAMP(i) do {} while (0)
#define STAMP2(i) do {} while (0)
#define FLUSH() do {} while (0)

typedef unsigned v16u __attribute__((ext_vector_type(16)));
__device__ __forceinline__ char* as_ptr(unsigned lo, unsigned hi) { return (char*)(((u64)hi << 32) | lo); }

__global__ void __launch_bounds__(448) fused_decode(const char*, const char*, const char*, const char*, const char*, const char*, const char*,
                                                    char*, char*) {
  v16u ka16;
  v2u ka2;
  asm volatile("s_load_dwordx16 s[36:51], %2, 0x0\n\ts_load_dwordx2 s[52:53], %2, 0x40"
               : "={s[36:51]}"(ka16), "={s[52:53]}"(ka2) : "s"(__builtin_amdgcn_kernarg_segment_ptr()));
  __builtin_amdgcn_sched_barrier(0);
  extern __shared__ __attribute__((aligned(16))) char smem[];
  LAS char* L = (LAS char*)smem;
  const unsigned lbase = (unsigned)(uintptr_t)L;
  ctrl_t ctrl = (ctrl_t)(L + L_CTRL);
  const int lane = threadIdx.x & 63, bid = blockIdx.x;
  int wave = __builtin_amdgcn_readfirstlane(threadIdx.x >> 6);
  const int xg = bid & 7, tg = bid >> 3, kvh = tg >> 2, ii = tg & 3, gi = 4 * xg + ii, wid = kvh * 32 + gi, bb = gi >> 4;
  const int nt = REBAL ? (ii == 0 ? 33 : ii == 1 ? 31 : 32) : 32, tile0 = (xg & 3) * 128 + (REBAL && ii == 1 ? 33 : 32 * ii);
  const int seq_wo = SEQ_KV + 2 * nt, g_kv = 4 * seq_wo, g_wo = g_kv + 4;
  const int g = lane >> 4, col = lane & 15, q4 = (lane >> 2) & 3, p4 = lane & 3;
  const int ffn = xg * 8 + kvh;
  const bool has_ffn = ii == 1;
  unsigned vk[4], vv[2], vx = lane * 16, vq = (lane >> 1) * 8192 + (lane & 1) * 16;
#pragma unroll
  for (int j = 0; j < 4; ++j) vk[j] = g * 256 + ((col ^ (4 * j + g)) * 16);
#pragma unroll
  for (int j = 0; j < 2; ++j) vv[j] = g * 256 + ((col ^ ((4 * j + g) << 1)) * 16);
  unsigned kvoff = ((bb * 8 + kvh) * SK + tile0 * 64) * 256, qoff = (kvh * 512 + gi * 16) * 2;
  asm volatile("" : "+v"(vk[0]), "+v"(vk[1]), "+v"(vk[2]), "+v"(vk[3]), "+v"(vv[0]), "+v"(vv[1]), "+v"(vx), "+v"(vq));
  asm volatile("" : "+s"(kvoff), "+s"(qoff), "+s"(wave));
  ctrl[lane] = 0u;
  ctrl[C_LANDED + lane] = 0u;
  *(volatile LAS unsigned*)(L + L_O + lane * 4) = SENT;
  asm volatile("s_waitcnt lgkmcnt(0)\n\ts_barrier" : "+{s[36:51]}"(ka16), "+{s[52:53]}"(ka2) :: "memory");
  const char *x = as_ptr(ka16[0], ka16[1]), *kc = as_ptr(ka16[2], ka16[3]), *vc = as_ptr(ka16[4], ka16[5]), *wq = as_ptr(ka16[6], ka16[7]),
             *wo = as_ptr(ka16[8], ka16[9]), *w1 = as_ptr(ka16[10], ka16[11]), *w2 = as_ptr(ka16[12], ka16[13]);
  char *out = as_ptr(ka16[14], ka16[15]), *ws = as_ptr(ka2[0], ka2[1]);
  const __amdgpu_buffer_rsrc_t rs = __builtin_amdgcn_make_buffer_rsrc((void*)ws, 0, WS_END, 0x00020000);
  auto pretouch = [&]() {
    const u64 pk_ = (u64)(kc + kvoff), pv_ = (u64)(vc + kvoff), pq_ = (u64)(wq + qoff), pq2 = pq_ + 511 * 8192;
    asm volatile("v_writelane_b32 v100, %0, 0\n\tv_writelane_b32 v101, %1, 0\n\tv_writelane_b32 v100, %2, 1\n\tv_writelane_b32 v101, %3, 1\n\t"
                 "v_writelane_b32 v100, %4, 2\n\tv_writelane_b32 v101, %5, 2\n\tv_writelane_b32 v100, %6, 3\n\tv_writelane_b32 v101, %7, 3\n\t"
                 "v_writelane_b32 v100, %8, 4\n\tv_writelane_b32 v101, %9, 4\n\tv_writelane_b32 v100, %10, 5\n\tv_writelane_b32 v101, %11, 5\n\t"
                 "s_mov_b32 m0, %12\n\ts_mov_b64 exec, 63\n\tglobal_load_lds_dword v[100:101], off nt\n\ts_mov_b64 exec, -1"
                 :: "s"((unsigned)pk_), "s"((unsigned)(pk_ >> 32)), "s"((unsigned)pv_), "s"((unsigned)(pv_ >> 32)), "s"(ka16[0]), "s"(ka16[1]),
                    "s"(ka2[0]), "s"(ka2[1]), "s"((unsigned)pq_), "s"((unsigned)(pq_ >> 32)), "s"((unsigned)pq2), "s"((unsigned)(pq2 >> 32)),
                    "s"(lbase + L_W + 4096) : "v100", "v101", "memory");
  };
  const unsigned t_start = (unsigned)__builtin_amdgcn_s_memrealtime();
  if (PRETOUCH == 3 && wave == 2) pretouch();
  if (wave == 1) {
    __builtin_amdgcn_s_setprio(3);
    STAMP2(0);
    if (PRETOUCH == 1) pretouch();
#pragma unroll
    for (int j = 0; j < 4; ++j) dma4(vk[0], vk[1], vk[2], vk[3], kc + kvoff + j * 4096, lbase + SEQ_KV * SLOT + j * 4096);
#pragma unroll
    for (int j = 0; j < 4 * (NINIT > 1); ++j) dma4(vv[0], vv[1], vv[0], vv[1], vc + kvoff + j * 4096, lbase + (SEQ_KV + 1) * SLOT + j * 4096);
#pragma unroll
    for (int j = 0; j < 4 * (NINIT > 2); ++j) dma4(vk[0], vk[1], vk[2], vk[3], kc + kvoff + SLOT + j * 4096, lbase + (SEQ_KV + 2) * SLOT + j * 4096);
  }
  if (opaque(wave) == 0) {
    if (AUXPRIO) __builtin_amdgcn_s_setprio(3);
    STAMP2(7);
    if (PRETOUCH == 2) pretouch();
    asm volatile("s_mov_b32 m0, %4\n\ts_nop 0\n\tglobal_load_lds_dwordx4 %0, %1\n\tglobal_load_lds_dwordx4 %0, %1 offset:1024\n\t"
                 "s_mov_b32 m0, %5\n\ts_nop 0\n\tglobal_load_lds_dwordx4 %2, %3 sc1"
                 :: "v"(vx), "s"(x), "v"(WS_EPOCH), "s"(ws), "s"(lbase + L_W), "s"(lbase + L_W + 2048) : "memory");
  }
  if (opaque(wave) == 6) {
#pragma nounroll
    for (int j = 0; j < 16; ++j) dma1(vq, wq + qoff + (size_t)j * (32 * 8192), lbase + j * 1024);
    wait_vm<0>();
    if (lane == 0) ctrl[C_WQ] = 1u;
  }
  const int role = opaque(wave);
  int budget = SPIN_BUDGET;

  if (__builtin_expect(role == 1, 1)) {
    const char* kb = kc + kvoff;
    const char* vb = vc + kvoff;
    const char* wb = wo + (size_t)(kvh * 512 + 32 * (gi >> 3) + 8 * ((gi >> 2) & 1)) * 1024 + ii * 256;
    const unsigned va_pub = lbase + L_CTRL + (C_LANDED + lane) * 4;
    const int w16 = W0 * 16 + (ii == 0 ? FF0 : ii == 1 ? FF1 : 0);
    int issued = 4 + 4 * NINIT, slot = SEQ_KV + NINIT, pub = 0, wl = WI / 4, dith = 0;
#pragma nounroll
    for (int seq = SEQ_KV + NINIT; seq <= seq_wo; ++seq) {
      {
        const v4u sc = *(volatile LAS v4u*)(ctrl + C_SCHED);
        v4u f = *(volatile LAS v4u*)(ctrl + C_FREED);
        const int el = (int)((unsigned)__builtin_amdgcn_s_memrealtime() - __builtin_amdgcn_readfirstlane(sc[0]));
        const int rate = __builtin_amdgcn_readfirstlane(sc[2]);
        const int lag16 = rate ? ((((int)__builtin_amdgcn_readfirstlane(sc[1]) + ((el * rate) >> 8)) * nt) >> 5) - (pub - 4) * 16 : (WI - W0) * 16 * KD / (KN ? KN : 1);
        const int wk = w16 + (KEND && seq > KEND ? 2 * lag16 : lag16 * KN / KD) + dith;
        wl = __builtin_amdgcn_readfirstlane(min(max((wk + 32) >> 6, WMIN / 4), WMAX / 4));
        dith = min(max(wk - wl * 64, -32), 32);
        if ((int)__builtin_amdgcn_readfirstlane(min(min(f[0], f[1]), min(f[2], f[3]))) <= seq - NSLOT) {
          wait_vm<0>(); pub = issued; ctrl[C_LANDED + lane] = pub;
          do {
            __builtin_amdgcn_s_sleep(1);
            f = *(volatile LAS v4u*)(ctrl + C_FREED);
          } while ((int)__builtin_amdgcn_readfirstlane(min(min(f[0], f[1]), min(f[2], f[3]))) <= seq - NSLOT && --budget > 0);
        }
      }
      const bool is_wo = seq == seq_wo, is_k = (seq & 1) != 0 && !is_wo;
      unsigned v[4];
#pragma unroll
      for (int j = 0; j < 4; ++j) v[j] = is_k ? vk[j] : is_wo ? vv[j & 1] + g * 768 + j * (127 * 1024) : vv[j & 1];
      const char* src = is_wo ? wb : (is_k ? kb : vb) + ((seq - SEQ_KV) >> 1) * SLOT;
      issue_slot(v[0], v[1], v[2], v[3], src, is_wo ? 12288 : 4096, lbase + slot * SLOT, wl, issued - wl, pub, va_pub);
      issued += 4;
      slot = slot + 1 == NSLOT ? 0 : slot + 1;
    }
#define DRAIN_TO(N) { wait_vm<4 * (N)>(); pub = max(pub, issued - (N)); ctrl[C_LANDED + lane] = pub; }
    DRAIN_TO(7) DRAIN_TO(6) DRAIN_TO(5) DRAIN_TO(4) DRAIN_TO(0)
#undef DRAIN_TO
    STAMP(2);
    FLUSH();
    return;
  }

  if (role == 0) {
    u64 tag;
    v4u xb[16];
    {
      wait_vm<0>();
      STAMP(4);
      tag = *(volatile LAS u64*)(L + L_W + 2048) + 1;
      const v4u xv0 = *(volatile LAS v4u*)(L + L_W + lane * 32), xv1 = *(volatile LAS v4u*)(L + L_W + lane * 32 + 16);
      float v[16];
#pragma unroll
      for (int e = 0; e < 4; ++e) { v[2 * e] = bf_lo(xv0[e]); v[2 * e + 1] = bf_hi(xv0[e]); v[8 + 2 * e] = bf_lo(xv1[e]); v[9 + 2 * e] = bf_hi(xv1[e]); }
      float s4[4], q4s[4];
#pragma unroll
      for (int e = 0; e < 4; ++e) s4[e] = (v[e] + v[4 + e]) + (v[8 + e] + v[12 + e]);
      const float mean = half_sum((s4[0] + s4[1]) + (s4[2] + s4[3])) * (1.f / D);
#pragma unroll
      for (int e = 0; e < 4; ++e) {
        const float d0 = v[e] - mean, d1 = v[4 + e] - mean, d2 = v[8 + e] - mean, d3 = v[12 + e] - mean;
        q4s[e] = fmaf(d0, d0, d1 * d1) + fmaf(d2, d2, d3 * d3);
      }
      const float var = half_sum((q4s[0] + q4s[1]) + (q4s[2] + q4s[3])) * (1.f / D);
      const float mb = rbf(mean), den = rbf(__builtin_amdgcn_sqrtf(rbf(rbf(var) + 1e-5f))), rden = __builtin_amdgcn_rcpf(den);
      unsigned o[8];
#pragma unroll
      for (int e = 0; e < 8; ++e) {
        const unsigned dp = pk_bf16(v[2 * e] - mb, v[2 * e + 1] - mb);
        const float d0 = bf_lo(dp), d1 = bf_hi(dp), q0 = d0 * rden, q1 = d1 * rden;
        o[e] = pk_bf16(fmaf(fmaf(-q0, den, d0), rden, q0), fmaf(fmaf(-q1, den, d1), rden, q1));
      }
      *(LAS v4u*)(L + L_XN + lane * 32) = v4u{o[0], o[1], o[2], o[3]};
      *(LAS v4u*)(L + L_XN + lane * 32 + 16) = v4u{o[4], o[5], o[6], o[7]};
      asm volatile("" ::: "memory");
      LAS char* xa = L + L_XN + (((col >> 1) & 1) * 512 + 4 * g) * 2;
#pragma unroll
      for (int ks = 0; ks < 16; ++ks) xb[ks] = cat(*(LAS v2u*)(xa + ks * 64), *(LAS v2u*)(xa + ks * 64 + 32));
      __builtin_amdgcn_sched_barrier(0);
      STAMP(0);
      lds_wait_ge(ctrl, C_WQ, 1u, budget);
      STAMP(1);
      f32x4 qp[2] = {{0.f, 0.f, 0.f, 0.f}, {0.f, 0.f, 0.f, 0.f}};
      LAS char* wa = L + 8 * lane;
      v4u fa[16];
#pragma unroll
      for (int ks = 0; ks < 16; ++ks) fa[ks] = cat(lds_tr(wa + ks * 1024), lds_tr(wa + ks * 1024 + 512));
      __builtin_amdgcn_sched_barrier(0);
#pragma unroll
      for (int ks = 0; ks < 16; ++ks) qp[ks & 1] = mfma32(fa[ks], xb[ks], qp[ks & 1]);
      const f32x4 qa = qp[0] + qp[1];
      if ((col & 13) == 0) *(LAS v2u*)(L + L_Q + (col >> 1) * 32 + g * 8) = v2u{pk_bf16(qa[0], qa[1]), pk_bf16(qa[2], qa[3])};
      asm volatile("" ::: "memory");
      const unsigned thi = (unsigned)(tag >> 16), tlo = (unsigned)tag << 16;
      const unsigned qv = *(LAS unsigned short*)(L + L_Q + (lane & 31) * 2);
      __builtin_amdgcn_raw_buffer_store_b64(v2u{tlo | qv, thi}, rs, lane < 32 && !(ABL & 16) ? WS_QG + ((kvh * 2 + g) * 512 + gi * 16 + col) * 8 : OOB, 0, AUX_SC1);
      if (lane < 4) ctrl[C_FREED + lane] = 1u;
      if (lane == 0) *(volatile LAS u64*)(ctrl + C_TAG) = tag;
      STAMP(2);
    }
    {
      const unsigned thi = (unsigned)(tag >> 16), tlo = (unsigned)tag << 16;
      while ((int)((unsigned)__builtin_amdgcn_s_memrealtime() - t_start) < QDELAY) __builtin_amdgcn_s_sleep(2);
      const int qo = WS_QG + (kvh * 2 + bb) * 4096 + lane * 8;
      v2u a[8];
#pragma unroll
      for (int k = 0; k < 8; ++k) a[k] = v2u{~tlo, ~thi};
      for (; !(ABL & 2);) {
        asm volatile("" ::: "memory");
        bool all = true;
        v2u nv[8];
#pragma unroll
        for (int k = 0; k < 8; ++k) {
          const bool ok = a[k][1] == thi && ((a[k][0] ^ tlo) >> 16) == 0;
          nv[k] = __builtin_amdgcn_raw_buffer_load_b64(rs, ok ? OOB : qo + 512 * k, 0, AUX_SC1);
        }
#pragma unroll
        for (int k = 0; k < 8; ++k) {
          const bool ok = a[k][1] == thi && ((a[k][0] ^ tlo) >> 16) == 0;
          a[k] = ok ? a[k] : nv[k];
          all = all && a[k][1] == thi && ((a[k][0] ^ tlo) >> 16) == 0;
        }
        if (__all(all)) break;
        if (--budget <= 0) break;
        if (QSLEEP) __builtin_amdgcn_s_sleep(QSLEEP);
      }
#pragma unroll
      for (int k = 0; k < 8; ++k) *(LAS unsigned short*)(L + L_Q + (64 * k + lane) * 2) = (unsigned short)a[k][0];
      wait_lgkm();
      if (lane == 0) ctrl[C_Q] = 1u;
    }
    STAMP(3);
    auto ffn_slice = [&]() {
      const int u0 = (28 * ffn) & ~7, lo = 28 * ffn - u0;
      const unsigned vo1 = (lane >> 2) * (FF * 2) + ((p4 ^ ((lane >> 3) & 2)) * 16);
      LAS char* wa = L + L_W + (4 * g + q4) * 64 + 8 * p4;
      f32x4 h0 = {0.f, 0.f, 0.f, 0.f}, h1 = h0;
#pragma unroll
      for (int rd = 0; rd < 4; ++rd) {
#pragma unroll
        for (int j = 0; j < 8; ++j) dma1(vo1, w1 + u0 * 2 + (size_t)(rd * 128 + j * 16) * (FF * 2), lbase + L_W + j * 1024);
        wait_vm<0>();
        v4u fa[4][2];
#pragma unroll
        for (int ks = 0; ks < 4; ++ks)
#pragma unroll
          for (int nb = 0; nb < 2; ++nb) {
            LAS char* p = wa + ks * 2048 + ((nb ^ (g & 1)) * 32);
            fa[ks][nb] = cat(lds_tr(p), lds_tr(p + 1024));
          }
        __builtin_amdgcn_sched_barrier(0);
        wait_lgkm();
#pragma unroll
        for (int ks = 0; ks < 4; ++ks) { h0 = mfma32(fa[ks][0], xb[4 * rd + ks], h0); h1 = mfma32(fa[ks][1], xb[4 * rd + ks], h1); }
      }
      float hv[8];
#pragma unroll
      for (int j = 0; j < 8; ++j) {
        const int u = (j >> 2) * 16 + 4 * g + (j & 3);
        hv[j] = (u >= lo && u < lo + 28) ? silu(rbf(j < 4 ? h0[j & 3] : h1[j & 3])) : 0.f;
      }
      const v4u hb = {pk_bf16(hv[0], hv[1]), pk_bf16(hv[2], hv[3]), pk_bf16(hv[4], hv[5]), pk_bf16(hv[6], hv[7])};
      const unsigned vo2[2] = {(unsigned)(g * 1024 + ((col ^ (g << 1)) * 16)), (unsigned)(g * 1024 + ((col ^ ((4 + g) << 1)) * 16))};
      const int sw = (4 * (g & 1) + q4) << 1;
      LAS char* w2a = L + L_W + (4 * g + q4) * 256 + 8 * (p4 & 1);
      const int mine = 2 * (col >> 2) + (col & 1);
#pragma unroll
      for (int cb = 0; cb < 4; ++cb) {
#pragma unroll
        for (int j = 0; j < 8; ++j) dma1(vo2[j & 1], w2 + (size_t)(u0 + 4 * j) * 1024 + cb * 256, lbase + L_W + j * 1024);
        wait_vm<0>();
        v4u wf[8];
#pragma unroll
        for (int nb = 0; nb < 8; ++nb) {
          LAS char* p = w2a + (((2 * nb + (p4 >> 1)) ^ sw) * 16);
          wf[nb] = cat(lds_tr(p), lds_tr(p + 4096));
        }
        __builtin_amdgcn_sched_barrier(0);
        wait_lgkm();
        f32x4 fo = {0.f, 0.f, 0.f, 0.f};
#pragma unroll
        for (int nb = 0; nb < 8; ++nb) fo = mfma32(wf[nb], mine == nb ? hb : v4u{0u, 0u, 0u, 0u}, fo);
        __builtin_amdgcn_raw_buffer_store_b128(not_sent(__builtin_bit_cast(v4u, fo)), rs, WS_PF + ffn * 4096 + cb * 1024 + lane * 16, 0, AUX_SC1);
      }
    };
    if (has_ffn && !(ABL & 8)) { lds_wait_ge<4>(ctrl, C_CLR, 4u, budget); if (FFNAT) lds_wait_ge<8>(ctrl, C_LANDED, FFNAT, budget);
      ffn_slice();
      if (PFFLAG) { wait_vm<0>(); if (lane == 0) __hip_atomic_store((GAS u64*)(ws + WS_FOK) + ffn, tag, RLX_AGENT); }
    }

    lds_wait_ge<8>(ctrl, C_ALL, 1u, budget);
    const int cb = xg >> 1, pc = (xg & 1) * 32 + tg, cw = pc >> 4, gw = (pc >> 2) & 3;
    const int jj = (g >> 1) | ((g & 1) << 1), oidx = ((pc >> 1) & 1) * 512 + cb * 128 + cw * 32 + (pc & 1) * 16 + gw * 4 + jj;
    const float xin = __uint_as_float((unsigned)*(const GAS unsigned short*)(x + oidx * 2) << 16);
    float gate = 0.f;
    if (TOUCH) {
      const unsigned t0 = *(const GAS unsigned short*)(out + oidx * 2), t1 = *(const GAS unsigned short*)(wo + (size_t)(kvh * 512 + g * 128 + 4 * gi) * 1024);
      asm volatile("" :: "v"(t0), "v"(t1));
    }
#pragma nounroll
    for (int pass = 0; pass < 2; ++pass) {
      const bool last = opaque(pass) != 0;
      if (last) { lds_wait_ge<8>(ctrl, C_LANDED, g_kv - 4, budget); lds_wait_ge(ctrl, C_PUB, 1u, budget); }
      else {
        lds_wait_ge<8>(ctrl, C_LANDED, PFAT, budget);
        while (PFFLAG && !__all(__hip_atomic_load((GAS u64*)(ws + WS_FOK) + lane, RLX_AGENT) == tag) && --budget > 0) __builtin_amdgcn_s_sleep(8);
      }
      const int po = (last ? WS_P2 + pc * 16 : WS_PF + gw * 256 + cw * 64 + (pc & 3) * 16) + lane * 4096 + cb * 1024 + (!last && (ABL & 8) ? OOB - WS_END : 0);
      const v4u sv = poll_box<PSLEEP2>(rs, po, budget);
      STAMP(5);
      float t = rows_sum4(__uint_as_float(sv[0]), __uint_as_float(sv[1]), __uint_as_float(sv[2]), __uint_as_float(sv[3]));
      t += dpp<XOR1>(t); t += dpp<XOR2>(t); t += dpp<ROR8>(t); t += dpp<ROR4>(t);
      t = rbf(t);
      if (last) {
        const float y = xin + rbf(t * gate);
        const __amdgpu_buffer_rsrc_t ro = __builtin_amdgcn_make_buffer_rsrc((void*)out, 0, 2 * D * 2, 0x00020000);
        __builtin_amdgcn_raw_buffer_store_b16(budget <= 0 ? (unsigned short)0x7fc0 : (unsigned short)pk_bf16(y, y), ro, col == 0 ? oidx * 2 : OOB, 0, OUTAUX);
        break;
      }
      gate = rbf(silu(t));
    }
    STAMP2(6);
    FLUSH();
    return;
  }

  if (role == 6) {
    unsigned prev = t_start + HEAD;
    lds_wait_ge<8>(ctrl, C_Q, 1u, budget);
    const u64 tag = *(volatile LAS u64*)(ctrl + C_TAG);
    lds_wait_ge<8>(ctrl, C_CLR, 4u, budget);
#pragma nounroll
    for (int k = 0; k < NROUND; ++k) {
      GAS u64* line = (GAS u64*)(ws + WS_TAU) + (k * 8 + kvh) * 32;
      const int at = MARK + k * MSTEP, mark = 4 + ((at * nt) >> 5);
      lds_wait_ge<1>(ctrl, C_LANDED, mark, budget);
      const unsigned tau = (unsigned)__builtin_amdgcn_s_memrealtime();
      if (lane == 0 && (k == 0 || ii == 3)) __hip_atomic_store(line + (k ? xg : gi), (tag << 16) | (tau & 0xffffu), RLX_AGENT);
      lds_wait_ge<4>(ctrl, C_LANDED, mark + MDEL, budget);
      const u64 v = __hip_atomic_load(line + (lane & (k ? 7 : 31)), RLX_AGENT);
      const unsigned seen_t = ((v ^ (tag << 16)) >> 16) == 0 ? (unsigned)v : (unsigned)__builtin_amdgcn_s_memrealtime();
      const float d = wave_sum((float)(short)(seen_t - tau));
      const unsigned mean = tau + __builtin_amdgcn_readfirstlane((int)(d * (1.f / 64)));
      const int rate = (k ? MSTEP : MARK) * 16 * OPT / max((int)(mean - prev), 1);
      prev = mean;
      if (lane == 0) *(volatile LAS v4u*)(ctrl + C_SCHED) = v4u{mean, (unsigned)(at * 16), (unsigned)rate, 0u};
      if (k == 0) {
        while (!__all(((__hip_atomic_load(line + (lane & 31), RLX_AGENT) ^ (tag << 16)) >> 16) == 0) && --budget > 0) __builtin_amdgcn_s_sleep(8);
        if (lane == 0) ctrl[C_FLAG] = 1u;
        if (gi == 0 && lane == 0) __hip_atomic_store((GAS u64*)(ws + WS_GOK) + kvh, tag, RLX_AGENT);
      } else if (k == 1) {
        while (!__all(__hip_atomic_load((GAS u64*)(ws + WS_GOK) + (lane & 7), RLX_AGENT) == tag) && --budget > 0) __builtin_amdgcn_s_sleep(8);
        if (lane == 0) ctrl[C_ALL] = 1u;
        if (bid == 0 && lane == 0) __hip_atomic_store((GAS u64*)(ws + WS_EPOCH), tag, RLX_AGENT);
      }
    }
    return;
  }

  const int c = role - 2;
  const int blk = col >> 2;
  unsigned ka[4], va[2];
#pragma unroll
  for (int k = 0; k < 4; ++k) ka[k] = col * 256 + (((4 * k + g) ^ col) * 16);
#pragma unroll
  for (int n = 0; n < 2; ++n)
    va[n] = (4 * g + q4) * 256 + (((4 * c + 2 * n + (p4 >> 1)) ^ ((4 * (g & 1) + q4) << 1)) * 16) + 8 * (p4 & 1);
  constexpr float SCALE = 0.08838834764831845f, LOG2E = 1.4426950408889634f;
  {
    const int ntp = nt, fw = C_FREED + c;
    {
      lds_wait_ge<4>(ctrl, C_FREED, 1u, budget);
      const v4u sv = {SENT, SENT, SENT, SENT};
      if (c < 3 && !(ABL & 4)) __builtin_amdgcn_raw_buffer_store_b128(sv, rs, (c < 2 ? WS_P1 + wid * 2048 + c * 1024 : WS_P2 + wid * 1024) + lane * 16, 0, CLRAUX);
      if (!PFFLAG && has_ffn && !(ABL & 12)) __builtin_amdgcn_raw_buffer_store_b128(sv, rs, WS_PF + ffn * 4096 + c * 1024 + lane * 16, 0, AUX_SC1);
      wait_vm<0>();
      if (lane == 0) __hip_atomic_fetch_add((LAS unsigned*)(ctrl + C_CLR), 1u, __ATOMIC_RELAXED, __HIP_MEMORY_SCOPE_WORKGROUP);
      lds_wait_ge<2>(ctrl, C_Q, 1u, budget);
    }
    v4u qf[4][4];
#pragma unroll
    for (int k = 0; k < 4; ++k) {
      const v4u qv = *(LAS v4u*)(L + L_Q + ((col & 3) * 128 + 32 * k + 8 * g) * 2);
#pragma unroll
      for (int a = 0; a < 4; ++a) qf[a][k] = blk == a ? qv : v4u{0u, 0u, 0u, 0u};
    }
    float ms = -3.0e38f, lsum = 0.f, linv = 0.f;
    unsigned lam = 0;
    f32x4 acc0 = {0.f, 0.f, 0.f, 0.f}, acc1 = {0.f, 0.f, 0.f, 0.f};
    int slot = SEQ_KV;
    unsigned seen = 0u;
    auto need = [&](unsigned want) {
      if (seen < want) seen = (CSLEEP && want < (unsigned)g_kv - 8) ? lds_wait_ge<CSLEEP>(ctrl, C_LANDED, want, budget) : lds_wait_ge(ctrl, C_LANDED, want, budget);
      asm volatile("" ::: "memory");
    };
#pragma nounroll
    for (int t = 0; t < ntp; ++t) {
      const int seq = SEQ_KV + 2 * t;
      LAS char* sb = L + slot * SLOT;
      slot = slot + 1 == NSLOT ? 0 : slot + 1;
      need(4 * seq + 4);
      v4u kf[4][4];
#pragma unroll
      for (int k = 0; k < 4; ++k)
#pragma unroll
        for (int a = 0; a < 4; ++a) kf[a][k] = ((ABL & 32) && a) ? kf[0][k] : *(LAS v4u*)(sb + ka[k] + ((ABL & 32) ? c : a) * 4096);
      __builtin_amdgcn_sched_barrier(0);
      f32x4 s0 = {0.f, 0.f, 0.f, 0.f}, s1 = {0.f, 0.f, 0.f, 0.f};
#pragma unroll
      for (int k = 0; k < 4; ++k) {
        s0 = mfma32(kf[0][k], qf[0][k], s0); s1 = mfma32(kf[1][k], qf[1][k], s1);
        s0 = mfma32(kf[2][k], qf[2][k], s0); s1 = mfma32(kf[3][k], qf[3][k], s1);
      }
      __builtin_amdgcn_sched_barrier(0);
      wait_lgkm();
      if (lane == 0) ctrl[fw] = seq + 1;
      s0 += s1;
      float s[4];
#pragma unroll
      for (int j = 0; j < 2; ++j) {
        const unsigned a = pk_bf16(s0[2 * j], s0[2 * j + 1]), b = pk_bf16(bf_lo(a) * SCALE, bf_hi(a) * SCALE);
        s[2 * j] = bf_lo(b); s[2 * j + 1] = bf_hi(b);
      }
      float mx = max2(max2(s[0], s[1]), max2(s[2], s[3]));
      mx = max2(mx, dpp<ROR8>(mx)); mx = max2(mx, dpp<ROR4>(mx)); mx = xrow_max(mx);
      const float mn = max2(ms, mx * LOG2E), alpha = __builtin_amdgcn_exp2f(ms - mn);
      ms = mn;
#pragma unroll
      for (int j = 0; j < 4; ++j) s[j] = __builtin_amdgcn_exp2f(fmaf(s[j], LOG2E, -mn));
      lsum = fmaf(lsum, alpha, (s[0] + s[1]) + (s[2] + s[3]));
      const unsigned p0 = pk_bf16(s[0], s[1]), p1 = pk_bf16(s[2], s[3]);
      v4u pA = {blk == 0 ? p0 : 0u, blk == 0 ? p1 : 0u, blk == 1 ? p0 : 0u, blk == 1 ? p1 : 0u};
      v4u pB = {blk == 2 ? p0 : 0u, blk == 2 ? p1 : 0u, blk == 3 ? p0 : 0u, blk == 3 ? p1 : 0u};
      acc0 *= alpha; acc1 *= alpha;
      if (t == ntp - 1) {
        lds_wait_ge(ctrl, C_FLAG, 1u, budget);
        const float lt = quad_stride_sum(lsum);
        linv = __builtin_amdgcn_rcpf(lt);
        lam = __float_as_uint(ms + __builtin_amdgcn_logf(lt));
      }
      asm volatile("" : "+v"(pA), "+v"(pB), "+v"(acc0), "+v"(acc1), "+v"(lsum), "+v"(linv), "+v"(lam));
      sb = L + slot * SLOT;
      slot = slot + 1 == NSLOT ? 0 : slot + 1;
      need(4 * seq + 6);
      acc0 = mfma32(cat(lds_tr(sb + va[0]), lds_tr(sb + va[0] + 4096)), pA, acc0);
      acc1 = mfma32(cat(lds_tr(sb + va[1]), lds_tr(sb + va[1] + 4096)), pA, acc1);
      need(4 * seq + 8);
      v4u v0 = cat(lds_tr(sb + va[0] + 8192), lds_tr(sb + va[0] + 12288)), v1 = cat(lds_tr(sb + va[1] + 8192), lds_tr(sb + va[1] + 12288));
      wait_lgkm();
      if (lane == 0) ctrl[fw] = seq + 2;
      asm volatile("" : "+v"(v0), "+v"(v1) :: "memory");
      acc0 = mfma32(v0, pB, acc0);
      acc1 = mfma32(v1, pB, acc1);
    }

    STAMP(0);

    v4u pk;
#pragma unroll
    for (int j = 0; j < 4; ++j) {
      float a = ((col & 4) ? acc1[j] : acc0[j]) + dpp<ROR4>((col & 4) ? acc0[j] : acc1[j]);
      a += dpp<ROR8>(a);
      pk[j] = ((__float_as_uint(a * linv) + 0x80u) & 0xffffff00u) | ((lam >> (8 * j)) & 0xffu);
    }
    __builtin_amdgcn_raw_buffer_store_b128(not_sent(pk), rs, col < 8 ? WS_P1 + wid * 2048 + c * 512 + g * 128 + col * 16 : OOB, 0, AUX_SC1);
    STAMP(1);
    const int rec = WS_P1 + (kvh * 32 + (c >> 1) * 16 + (lane >> 3)) * 2048 + (gi >> 2) * 256 + (c & 1) * 128 + (lane & 7) * 16;
    v4u a0, a1, wa[4];
    need(g_wo);
    LAS char* wp = L + slot * SLOT;
#pragma unroll
    for (int n = 0; n < 4; ++n) wa[n] = cat(lds_tr(wp + va[n & 1] + (n >> 1) * 4096), lds_tr(wp + va[n & 1] + (n >> 1) * 4096 + 8192));
    __builtin_amdgcn_sched_barrier(0);
    STAMP(6);
    poll_box2<PSLEEP>(rs, rec, rec + 8 * 2048, a0, a1, budget);
    STAMP(2);
    const float lA = __uint_as_float(__builtin_amdgcn_perm(__builtin_amdgcn_perm(a0[3], a0[2], 0x04000c0cu), __builtin_amdgcn_perm(a0[1], a0[0], 0x0c0c0400u), 0x07060100u));
    const float lB = __uint_as_float(__builtin_amdgcn_perm(__builtin_amdgcn_perm(a1[3], a1[2], 0x04000c0cu), __builtin_amdgcn_perm(a1[1], a1[0], 0x0c0c0400u), 0x07060100u));
    float M = max2(lA, lB);
    M = xrow_max(max2(M, dpp<ROR8>(M)));
    const float wA = __builtin_amdgcn_exp2f(lA - M), wB = __builtin_amdgcn_exp2f(lB - M);
    float r[4];
#pragma unroll
    for (int j = 0; j < 4; ++j) r[j] = fmaf(wA, __uint_as_float(a0[j] & 0xffffff00u), wB * __uint_as_float(a1[j] & 0xffffff00u));
    float wsum = xrow_sum(wA + wB), ov = rows_sum4(r[0], r[1], r[2], r[3]);
    wsum += dpp<ROR8>(wsum); ov += dpp<ROR8>(ov);
    ov *= __builtin_amdgcn_rcpf(wsum);
    const auto pr = __builtin_amdgcn_permlane32_swap(__float_as_uint(ov), __float_as_uint(ov), false, false);
    if ((lane & 40) == 0) *(LAS unsigned*)(L + L_O + c * 64 + col * 8 + g * 4) = min(pk_bf16(__uint_as_float(pr[0]), __uint_as_float(pr[1])), SENT - 1);
    v4u o0, o1;
    poll_lds_o(lbase + L_O + ((col >> 1) & 1) * 128 + g * 8, o0, o1, budget);
    const f32x4 zero = {0.f, 0.f, 0.f, 0.f};
    const f32x4 sp0 = mfma32(wa[2], o1, mfma32(wa[0], o0, zero)), sp1 = mfma32(wa[3], o1, mfma32(wa[1], o0, zero));
    const f32x4 sp = (col & 1) ? sp1 : sp0;
    v4u so;
#pragma unroll
    for (int j = 0; j < 4; ++j) {
      const auto sw = __builtin_amdgcn_permlane16_swap(__float_as_uint(sp[j]), __float_as_uint(sp[j]), false, false);
      so[j] = !FULLLINE ? __float_as_uint(sp[j]) : (col & 4) ? sw[1] : sw[0];
    }
    __builtin_amdgcn_raw_buffer_store_b128(not_sent(so), rs, (FULLLINE ? (lane & 24) == 0 : col < 4) ? WS_P2 + wid * 1024 + c * 256 + g * 64 + col * 16 : OOB, 0, AUX_SC1);
    if (lane == 0) ctrl[C_PUB] = 1u;
    STAMP(3);
  }
  FLUSH();
}

extern "C" void kernel_launch(void* const* d_in, const int* in_sizes, int n_in, void* d_out, int out_size, void* d_ws, size_t ws_size,
                              hipStream_t stream) {
  (void)hipFuncSetAttribute((const void*)fused_decode, hipFuncAttributeMaxDynamicSharedMemorySize, L_BYTES);
  hipLaunchKernelGGL(fused_decode, dim3(NWG), dim3(448), L_BYTES, stream, (const char*)d_in[0], (const char*)d_in[1], (const char*)d_in[2],
                     (const char*)d_in[3], (const char*)d_in[4], (const char*)d_in[6], (const char*)d_in[7], (char*)d_out, (char*)d_ws);
}
```

```cpp
#include <hip/hip_runtime.h>
#include <hip/hip_bf16.h>
#include <stdint.h>

#define DBG 0
#define W0 20
#define WI 20
#define WMIN 12
#define WMAX 32
#define KN 1
#define KD 1
#define FF0 32
#define FF1 -16
#define NROUND 4
#define MARK 64
#define MSTEP 48
#define MDEL 16
#define TG16 220
#define LAG0 1
#define LAGHI 4
#define LAGLO 0
#define QDELAY 300
#define QSLEEP 4
#define PFAT 176

#define GAS __attribute__((address_space(1)))
#define LAS __attribute__((address_space(3)))
#define RLX_AGENT __ATOMIC_RELAXED, __HIP_MEMORY_SCOPE_AGENT
typedef unsigned long long u64;
typedef unsigned v4u __attribute__((ext_vector_type(4)));
typedef unsigned v2u __attribute__((ext_vector_type(2)));
typedef float f32x4 __attribute__((ext_vector_type(4)));
typedef float f32x2 __attribute__((ext_vector_type(2)));
typedef __bf16 bf16x8 __attribute__((ext_vector_type(8)));
typedef __bf16 bf16x4 __attribute__((ext_vector_type(4)));
typedef __bf16 bf16x2 __attribute__((ext_vector_type(2)));

constexpr int D = 512, SK = 32768, FF = 1792, NWG = 256, NSLOT = 8, SLOT = 16384;
constexpr unsigned SENT = 0xFFFFFFFFu;
constexpr int AUX_SC1 = 16, OOB = 0x7ffffff0;

constexpr int WS_EPOCH = 0, WS_GOK = 4096, WS_FOK = 4608, WS_TAU = 8192, WS_QG = 32768, WS_P1 = WS_QG + 65536, WS_P2 = WS_P1 + NWG * 2048,
              WS_PF = WS_P2 + NWG * 1024, WS_END = WS_PF + 64 * 4096, WS_DBG = 131072 + 3 * 256 * 4096;

constexpr int L_S = NSLOT * SLOT, L_CTRL = L_S + 33 * 512, L_XN = L_CTRL + 512, L_Q = L_XN + 2048, L_O = L_Q + 1024, L_M = L_O + 256,
              L_W = L_M + 64, L_TR = L_W + 8192, L_BYTES = L_TR + (DBG == 1 ? 256 : 0);
constexpr int C_LANDED = 64, C_ALL = 0, C_WQ = 1, C_Q = 2, C_CLR = 3, C_FREED = 4, C_PUB = 8, C_FLAG = 9, C_TAG = 10, C_SCHED = 12, C_MAX = 16;
static_assert(NROUND >= 2 && WS_TAU + NROUND * 2048 <= WS_QG && MARK + (NROUND - 1) * MSTEP + MDEL <= 248 && L_BYTES <= 163840, "");
constexpr int SPIN_BUDGET = 1 << 20;

__device__ __forceinline__ unsigned pk_bf16(float lo, float hi) {
  return __builtin_bit_cast(unsigned, __builtin_convertvector(f32x2{lo, hi}, bf16x2));
}
__device__ __forceinline__ float bf_lo(unsigned u) { return __uint_as_float(u << 16); }
__device__ __forceinline__ float bf_hi(unsigned u) { return __uint_as_float(u & 0xffff0000u); }
__device__ __forceinline__ float rbf(float f) { return bf_lo(pk_bf16(f, f)); }
template <int CTRL> __device__ __forceinline__ float dpp(float x) {
  return __builtin_bit_cast(float, __builtin_amdgcn_mov_dpp(__builtin_bit_cast(int, x), CTRL, 0xf, 0xf, true));
}
constexpr int XOR1 = 0xB1, XOR2 = 0x4E, ROR4 = 0x124, ROR8 = 0x128;
__device__ __forceinline__ float xrow_sum(float x) {
  auto s = __builtin_amdgcn_permlane16_swap(__float_as_uint(x), __float_as_uint(x), false, false);
  x = __uint_as_float(s[0]) + __uint_as_float(s[1]);
  auto t = __builtin_amdgcn_permlane32_swap(__float_as_uint(x), __float_as_uint(x), false, false);
  return __uint_as_float(t[0]) + __uint_as_float(t[1]);
}
__device__ __forceinline__ float quad_stride_sum(float x) {
  x += dpp<ROR8>(x); x += dpp<ROR4>(x);
  return xrow_sum(x);
}
__device__ __forceinline__ float rows_sum4(float a, float b, float c, float d) {
  const auto s = __builtin_amdgcn_permlane32_swap(__float_as_uint(a), __float_as_uint(b), false, false);
  const auto t = __builtin_amdgcn_permlane32_swap(__float_as_uint(c), __float_as_uint(d), false, false);
  const float ab = __uint_as_float(s[0]) + __uint_as_float(s[1]), cd = __uint_as_float(t[0]) + __uint_as_float(t[1]);
  const auto u = __builtin_amdgcn_permlane16_swap(__float_as_uint(ab), __float_as_uint(cd), false, false);
  return __uint_as_float(u[0]) + __uint_as_float(u[1]);
}
__device__ __forceinline__ float max2(float a, float b) {
  float top = 3.4028235e38f;
  asm("" : "+s"(top));
  return __builtin_amdgcn_fmed3f(a, b, top);
}
__device__ __forceinline__ float xrow_max(float x) {
  const auto s = __builtin_amdgcn_permlane16_swap(__float_as_uint(x), __float_as_uint(x), false, false);
  x = max2(__uint_as_float(s[0]), __uint_as_float(s[1]));
  const auto t = __builtin_amdgcn_permlane32_swap(__float_as_uint(x), __float_as_uint(x), false, false);
  return max2(__uint_as_float(t[0]), __uint_as_float(t[1]));
}
__device__ __forceinline__ float wave_sum(float x) {
  x += dpp<XOR1>(x); x += dpp<XOR2>(x);
  return quad_stride_sum(x);
}
__device__ __forceinline__ float half_sum(float x) {
  x += dpp<XOR1>(x); x += dpp<XOR2>(x); x += dpp<ROR8>(x); x += dpp<ROR4>(x);
  const auto s = __builtin_amdgcn_permlane16_swap(__float_as_uint(x), __float_as_uint(x), false, false);
  return __uint_as_float(s[0]) + __uint_as_float(s[1]);
}
__device__ __forceinline__ f32x4 mfma32(v4u a, v4u b, f32x4 c) {
  return __builtin_amdgcn_mfma_f32_16x16x32_bf16(__builtin_bit_cast(bf16x8, a), __builtin_bit_cast(bf16x8, b), c, 0, 0, 0);
}
__device__ __forceinline__ v2u lds_tr(LAS char* p) {
  return __builtin_bit_cast(v2u, __builtin_amdgcn_ds_read_tr16_b64_v4bf16((LAS bf16x4*)p));
}
__device__ __forceinline__ v4u cat(v2u a, v2u b) { return v4u{a[0], a[1], b[0], b[1]}; }
__device__ __forceinline__ v4u not_sent(v4u u) {
  return v4u{min(u[0], SENT - 1), min(u[1], SENT - 1), min(u[2], SENT - 1), min(u[3], SENT - 1)};
}
__device__ __forceinline__ float silu(float v) { return v / (1.f + __expf(-v)); }

__device__ __forceinline__ void dma1(unsigned voff, const char* sbase, unsigned ldsaddr) {
  asm volatile("s_mov_b32 m0, %2\n\ts_nop 0\n\tglobal_load_lds_dwordx4 %0, %1 nt" :: "v"(voff), "s"(sbase), "s"(ldsaddr) : "memory");
}
__device__ __forceinline__ void dma4(unsigned v0, unsigned v1, unsigned v2, unsigned v3, const char* sbase, unsigned ldsaddr) {
  asm volatile("s_mov_b32 m0, %5\n\ts_nop 0\n\t"
               "global_load_lds_dwordx4 %0, %4 nt\n\tglobal_load_lds_dwordx4 %1, %4 offset:1024 nt\n\t"
               "global_load_lds_dwordx4 %2, %4 offset:2048 nt\n\tglobal_load_lds_dwordx4 %3, %4 offset:3072 nt"
               :: "v"(v0), "v"(v1), "v"(v2), "v"(v3), "s"(sbase), "s"(ldsaddr) : "memory");
}
template <int N> __device__ __forceinline__ void wait_vm() { asm volatile("s_waitcnt vmcnt(%0)" :: "i"(N) : "memory"); }
#define SLOT_GROUP(W, STEP) \
  "s_mov_b32 m0, %[lds]\n\ts_add_u32 %[lds], %[lds], 0x1000\n\t" \
  "global_load_lds_dwordx4 %[v0], vcc nt\n\tglobal_load_lds_dwordx4 %[v1], vcc offset:1024 nt\n\t" \
  "global_load_lds_dwordx4 %[v2], vcc offset:2048 nt\n\tglobal_load_lds_dwordx4 %[v3], vcc offset:3072 nt\n\t" \
  "s_add_u32 vcc_lo, vcc_lo, " STEP "\n\ts_addc_u32 vcc_hi, vcc_hi, 0\n\t" \
  "s_add_i32 %[cand], %[cand], 1\n\ts_max_i32 %[pub], %[pub], %[cand]\n\tv_mov_b32 %[tmp], %[pub]\n\t" \
  "s_waitcnt vmcnt(" #W ")\n\tds_write_b32 %[va], %[tmp]\n\t"
#define SLOT_LEVEL(W) \
  "s_mov_b64 vcc, %[src]\n\t" SLOT_GROUP(W, "0x1000") SLOT_GROUP(W, "%[mid]") SLOT_GROUP(W, "0x1000") SLOT_GROUP(W, "0") "s_branch 9f\n\t"
__device__ __forceinline__ void issue_slot(unsigned v0, unsigned v1, unsigned v2, unsigned v3, const char* src, int mid, unsigned lds,
                                           int w, int cand, int& pub, unsigned va) {
  unsigned tmp;
  int jump;
  asm volatile("s_mul_i32 %[jmp], %[lvl], 8f-7f\n\ts_getpc_b64 vcc\n6:\n\ts_add_u32 %[jmp], %[jmp], 7f-6b\n\t"
               "s_add_u32 vcc_lo, vcc_lo, %[jmp]\n\ts_addc_u32 vcc_hi, vcc_hi, 0\n\ts_setpc_b64 vcc\n7:\n\t"
               SLOT_LEVEL(12) "8:\n\t" SLOT_LEVEL(16) SLOT_LEVEL(20) SLOT_LEVEL(24) SLOT_LEVEL(28) SLOT_LEVEL(32) "9:"
               : [lds] "+&s"(lds), [cand] "+&s"(cand), [pub] "+&s"(pub), [tmp] "=&v"(tmp), [jmp] "=&s"(jump)
               : [v0] "v"(v0), [v1] "v"(v1), [v2] "v"(v2), [v3] "v"(v3), [src] "s"(src), [mid] "s"(mid), [lvl] "s"(w - 3), [va] "v"(va)
               : "vcc", "scc", "memory");
}
__device__ __forceinline__ int opaque(int v) { asm volatile("" : "+s"(v)); return v; }
__device__ __forceinline__ void wait_lgkm() { asm volatile("s_waitcnt lgkmcnt(0)" ::: "memory"); }

typedef volatile LAS unsigned* ctrl_t;
template <int SLEEP = 0> __device__ __forceinline__ unsigned lds_wait_ge(ctrl_t ctrl, int word, unsigned want, int& budget) {
  unsigned v, tmp;
  int go;
  budget = __builtin_amdgcn_readfirstlane(budget);
  want = __builtin_amdgcn_readfirstlane(want);
  asm volatile("1:\n\tds_read_b32 %[t], %[a]\n\t.if %[slp]\n\ts_sleep %[slp]\n\t.endif\n\ts_waitcnt lgkmcnt(0)\n\tv_readfirstlane_b32 %[v], %[t]\n\t"
               "s_cmp_lt_u32 %[v], %[w]\n\ts_cselect_b32 %[g], %[b], 0\n\ts_subb_u32 %[b], %[b], 0\n\ts_cmp_gt_i32 %[g], 0\n\ts_cbranch_scc1 1b"
               : [v] "=&s"(v), [t] "=&v"(tmp), [g] "=&s"(go), [b] "+&s"(budget)
               : [a] "v"((unsigned)(uintptr_t)(ctrl + word)), [w] "s"(want), [slp] "i"(SLEEP) : "scc", "memory");
  return v;
}
#define BOX_LOOP_END \
  "v_cmpx_eq_u32 vcc, -1, %[t]\n\ts_sub_i32 %[b], %[b], 1\n\ts_cmp_gt_i32 %[b], 0\n\ts_cselect_b64 vcc, exec, 0\n\t" \
  "s_cmp_lg_u64 vcc, 0\n\ts_cbranch_scc0 2f\n\t.if %[slp]\n\ts_sleep %[slp]\n\t.endif\n\ts_branch 1b\n2:\n\ts_mov_b64 exec, -1"
template <int SLEEP> __device__ __forceinline__ v4u poll_box(__amdgpu_buffer_rsrc_t rs, int off, int& budget) {
  v4u a;
  unsigned tmp;
  budget = __builtin_amdgcn_readfirstlane(budget);
  asm volatile("1:\n\tbuffer_load_dwordx4 v[100:103], %[o], %[rs], 0 offen sc1\n\ts_waitcnt vmcnt(0)\n\t"
               "v_max_u32 %[t], v100, v101\n\tv_max3_u32 %[t], %[t], v102, v103\n\t" BOX_LOOP_END
               : "=&{v[100:103]}"(a), [t] "=&v"(tmp), [b] "+&s"(budget) : [o] "v"(off), [rs] "s"(rs), [slp] "i"(SLEEP) : "vcc", "scc", "memory");
  return a;
}
template <int SLEEP> __device__ __forceinline__ void poll_box2(__amdgpu_buffer_rsrc_t rs, int off0, int off1, v4u& a, v4u& b, int& budget) {
  unsigned tmp;
  budget = __builtin_amdgcn_readfirstlane(budget);
  asm volatile("1:\n\tbuffer_load_dwordx4 v[100:103], %[o0], %[rs], 0 offen sc1\n\tbuffer_load_dwordx4 v[104:107], %[o1], %[rs], 0 offen sc1\n\t"
               "s_waitcnt vmcnt(0)\n\tv_max_u32 %[t], v100, v101\n\tv_max3_u32 %[t], %[t], v102, v103\n\t"
               "v_max3_u32 %[t], %[t], v104, v105\n\tv_max3_u32 %[t], %[t], v106, v107\n\t" BOX_LOOP_END
               : "=&{v[100:103]}"(a), "=&{v[104:107]}"(b), [t] "=&v"(tmp), [b] "+&s"(budget)
               : [o0] "v"(off0), [o1] "v"(off1), [rs] "s"(rs), [slp] "i"(SLEEP) : "vcc", "scc", "memory");
}
__device__ __forceinline__ void poll_lds_o(unsigned addr, v4u& a, v4u& b, int& budget) {
  unsigned tmp;
  budget = __builtin_amdgcn_readfirstlane(budget);
  asm volatile("1:\n\tds_read_b64 v[100:101], %[o]\n\tds_read_b64 v[102:103], %[o] offset:32\n\tds_read_b64 v[104:105], %[o] offset:64\n\t"
               "ds_read_b64 v[106:107], %[o] offset:96\n\ts_waitcnt lgkmcnt(0)\n\tv_max_u32 %[t], v100, v101\n\tv_max3_u32 %[t], %[t], v102, v103\n\t"
               "v_max3_u32 %[t], %[t], v104, v105\n\tv_max3_u32 %[t], %[t], v106, v107\n\t" BOX_LOOP_END
               : "=&{v[100:103]}"(a), "=&{v[104:107]}"(b), [t] "=&v"(tmp), [b] "+&s"(budget) : [o] "v"(addr), [slp] "i"(0) : "vcc", "scc", "memory");
}

#define STAMP(i) do {} while (0)
#define STAMP2(i) do {} while (0)
#define FLUSH() do {} while (0)

typedef unsigned v16u __attribute__((ext_vector_type(16)));
__device__ __forceinline__ char* as_ptr(unsigned lo, unsigned hi) { return (char*)(((u64)hi << 32) | lo); }

__global__ void __launch_bounds__(448) fused_decode(const char*, const char*, const char*, const char*, const char*, const char*, const char*,
                                                    char*, char*) {
  v16u ka16;
  v2u ka2;
  asm volatile("s_load_dwordx16 s[36:51], %2, 0x0\n\ts_load_dwordx2 s[52:53], %2, 0x40"
               : "={s[36:51]}"(ka16), "={s[52:53]}"(ka2) : "s"(__builtin_amdgcn_kernarg_segment_ptr()));
  __builtin_amdgcn_sched_barrier(0);
  extern __shared__ __attribute__((aligned(16))) char smem[];
  LAS char* L = (LAS char*)smem;
  const unsigned lbase = (unsigned)(uintptr_t)L;
  ctrl_t ctrl = (ctrl_t)(L + L_CTRL);
  const int lane = threadIdx.x & 63, bid = blockIdx.x;
  int wave = __builtin_amdgcn_readfirstlane(threadIdx.x >> 6);
  const int xg = bid & 7, tg = bid >> 3, kvh = tg >> 2, ii = tg & 3, gi = 4 * xg + ii, wid = kvh * 32 + gi, bb = gi >> 4;
  const int nt = ii == 0 ? 33 : ii == 1 ? 31 : 32, tile0 = (xg & 3) * 128 + (ii == 1 ? 33 : 32 * ii);
  const int g_kv = 8 * nt, g_wo = g_kv + 4;
  const int g = lane >> 4, col = lane & 15, q4 = (lane >> 2) & 3, p4 = lane & 3;
  const int ffn = xg * 8 + kvh;
  const bool has_ffn = ii == 1;
  unsigned vk[4], vx = lane * 16, vq = (lane >> 1) * 8192 + (lane & 1) * 16;
#pragma unroll
  for (int j = 0; j < 4; ++j) vk[j] = g * 256 + ((col ^ (4 * j + g)) * 16);
  unsigned kvoff = ((bb * 8 + kvh) * SK + tile0 * 64) * 256, qoff = (kvh * 512 + gi * 16) * 2;
  asm volatile("" : "+v"(vk[0]), "+v"(vk[1]), "+v"(vk[2]), "+v"(vk[3]), "+v"(vx), "+v"(vq));
  asm volatile("" : "+s"(kvoff), "+s"(qoff), "+s"(wave));
  ctrl[lane] = 0u;
  ctrl[C_LANDED + lane] = 0u;
  *(volatile LAS unsigned*)(L + L_O + lane * 4) = SENT;
  asm volatile("s_waitcnt lgkmcnt(0)\n\ts_barrier" : "+{s[36:51]}"(ka16), "+{s[52:53]}"(ka2) :: "memory");
  const char *x = as_ptr(ka16[0], ka16[1]), *kc = as_ptr(ka16[2], ka16[3]), *vc = as_ptr(ka16[4], ka16[5]), *wq = as_ptr(ka16[6], ka16[7]),
             *wo = as_ptr(ka16[8], ka16[9]), *w1 = as_ptr(ka16[10], ka16[11]), *w2 = as_ptr(ka16[12], ka16[13]);
  char *out = as_ptr(ka16[14], ka16[15]), *ws = as_ptr(ka2[0], ka2[1]);
  const __amdgpu_buffer_rsrc_t rs = __builtin_amdgcn_make_buffer_rsrc((void*)ws, 0, WS_END, 0x00020000);
  const unsigned t_start = (unsigned)__builtin_amdgcn_s_memrealtime();
  if (wave == 1) {
    __builtin_amdgcn_s_setprio(3);
    STAMP2(0);
    const u64 pk_ = (u64)(kc + kvoff), pv_ = (u64)(vc + kvoff), pq_ = (u64)(wq + qoff), pq2 = pq_ + 511 * 8192;
    asm volatile("v_writelane_b32 v100, %0, 0\n\tv_writelane_b32 v101, %1, 0\n\tv_writelane_b32 v100, %2, 1\n\tv_writelane_b32 v101, %3, 1\n\t"
                 "v_writelane_b32 v100, %4, 2\n\tv_writelane_b32 v101, %5, 2\n\tv_writelane_b32 v100, %6, 3\n\tv_writelane_b32 v101, %7, 3\n\t"
                 "v_writelane_b32 v100, %8, 4\n\tv_writelane_b32 v101, %9, 4\n\tv_writelane_b32 v100, %10, 5\n\tv_writelane_b32 v101, %11, 5\n\t"
                 "s_mov_b32 m0, %12\n\ts_mov_b64 exec, 63\n\tglobal_load_lds_dword v[100:101], off nt\n\ts_mov_b64 exec, -1"
                 :: "s"((unsigned)pk_), "s"((unsigned)(pk_ >> 32)), "s"((unsigned)pv_), "s"((unsigned)(pv_ >> 32)), "s"(ka16[0]), "s"(ka16[1]),
                    "s"(ka2[0]), "s"(ka2[1]), "s"((unsigned)pq_), "s"((unsigned)(pq_ >> 32)), "s"((unsigned)pq2), "s"((unsigned)(pq2 >> 32)),
                    "s"(lbase + L_W + 4096) : "v100", "v101", "memory");
#pragma unroll
    for (int j = 0; j < 4; ++j) dma4(vk[0], vk[1], vk[2], vk[3], kc + kvoff + j * 4096, lbase + j * 4096);
  }
  if (opaque(wave) == 0) {
    STAMP2(7);
    asm volatile("s_mov_b32 m0, %4\n\ts_nop 0\n\tglobal_load_lds_dwordx4 %0, %1\n\tglobal_load_lds_dwordx4 %0, %1 offset:1024\n\t"
                 "s_mov_b32 m0, %5\n\ts_nop 0\n\tglobal_load_lds_dwordx4 %2, %3 sc1"
                 :: "v"(vx), "s"(x), "v"(WS_EPOCH), "s"(ws), "s"(lbase + L_W), "s"(lbase + L_W + 2048) : "memory");
  }
  if (opaque(wave) == 6) {
#pragma nounroll
    for (int j = 0; j < 16; ++j) dma1(vq, wq + qoff + (size_t)j * (32 * 8192), lbase + L_S + j * 1024);
    wait_vm<0>();
    if (lane == 0) ctrl[C_WQ] = 1u;
  }
  const int role = opaque(wave);
  int budget = SPIN_BUDGET;

  if (__builtin_expect(role == 1, 1)) {
    unsigned vv[2];
#pragma unroll
    for (int j = 0; j < 2; ++j) vv[j] = g * 256 + ((col ^ ((4 * j + g) << 1)) * 16);
    const char* kb = kc + kvoff;
    const char* vb = vc + kvoff;
    const char* wb = wo + (size_t)(kvh * 512 + 32 * (gi >> 3) + 8 * ((gi >> 2) & 1)) * 1024 + ii * 256;
    const unsigned va_pub = lbase + L_CTRL + (C_LANDED + lane) * 4;
    const int w16 = W0 * 16 + (ii == 0 ? FF0 : ii == 1 ? FF1 : 0);
    int issued = 4, pub = 0, wl = WI / 4, dith = 0;
#pragma nounroll
    for (int seq = 1; seq <= 2 * nt; ++seq) {
      {
        const v4u sc = *(volatile LAS v4u*)(ctrl + C_SCHED);
        v4u f = *(volatile LAS v4u*)(ctrl + C_FREED);
        const int el = (int)((unsigned)__builtin_amdgcn_s_memrealtime() - __builtin_amdgcn_readfirstlane(sc[0]));
        const int rate = __builtin_amdgcn_readfirstlane(sc[2]);
        const int lag16 = rate ? ((((int)__builtin_amdgcn_readfirstlane(sc[1]) + ((el * rate) >> 8)) * nt) >> 5) - pub * 16 : (WI - W0) * 16 * KD / (KN ? KN : 1);
        const int wk = w16 + lag16 * KN / KD + dith;
        wl = __builtin_amdgcn_readfirstlane(min(max((wk + 32) >> 6, WMIN / 4), WMAX / 4));
        dith = min(max(wk - wl * 64, -32), 32);
        if ((int)__builtin_amdgcn_readfirstlane(min(min(f[0], f[1]), min(f[2], f[3]))) <= seq - NSLOT) {
          wait_vm<0>(); pub = issued; ctrl[C_LANDED + lane] = pub;
          do {
            __builtin_amdgcn_s_sleep(1);
            f = *(volatile LAS v4u*)(ctrl + C_FREED);
          } while ((int)__builtin_amdgcn_readfirstlane(min(min(f[0], f[1]), min(f[2], f[3]))) <= seq - NSLOT && --budget > 0);
        }
      }
      const bool is_wo = seq == 2 * nt, is_k = seq < nt;
      unsigned v[4];
#pragma unroll
      for (int j = 0; j < 4; ++j) v[j] = is_k ? vk[j] : is_wo ? vv[j & 1] + g * 768 + j * (127 * 1024) : vv[j & 1];
      const char* src = is_wo ? wb : is_k ? kb + seq * SLOT : vb + (seq - nt) * SLOT;
      issue_slot(v[0], v[1], v[2], v[3], src, is_wo ? 12288 : 4096, lbase + (seq & (NSLOT - 1)) * SLOT, wl, issued - wl, pub, va_pub);
      issued += 4;
    }
#define DRAIN_TO(N) { wait_vm<4 * (N)>(); pub = max(pub, issued - (N)); ctrl[C_LANDED + lane] = pub; }
    DRAIN_TO(7) DRAIN_TO(6) DRAIN_TO(5) DRAIN_TO(4) DRAIN_TO(0)
#undef DRAIN_TO
    STAMP(2);
    FLUSH();
    return;
  }

  if (role == 0) {
    u64 tag;
    v4u xb[16];
    {
      wait_vm<0>();
      STAMP(4);
      tag = *(volatile LAS u64*)(L + L_W + 2048) + 1;
      const v4u xv0 = *(volatile LAS v4u*)(L + L_W + lane * 32), xv1 = *(volatile LAS v4u*)(L + L_W + lane * 32 + 16);
      float v[16];
#pragma unroll
      for (int e = 0; e < 4; ++e) { v[2 * e] = bf_lo(xv0[e]); v[2 * e + 1] = bf_hi(xv0[e]); v[8 + 2 * e] = bf_lo(xv1[e]); v[9 + 2 * e] = bf_hi(xv1[e]); }
      float s4[4], q4s[4];
#pragma unroll
      for (int e = 0; e < 4; ++e) s4[e] = (v[e] + v[4 + e]) + (v[8 + e] + v[12 + e]);
      const float mean = half_sum((s4[0] + s4[1]) + (s4[2] + s4[3])) * (1.f / D);
#pragma unroll
      for (int e = 0; e < 4; ++e) {
        const float d0 = v[e] - mean, d1 = v[4 + e] - mean, d2 = v[8 + e] - mean, d3 = v[12 + e] - mean;
        q4s[e] = fmaf(d0, d0, d1 * d1) + fmaf(d2, d2, d3 * d3);
      }
      const float var = half_sum((q4s[0] + q4s[1]) + (q4s[2] + q4s[3])) * (1.f / D);
      const float mb = rbf(mean), den = rbf(__builtin_amdgcn_sqrtf(rbf(rbf(var) + 1e-5f))), rden = __builtin_amdgcn_rcpf(den);
      unsigned o[8];
#pragma unroll
      for (int e = 0; e < 8; ++e) {
        const unsigned dp = pk_bf16(v[2 * e] - mb, v[2 * e + 1] - mb);
        const float d0 = bf_lo(dp), d1 = bf_hi(dp), q0 = d0 * rden, q1 = d1 * rden;
        o[e] = pk_bf16(fmaf(fmaf(-q0, den, d0), rden, q0), fmaf(fmaf(-q1, den, d1), rden, q1));
      }
      *(LAS v4u*)(L + L_XN + lane * 32) = v4u{o[0], o[1], o[2], o[3]};
      *(LAS v4u*)(L + L_XN + lane * 32 + 16) = v4u{o[4], o[5], o[6], o[7]};
      asm volatile("" ::: "memory");
      LAS char* xa = L + L_XN + (((col >> 1) & 1) * 512 + 4 * g) * 2;
#pragma unroll
      for (int ks = 0; ks < 16; ++ks) xb[ks] = cat(*(LAS v2u*)(xa + ks * 64), *(LAS v2u*)(xa + ks * 64 + 32));
      __builtin_amdgcn_sched_barrier(0);
      STAMP(0);
      lds_wait_ge(ctrl, C_WQ, 1u, budget);
      STAMP(1);
      f32x4 qp[2] = {{0.f, 0.f, 0.f, 0.f}, {0.f, 0.f, 0.f, 0.f}};
      LAS char* wa = L + L_S + 8 * lane;
      v4u fa[16];
#pragma unroll
      for (int ks = 0; ks < 16; ++ks) fa[ks] = cat(lds_tr(wa + ks * 1024), lds_tr(wa + ks * 1024 + 512));
      __builtin_amdgcn_sched_barrier(0);
#pragma unroll
      for (int ks = 0; ks < 16; ++ks) qp[ks & 1] = mfma32(fa[ks], xb[ks], qp[ks & 1]);
      const f32x4 qa = qp[0] + qp[1];
      if ((col & 13) == 0) *(LAS v2u*)(L + L_Q + (col >> 1) * 32 + g * 8) = v2u{pk_bf16(qa[0], qa[1]), pk_bf16(qa[2], qa[3])};
      asm volatile("" ::: "memory");
      const unsigned thi = (unsigned)(tag >> 16), tlo = (unsigned)tag << 16;
      const unsigned qv = *(LAS unsigned short*)(L + L_Q + (lane & 31) * 2);
      __builtin_amdgcn_raw_buffer_store_b64(v2u{tlo | qv, thi}, rs, lane < 32 ? WS_QG + ((kvh * 2 + g) * 512 + gi * 16 + col) * 8 : OOB, 0, AUX_SC1);
      if (lane == 0) *(volatile LAS u64*)(ctrl + C_TAG) = tag;
      STAMP(2);
    }
    {
      const unsigned thi = (unsigned)(tag >> 16), tlo = (unsigned)tag << 16;
      while ((int)((unsigned)__builtin_amdgcn_s_memrealtime() - t_start) < QDELAY) __builtin_amdgcn_s_sleep(2);
      const int qo = WS_QG + (kvh * 2 + bb) * 4096 + lane * 8;
      v2u a[8];
#pragma unroll
      for (int k = 0; k < 8; ++k) a[k] = v2u{~tlo, ~thi};
      for (;;) {
        asm volatile("" ::: "memory");
        bool all = true;
        v2u nv[8];
#pragma unroll
        for (int k = 0; k < 8; ++k) {
          const bool ok = a[k][1] == thi && ((a[k][0] ^ tlo) >> 16) == 0;
          nv[k] = __builtin_amdgcn_raw_buffer_load_b64(rs, ok ? OOB : qo + 512 * k, 0, AUX_SC1);
        }
#pragma unroll
        for (int k = 0; k < 8; ++k) {
          const bool ok = a[k][1] == thi && ((a[k][0] ^ tlo) >> 16) == 0;
          a[k] = ok ? a[k] : nv[k];
          all = all && a[k][1] == thi && ((a[k][0] ^ tlo) >> 16) == 0;
        }
        if (__all(all)) break;
        if (--budget <= 0) break;
        if (QSLEEP) __builtin_amdgcn_s_sleep(QSLEEP);
      }
#pragma unroll
      for (int k = 0; k < 8; ++k) *(LAS unsigned short*)(L + L_Q + (64 * k + lane) * 2) = (unsigned short)a[k][0];
      wait_lgkm();
      if (lane == 0) ctrl[C_Q] = 1u;
    }
    STAMP(3);
    auto ffn_slice = [&]() {
      const int u0 = (28 * ffn) & ~7, lo = 28 * ffn - u0;
      const unsigned vo1 = (lane >> 2) * (FF * 2) + ((p4 ^ ((lane >> 3) & 2)) * 16);
      LAS char* wa = L + L_W + (4 * g + q4) * 64 + 8 * p4;
      f32x4 h0 = {0.f, 0.f, 0.f, 0.f}, h1 = h0;
#pragma unroll
      for (int rd = 0; rd < 4; ++rd) {
#pragma unroll
        for (int j = 0; j < 8; ++j) dma1(vo1, w1 + u0 * 2 + (size_t)(rd * 128 + j * 16) * (FF * 2), lbase + L_W + j * 1024);
        wait_vm<0>();
        v4u fa[4][2];
#pragma unroll
        for (int ks = 0; ks < 4; ++ks)
#pragma unroll
          for (int nb = 0; nb < 2; ++nb) {
            LAS char* p = wa + ks * 2048 + ((nb ^ (g & 1)) * 32);
            fa[ks][nb] = cat(lds_tr(p), lds_tr(p + 1024));
          }
        __builtin_amdgcn_sched_barrier(0);
        wait_lgkm();
#pragma unroll
        for (int ks = 0; ks < 4; ++ks) { h0 = mfma32(fa[ks][0], xb[4 * rd + ks], h0); h1 = mfma32(fa[ks][1], xb[4 * rd + ks], h1); }
      }
      float hv[8];
#pragma unroll
      for (int j = 0; j < 8; ++j) {
        const int u = (j >> 2) * 16 + 4 * g + (j & 3);
        hv[j] = (u >= lo && u < lo + 28) ? silu(rbf(j < 4 ? h0[j & 3] : h1[j & 3])) : 0.f;
      }
      const v4u hb = {pk_bf16(hv[0], hv[1]), pk_bf16(hv[2], hv[3]), pk_bf16(hv[4], hv[5]), pk_bf16(hv[6], hv[7])};
      const unsigned vo2[2] = {(unsigned)(g * 1024 + ((col ^ (g << 1)) * 16)), (unsigned)(g * 1024 + ((col ^ ((4 + g) << 1)) * 16))};
      const int sw = (4 * (g & 1) + q4) << 1;
      LAS char* w2a = L + L_W + (4 * g + q4) * 256 + 8 * (p4 & 1);
      const int mine = 2 * (col >> 2) + (col & 1);
#pragma unroll
      for (int cb = 0; cb < 4; ++cb) {
#pragma unroll
        for (int j = 0; j < 8; ++j) dma1(vo2[j & 1], w2 + (size_t)(u0 + 4 * j) * 1024 + cb * 256, lbase + L_W + j * 1024);
        wait_vm<0>();
        v4u wf[8];
#pragma unroll
        for (int nb = 0; nb < 8; ++nb) {
          LAS char* p = w2a + (((2 * nb + (p4 >> 1)) ^ sw) * 16);
          wf[nb] = cat(lds_tr(p), lds_tr(p + 4096));
        }
        __builtin_amdgcn_sched_barrier(0);
        wait_lgkm();
        f32x4 fo = {0.f, 0.f, 0.f, 0.f};
#pragma unroll
        for (int nb = 0; nb < 8; ++nb) fo = mfma32(wf[nb], mine == nb ? hb : v4u{0u, 0u, 0u, 0u}, fo);
        __builtin_amdgcn_raw_buffer_store_b128(not_sent(__builtin_bit_cast(v4u, fo)), rs, WS_PF + ffn * 4096 + cb * 1024 + lane * 16, 0, AUX_SC1);
      }
    };
    if (has_ffn) {
      ffn_slice();
      wait_vm<0>();
      if (lane == 0) __hip_atomic_store((GAS u64*)(ws + WS_FOK) + ffn, tag, RLX_AGENT);
    }

    lds_wait_ge<8>(ctrl, C_ALL, 1u, budget);
    const int cb = xg >> 1, pc = (xg & 1) * 32 + tg, cw = pc >> 4, gw = (pc >> 2) & 3;
    const int jj = (g >> 1) | ((g & 1) << 1), oidx = ((pc >> 1) & 1) * 512 + cb * 128 + cw * 32 + (pc & 1) * 16 + gw * 4 + jj;
    const float xin = __uint_as_float((unsigned)*(const GAS unsigned short*)(x + oidx * 2) << 16);
    float gate = 0.f;
    {
      const unsigned t0 = *(const GAS unsigned short*)(out + oidx * 2), t1 = *(const GAS unsigned short*)(wo + (size_t)(kvh * 512 + g * 128 + 4 * gi) * 1024);
      asm volatile("" :: "v"(t0), "v"(t1));
    }
#pragma nounroll
    for (int pass = 0; pass < 2; ++pass) {
      const bool last = opaque(pass) != 0;
      if (last) { lds_wait_ge<8>(ctrl, C_LANDED, g_kv - 4, budget); lds_wait_ge(ctrl, C_PUB, 1u, budget); }
      else {
        lds_wait_ge<8>(ctrl, C_LANDED, PFAT, budget);
        while (!__all(__hip_atomic_load((GAS u64*)(ws + WS_FOK) + lane, RLX_AGENT) == tag) && --budget > 0) __builtin_amdgcn_s_sleep(8);
      }
      const int po = (last ? WS_P2 + pc * 16 : WS_PF + gw * 256 + cw * 64 + (pc & 3) * 16) + lane * 4096 + cb * 1024;
      const v4u sv = poll_box<0>(rs, po, budget);
      STAMP(5);
      float t = rows_sum4(__uint_as_float(sv[0]), __uint_as_float(sv[1]), __uint_as_float(sv[2]), __uint_as_float(sv[3]));
      t += dpp<XOR1>(t); t += dpp<XOR2>(t); t += dpp<ROR8>(t); t += dpp<ROR4>(t);
      t = rbf(t);
      if (last) {
        const float y = xin + rbf(t * gate);
        const __amdgpu_buffer_rsrc_t ro = __builtin_amdgcn_make_buffer_rsrc((void*)out, 0, 2 * D * 2, 0x00020000);
        __builtin_amdgcn_raw_buffer_store_b16(budget <= 0 ? (unsigned short)0x7fc0 : (unsigned short)pk_bf16(y, y), ro, col == 0 ? oidx * 2 : OOB, 0, 0);
        break;
      }
      gate = rbf(silu(t));
    }
    STAMP2(6);
    FLUSH();
    return;
  }

  if (role == 6) {
    unsigned at_t = 0;
    int at_p = 0;
    lds_wait_ge<8>(ctrl, C_Q, 1u, budget);
    const u64 tag = *(volatile LAS u64*)(ctrl + C_TAG);
    lds_wait_ge<8>(ctrl, C_CLR, 4u, budget);
#pragma nounroll
    for (int k = 0; k < NROUND; ++k) {
      GAS u64* line = (GAS u64*)(ws + WS_TAU) + (k * 8 + kvh) * 32;
      const int at = MARK + k * MSTEP, mark = (at * nt) >> 5;
      lds_wait_ge<1>(ctrl, C_LANDED, mark, budget);
      const unsigned tau = (unsigned)__builtin_amdgcn_s_memrealtime();
      if (lane == 0 && (k == 0 || ii == 3)) __hip_atomic_store(line + (k ? xg : gi), (tag << 16) | (tau & 0xffffu), RLX_AGENT);
      lds_wait_ge<4>(ctrl, C_LANDED, mark + MDEL, budget);
      const u64 v = __hip_atomic_load(line + (lane & (k ? 7 : 31)), RLX_AGENT);
      const unsigned seen_t = ((v ^ (tag << 16)) >> 16) == 0 ? (unsigned)v : (unsigned)__builtin_amdgcn_s_memrealtime();
      const float d = wave_sum((float)(short)(seen_t - tau));
      const unsigned mean = tau + __builtin_amdgcn_readfirstlane((int)(d * (1.f / 64)));
      constexpr int rate = 65536 / TG16;
      const int common = k ? at_p + (((int)(mean - at_t) * rate) >> 8) - at * 16 : LAG0 * 16;
      at_t = mean;
      at_p = at * 16 + min(max(common, -LAGLO * 16), LAGHI * 16);
      if (lane == 0) *(volatile LAS v4u*)(ctrl + C_SCHED) = v4u{at_t, (unsigned)at_p, (unsigned)rate, 0u};
      if (k == 0) {
        while (!__all(((__hip_atomic_load(line + (lane & 31), RLX_AGENT) ^ (tag << 16)) >> 16) == 0) && --budget > 0) __builtin_amdgcn_s_sleep(8);
        if (lane == 0) ctrl[C_FLAG] = 1u;
        if (gi == 0 && lane == 0) __hip_atomic_store((GAS u64*)(ws + WS_GOK) + kvh, tag, RLX_AGENT);
      } else if (k == 1) {
        while (!__all(__hip_atomic_load((GAS u64*)(ws + WS_GOK) + (lane & 7), RLX_AGENT) == tag) && --budget > 0) __builtin_amdgcn_s_sleep(8);
        if (lane == 0) ctrl[C_ALL] = 1u;
        if (bid == 0 && lane == 0) __hip_atomic_store((GAS u64*)(ws + WS_EPOCH), tag, RLX_AGENT);
      }
    }
    return;
  }

  const int c = role - 2;
  const int blk = col >> 2;
  constexpr float SCALE = 0.08838834764831845f, LOG2E = 1.4426950408889634f;
  unsigned seen = 0u;
  auto need = [&](unsigned want) {
    if (seen < want) seen = lds_wait_ge(ctrl, C_LANDED, want, budget);
    asm volatile("" ::: "memory");
  };
  float ms;
  {
    unsigned ka[4];
#pragma unroll
    for (int k = 0; k < 4; ++k) ka[k] = col * 256 + (((4 * k + g) ^ col) * 16);
    v4u kf[4][4];
    auto fetch_k = [&](int t) {
      need(4 * t + 4);
      LAS char* sb = L + (t & (NSLOT - 1)) * SLOT;
#pragma unroll
      for (int k = 0; k < 4; ++k)
#pragma unroll
        for (int a = 0; a < 4; ++a) kf[a][k] = *(LAS v4u*)(sb + ka[k] + a * 4096);
      __builtin_amdgcn_sched_barrier(0);
      wait_lgkm();
      if (lane == 0) ctrl[C_FREED + c] = min(t + 4, nt);
      __builtin_amdgcn_sched_barrier(0);
    };
    fetch_k(c);
    STAMP(7);
    const v4u sv = {SENT, SENT, SENT, SENT};
    if (c < 3) __builtin_amdgcn_raw_buffer_store_b128(sv, rs, (c < 2 ? WS_P1 + wid * 2048 + c * 1024 : WS_P2 + wid * 1024) + lane * 16, 0, AUX_SC1);
    wait_vm<0>();
    if (lane == 0) __hip_atomic_fetch_add((LAS unsigned*)(ctrl + C_CLR), 1u, __ATOMIC_RELAXED, __HIP_MEMORY_SCOPE_WORKGROUP);
    lds_wait_ge<2>(ctrl, C_Q, 1u, budget);
    v4u qf[4][4];
#pragma unroll
    for (int k = 0; k < 4; ++k) {
      const v4u qv = *(LAS v4u*)(L + L_Q + ((col & 3) * 128 + 32 * k + 8 * g) * 2);
#pragma unroll
      for (int a = 0; a < 4; ++a) qf[a][k] = blk == a ? qv : v4u{0u, 0u, 0u, 0u};
    }
    float mx = -3.0e38f;
#pragma nounroll
    for (int t = c;; t += 4) {
      f32x4 s0 = {0.f, 0.f, 0.f, 0.f}, s1 = {0.f, 0.f, 0.f, 0.f};
#pragma unroll
      for (int k = 0; k < 4; ++k) {
        s0 = mfma32(kf[0][k], qf[0][k], s0); s1 = mfma32(kf[1][k], qf[1][k], s1);
        s0 = mfma32(kf[2][k], qf[2][k], s0); s1 = mfma32(kf[3][k], qf[3][k], s1);
      }
      s0 += s1;
      const unsigned a0 = pk_bf16(s0[0], s0[1]), a1 = pk_bf16(s0[2], s0[3]);
      const unsigned b0 = pk_bf16(bf_lo(a0) * SCALE, bf_hi(a0) * SCALE), b1 = pk_bf16(bf_lo(a1) * SCALE, bf_hi(a1) * SCALE);
      *(LAS v2u*)(L + L_S + t * 512 + lane * 8) = v2u{b0, b1};
      mx = max2(max2(mx, max2(bf_lo(b0), bf_hi(b0))), max2(bf_lo(b1), bf_hi(b1)));
      if (t + 4 >= nt) break;
      fetch_k(t + 4);
    }
    mx = max2(mx, dpp<ROR8>(mx)); mx = max2(mx, dpp<ROR4>(mx)); mx = xrow_max(mx);
    if (lane < 4) *(LAS float*)(L + L_M + lane * 16 + c * 4) = mx;
    wait_lgkm();
    if (lane == 0) __hip_atomic_fetch_add((LAS unsigned*)(ctrl + C_MAX), 1u, __ATOMIC_RELAXED, __HIP_MEMORY_SCOPE_WORKGROUP);
    lds_wait_ge(ctrl, C_MAX, 4u, budget);
    const f32x4 m4 = *(LAS f32x4*)(L + L_M + (col & 3) * 16);
    ms = max2(max2(m4[0], m4[1]), max2(m4[2], m4[3])) * LOG2E;
  }
  unsigned va[2];
#pragma unroll
  for (int n = 0; n < 2; ++n)
    va[n] = (4 * g + q4) * 256 + (((4 * c + 2 * n + (p4 >> 1)) ^ ((4 * (g & 1) + q4) << 1)) * 16) + 8 * (p4 & 1);
  float lsum = 0.f, linv = 0.f;
  unsigned lam = 0;
  f32x4 acc0 = {0.f, 0.f, 0.f, 0.f}, acc1 = {0.f, 0.f, 0.f, 0.f};
#pragma nounroll
  for (int t = 0; t < nt; ++t) {
    const int seq = nt + t;
    const v2u sc = *(LAS v2u*)(L + L_S + t * 512 + lane * 8);
    const float e0 = __builtin_amdgcn_exp2f(fmaf(bf_lo(sc[0]), LOG2E, -ms)), e1 = __builtin_amdgcn_exp2f(fmaf(bf_hi(sc[0]), LOG2E, -ms));
    const float e2 = __builtin_amdgcn_exp2f(fmaf(bf_lo(sc[1]), LOG2E, -ms)), e3 = __builtin_amdgcn_exp2f(fmaf(bf_hi(sc[1]), LOG2E, -ms));
    lsum += (e0 + e1) + (e2 + e3);
    const unsigned p0 = pk_bf16(e0, e1), p1 = pk_bf16(e2, e3);
    v4u pA = {blk == 0 ? p0 : 0u, blk == 0 ? p1 : 0u, blk == 1 ? p0 : 0u, blk == 1 ? p1 : 0u};
    v4u pB = {blk == 2 ? p0 : 0u, blk == 2 ? p1 : 0u, blk == 3 ? p0 : 0u, blk == 3 ? p1 : 0u};
    if (t == nt - 1) {
      lds_wait_ge(ctrl, C_FLAG, 1u, budget);
      const float lt = quad_stride_sum(lsum);
      linv = __builtin_amdgcn_rcpf(lt);
      lam = __float_as_uint(ms + __builtin_amdgcn_logf(lt));
    }
    asm volatile("" : "+v"(pA), "+v"(pB), "+v"(lsum), "+v"(linv), "+v"(lam));
    LAS char* sb = L + (seq & (NSLOT - 1)) * SLOT;
    need(4 * seq + 2);
    acc0 = mfma32(cat(lds_tr(sb + va[0]), lds_tr(sb + va[0] + 4096)), pA, acc0);
    acc1 = mfma32(cat(lds_tr(sb + va[1]), lds_tr(sb + va[1] + 4096)), pA, acc1);
    need(4 * seq + 4);
    v4u v0 = cat(lds_tr(sb + va[0] + 8192), lds_tr(sb + va[0] + 12288)), v1 = cat(lds_tr(sb + va[1] + 8192), lds_tr(sb + va[1] + 12288));
    wait_lgkm();
    if (lane == 0) ctrl[C_FREED + c] = seq + 1;
    asm volatile("" : "+v"(v0), "+v"(v1) :: "memory");
    acc0 = mfma32(v0, pB, acc0);
    acc1 = mfma32(v1, pB, acc1);
  }
  STAMP(0);

  v4u pk;
#pragma unroll
  for (int j = 0; j < 4; ++j) {
    float a = ((col & 4) ? acc1[j] : acc0[j]) + dpp<ROR4>((col & 4) ? acc0[j] : acc1[j]);
    a += dpp<ROR8>(a);
    pk[j] = ((__float_as_uint(a * linv) + 0x80u) & 0xffffff00u) | ((lam >> (8 * j)) & 0xffu);
  }
  __builtin_amdgcn_raw_buffer_store_b128(not_sent(pk), rs, col < 8 ? WS_P1 + wid * 2048 + c * 512 + g * 128 + col * 16 : OOB, 0, AUX_SC1);
  STAMP(1);
  const int rec = WS_P1 + (kvh * 32 + (c >> 1) * 16 + (lane >> 3)) * 2048 + (gi >> 2) * 256 + (c & 1) * 128 + (lane & 7) * 16;
  v4u a0, a1, wa[4];
  need(g_wo);
  LAS char* wp = L + ((2 * nt) & (NSLOT - 1)) * SLOT;
#pragma unroll
  for (int n = 0; n < 4; ++n) wa[n] = cat(lds_tr(wp + va[n & 1] + (n >> 1) * 4096), lds_tr(wp + va[n & 1] + (n >> 1) * 4096 + 8192));
  __builtin_amdgcn_sched_barrier(0);
  STAMP(6);
  poll_box2<0>(rs, rec, rec + 8 * 2048, a0, a1, budget);
  STAMP(2);
  const float lA = __uint_as_float(__builtin_amdgcn_perm(__builtin_amdgcn_perm(a0[3], a0[2], 0x04000c0cu), __builtin_amdgcn_perm(a0[1], a0[0], 0x0c0c0400u), 0x07060100u));
  const float lB = __uint_as_float(__builtin_amdgcn_perm(__builtin_amdgcn_perm(a1[3], a1[2], 0x04000c0cu), __builtin_amdgcn_perm(a1[1], a1[0], 0x0c0c0400u), 0x07060100u));
  float M = max2(lA, lB);
  M = xrow_max(max2(M, dpp<ROR8>(M)));
  const float wA = __builtin_amdgcn_exp2f(lA - M), wB = __builtin_amdgcn_exp2f(lB - M);
  float r[4];
#pragma unroll
  for (int j = 0; j < 4; ++j) r[j] = fmaf(wA, __uint_as_float(a0[j] & 0xffffff00u), wB * __uint_as_float(a1[j] & 0xffffff00u));
  float wsum = xrow_sum(wA + wB), ov = rows_sum4(r[0], r[1], r[2], r[3]);
  wsum += dpp<ROR8>(wsum); ov += dpp<ROR8>(ov);
  ov *= __builtin_amdgcn_rcpf(wsum);
  const auto pr = __builtin_amdgcn_permlane32_swap(__float_as_uint(ov), __float_as_uint(ov), false, false);
  if ((lane & 40) == 0) *(LAS unsigned*)(L + L_O + c * 64 + col * 8 + g * 4) = min(pk_bf16(__uint_as_float(pr[0]), __uint_as_float(pr[1])), SENT - 1);
  v4u o0, o1;
  poll_lds_o(lbase + L_O + ((col >> 1) & 1) * 128 + g * 8, o0, o1, budget);
  const f32x4 zero = {0.f, 0.f, 0.f, 0.f};
  const f32x4 sp0 = mfma32(wa[2], o1, mfma32(wa[0], o0, zero)), sp1 = mfma32(wa[3], o1, mfma32(wa[1], o0, zero));
  const f32x4 sp = (col & 1) ? sp1 : sp0;
  __builtin_amdgcn_raw_buffer_store_b128(not_sent(__builtin_bit_cast(v4u, sp)), rs, col < 4 ? WS_P2 + wid * 1024 + c * 256 + g * 64 + col * 16 : OOB, 0, AUX_SC1);
  if (lane == 0) ctrl[C_PUB] = 1u;
  STAMP(3);
  FLUSH();
}

extern "C" void kernel_launch(void* const* d_in, const int* in_sizes, int n_in, void* d_out, int out_size, void* d_ws, size_t ws_size,
                              hipStream_t stream) {
  (void)hipFuncSetAttribute((const void*)fused_decode, hipFuncAttributeMaxDynamicSharedMemorySize, L_BYTES);
  hipLaunchKernelGGL(fused_decode, dim3(NWG), dim3(448), L_BYTES, stream, (const char*)d_in[0], (const char*)d_in[1], (const char*)d_in[2],
                     (const char*)d_in[3], (const char*)d_in[4], (const char*)d_in[6], (const char*)d_in[7], (char*)d_out, (char*)d_ws);
}
```

```cpp
#include <hip/hip_runtime.h>
#include <hip/hip_bf16.h>
#include <stdint.h>

#define GAS __attribute__((address_space(1)))
#define LAS __attribute__((address_space(3)))
#define RLX_AGENT __ATOMIC_RELAXED, __HIP_MEMORY_SCOPE_AGENT
typedef unsigned long long u64;
typedef unsigned v4u __attribute__((ext_vector_type(4)));
typedef unsigned v2u __attribute__((ext_vector_type(2)));
typedef float f32x4 __attribute__((ext_vector_type(4)));
typedef float f32x2 __attribute__((ext_vector_type(2)));
typedef __bf16 bf16x8 __attribute__((ext_vector_type(8)));
typedef __bf16 bf16x4 __attribute__((ext_vector_type(4)));
typedef __bf16 bf16x2 __attribute__((ext_vector_type(2)));

constexpr int W0 = 16, WMIN = 12, WMAX = 32;
constexpr int KN = 3, KD = 2;
constexpr int FF0 = 32, FF1 = -16;
constexpr int TG16 = 218;
constexpr int NROUND = 4, MARK = 64, MSTEP = 48, MDEL = 16;
constexpr int LAG0 = 2, LAGHI = 3;
constexpr int QDELAY = 370, PFAT = 176;

constexpr int D = 512, SK = 32768, FF = 1792, NWG = 256, NSLOT = 8, SLOT = 16384;
constexpr unsigned SENT = 0xFFFFFFFFu;
constexpr int AUX_SC1 = 16, OOB = 0x7ffffff0;

constexpr int WS_EPOCH = 0, WS_GOK = 4096, WS_FOK = 4608, WS_TAU = 8192, WS_QG = 32768, WS_P1 = WS_QG + 65536, WS_P2 = WS_P1 + NWG * 2048,
              WS_PF = WS_P2 + NWG * 1024, WS_END = WS_PF + 64 * 4096;

constexpr int L_S = NSLOT * SLOT, L_CTRL = L_S + 33 * 512, L_XN = L_CTRL + 512, L_Q = L_XN + 2048, L_O = L_Q + 1024, L_M = L_O + 256,
              L_NIL = L_M + 64, L_W = L_NIL + 256, L_BYTES = L_W + 8192;
constexpr int C_LANDED = 64, C_ALL = 0, C_WQ = 1, C_Q = 2, C_CLR = 3, C_FREED = 4, C_PUB = 8, C_FLAG = 9, C_TAG = 10, C_SCHED = 12, C_MAX = 16;
static_assert(NROUND >= 2 && WS_TAU + NROUND * 2048 <= WS_QG && MARK + (NROUND - 1) * MSTEP + MDEL <= 248 && L_BYTES <= 163840, "");
constexpr int SPIN_BUDGET = 1 << 20;

__device__ __forceinline__ unsigned pk_bf16(float lo, float hi) {
  return __builtin_bit_cast(unsigned, __builtin_convertvector(f32x2{lo, hi}, bf16x2));
}
__device__ __forceinline__ float bf_lo(unsigned u) { return __uint_as_float(u << 16); }
__device__ __forceinline__ float bf_hi(unsigned u) { return __uint_as_float(u & 0xffff0000u); }
__device__ __forceinline__ float rbf(float f) { return bf_lo(pk_bf16(f, f)); }
template <int CTRL> __device__ __forceinline__ float dpp(float x) {
  return __builtin_bit_cast(float, __builtin_amdgcn_mov_dpp(__builtin_bit_cast(int, x), CTRL, 0xf, 0xf, true));
}
constexpr int XOR1 = 0xB1, XOR2 = 0x4E, ROR4 = 0x124, ROR8 = 0x128;
__device__ __forceinline__ float xrow_sum(float x) {
  auto s = __builtin_amdgcn_permlane16_swap(__float_as_uint(x), __float_as_uint(x), false, false);
  x = __uint_as_float(s[0]) + __uint_as_float(s[1]);
  auto t = __builtin_amdgcn_permlane32_swap(__float_as_uint(x), __float_as_uint(x), false, false);
  return __uint_as_float(t[0]) + __uint_as_float(t[1]);
}
__device__ __forceinline__ float quad_stride_sum(float x) {
  x += dpp<ROR8>(x); x += dpp<ROR4>(x);
  return xrow_sum(x);
}
__device__ __forceinline__ float rows_sum4(float a, float b, float c, float d) {
  const auto s = __builtin_amdgcn_permlane32_swap(__float_as_uint(a), __float_as_uint(b), false, false);
  const auto t = __builtin_amdgcn_permlane32_swap(__float_as_uint(c), __float_as_uint(d), false, false);
  const float ab = __uint_as_float(s[0]) + __uint_as_float(s[1]), cd = __uint_as_float(t[0]) + __uint_as_float(t[1]);
  const auto u = __builtin_amdgcn_permlane16_swap(__float_as_uint(ab), __float_as_uint(cd), false, false);
  return __uint_as_float(u[0]) + __uint_as_float(u[1]);
}
__device__ __forceinline__ float max2(float a, float b) {
  float top = 3.4028235e38f;
  asm("" : "+s"(top));
  return __builtin_amdgcn_fmed3f(a, b, top);
}
__device__ __forceinline__ float xrow_max(float x) {
  const auto s = __builtin_amdgcn_permlane16_swap(__float_as_uint(x), __float_as_uint(x), false, false);
  x = max2(__uint_as_float(s[0]), __uint_as_float(s[1]));
  const auto t = __builtin_amdgcn_permlane32_swap(__float_as_uint(x), __float_as_uint(x), false, false);
  return max2(__uint_as_float(t[0]), __uint_as_float(t[1]));
}
__device__ __forceinline__ float wave_sum(float x) {
  x += dpp<XOR1>(x); x += dpp<XOR2>(x);
  return quad_stride_sum(x);
}
__device__ __forceinline__ float half_sum(float x) {
  x += dpp<XOR1>(x); x += dpp<XOR2>(x); x += dpp<ROR8>(x); x += dpp<ROR4>(x);
  const auto s = __builtin_amdgcn_permlane16_swap(__float_as_uint(x), __float_as_uint(x), false, false);
  return __uint_as_float(s[0]) + __uint_as_float(s[1]);
}
__device__ __forceinline__ f32x4 mfma32(v4u a, v4u b, f32x4 c) {
  return __builtin_amdgcn_mfma_f32_16x16x32_bf16(__builtin_bit_cast(bf16x8, a), __builtin_bit_cast(bf16x8, b), c, 0, 0, 0);
}
__device__ __forceinline__ v2u lds_tr(LAS char* p) {
  return __builtin_bit_cast(v2u, __builtin_amdgcn_ds_read_tr16_b64_v4bf16((LAS bf16x4*)p));
}
__device__ __forceinline__ v4u cat(v2u a, v2u b) { return v4u{a[0], a[1], b[0], b[1]}; }
__device__ __forceinline__ v4u not_sent(v4u u) {
  return v4u{min(u[0], SENT - 1), min(u[1], SENT - 1), min(u[2], SENT - 1), min(u[3], SENT - 1)};
}
__device__ __forceinline__ float silu(float v) { return v / (1.f + __expf(-v)); }

__device__ __forceinline__ void dma1(unsigned voff, const char* sbase, unsigned ldsaddr) {
  asm volatile("s_mov_b32 m0, %2\n\ts_nop 0\n\tglobal_load_lds_dwordx4 %0, %1 nt" :: "v"(voff), "s"(sbase), "s"(ldsaddr) : "memory");
}
__device__ __forceinline__ void dma4(unsigned v0, unsigned v1, unsigned v2, unsigned v3, const char* sbase, unsigned ldsaddr) {
  asm volatile("s_mov_b32 m0, %5\n\ts_nop 0\n\t"
               "global_load_lds_dwordx4 %0, %4 nt\n\tglobal_load_lds_dwordx4 %1, %4 offset:1024 nt\n\t"
               "global_load_lds_dwordx4 %2, %4 offset:2048 nt\n\tglobal_load_lds_dwordx4 %3, %4 offset:3072 nt"
               :: "v"(v0), "v"(v1), "v"(v2), "v"(v3), "s"(sbase), "s"(ldsaddr) : "memory");
}
template <int N> __device__ __forceinline__ void wait_vm() { asm volatile("s_waitcnt vmcnt(%0)" :: "i"(N) : "memory"); }
#define SLOT_GROUP(W, STEP) \
  "s_max_i32 %[pub], %[pub], %[cand]\n\tv_mov_b32 %[tmp], %[pub]\n\ts_mov_b32 m0, %[lds]\n\ts_add_u32 %[lds], %[lds], 0x1000\n\t" \
  "s_add_i32 %[cand], %[cand], 1\n\ts_waitcnt vmcnt(" #W ")\n\t" \
  "global_load_lds_dwordx4 %[v0], vcc nt\n\tds_write_b32 %[va], %[tmp]\n\tglobal_load_lds_dwordx4 %[v1], vcc offset:1024 nt\n\t" \
  "global_load_lds_dwordx4 %[v2], vcc offset:2048 nt\n\tglobal_load_lds_dwordx4 %[v3], vcc offset:3072 nt\n\t" \
  "s_add_u32 vcc_lo, vcc_lo, " STEP "\n\ts_addc_u32 vcc_hi, vcc_hi, 0\n\t"
#define SLOT_LEVEL(W) \
  "s_mov_b64 vcc, %[src]\n\t" SLOT_GROUP(W, "0x1000") SLOT_GROUP(W, "%[mid]") SLOT_GROUP(W, "0x1000") SLOT_GROUP(W, "0") "s_branch 9f\n\t"
__device__ __forceinline__ void issue_slot(unsigned v0, unsigned v1, unsigned v2, unsigned v3, const char* src, int mid, unsigned lds,
                                           int w, int cand, int& pub, unsigned va) {
  unsigned tmp;
  int jump;
  asm volatile("s_mul_i32 %[jmp], %[lvl], 8f-7f\n\ts_getpc_b64 vcc\n6:\n\ts_add_u32 %[jmp], %[jmp], 7f-6b\n\t"
               "s_add_u32 vcc_lo, vcc_lo, %[jmp]\n\ts_addc_u32 vcc_hi, vcc_hi, 0\n\ts_setpc_b64 vcc\n7:\n\t"
               SLOT_LEVEL(12) "8:\n\t" SLOT_LEVEL(16) SLOT_LEVEL(20) SLOT_LEVEL(24) SLOT_LEVEL(28) SLOT_LEVEL(32) "9:"
               : [lds] "+&s"(lds), [cand] "+&s"(cand), [pub] "+&s"(pub), [tmp] "=&v"(tmp), [jmp] "=&s"(jump)
               : [v0] "v"(v0), [v1] "v"(v1), [v2] "v"(v2), [v3] "v"(v3), [src] "s"(src), [mid] "s"(mid), [lvl] "s"(w - 3), [va] "v"(va)
               : "vcc", "scc", "memory");
}
__device__ __forceinline__ int opaque(int v) { asm volatile("" : "+s"(v)); return v; }
__device__ __forceinline__ void wait_lgkm() { asm volatile("s_waitcnt lgkmcnt(0)" ::: "memory"); }

typedef volatile LAS unsigned* ctrl_t;
template <int SLEEP = 0> __device__ __forceinline__ unsigned lds_wait_ge(ctrl_t ctrl, int word, unsigned want, int& budget) {
  unsigned v, tmp;
  int go;
  budget = __builtin_amdgcn_readfirstlane(budget);
  want = __builtin_amdgcn_readfirstlane(want);
  asm volatile("1:\n\tds_read_b32 %[t], %[a]\n\t.if %[slp]\n\ts_sleep %[slp]\n\t.endif\n\ts_waitcnt lgkmcnt(0)\n\tv_readfirstlane_b32 %[v], %[t]\n\t"
               "s_cmp_lt_u32 %[v], %[w]\n\ts_cselect_b32 %[g], %[b], 0\n\ts_subb_u32 %[b], %[b], 0\n\ts_cmp_gt_i32 %[g], 0\n\ts_cbranch_scc1 1b"
               : [v] "=&s"(v), [t] "=&v"(tmp), [g] "=&s"(go), [b] "+&s"(budget)
               : [a] "v"((unsigned)(uintptr_t)(ctrl + word)), [w] "s"(want), [slp] "i"(SLEEP) : "scc", "memory");
  return v;
}
#define BOX_LOOP_END \
  "v_cmpx_eq_u32 vcc, -1, %[t]\n\ts_sub_i32 %[b], %[b], 1\n\ts_cmp_gt_i32 %[b], 0\n\ts_cselect_b64 vcc, exec, 0\n\t" \
  "s_cmp_lg_u64 vcc, 0\n\ts_cbranch_scc0 2f\n\t.if %[slp]\n\ts_sleep %[slp]\n\t.endif\n\ts_branch 1b\n2:\n\ts_mov_b64 exec, -1"
template <int SLEEP> __device__ __forceinline__ v4u poll_box(__amdgpu_buffer_rsrc_t rs, int off, int& budget) {
  v4u a;
  unsigned tmp;
  budget = __builtin_amdgcn_readfirstlane(budget);
  asm volatile("1:\n\tbuffer_load_dwordx4 v[100:103], %[o], %[rs], 0 offen sc1\n\ts_waitcnt vmcnt(0)\n\t"
               "v_max_u32 %[t], v100, v101\n\tv_max3_u32 %[t], %[t], v102, v103\n\t" BOX_LOOP_END
               : "=&{v[100:103]}"(a), [t] "=&v"(tmp), [b] "+&s"(budget) : [o] "v"(off), [rs] "s"(rs), [slp] "i"(SLEEP) : "vcc", "scc", "memory");
  return a;
}
template <int SLEEP> __device__ __forceinline__ void poll_box2(__amdgpu_buffer_rsrc_t rs, int off0, int off1, v4u& a, v4u& b, int& budget) {
  unsigned tmp;
  budget = __builtin_amdgcn_readfirstlane(budget);
  asm volatile("1:\n\tbuffer_load_dwordx4 v[100:103], %[o0], %[rs], 0 offen sc1\n\tbuffer_load_dwordx4 v[104:107], %[o1], %[rs], 0 offen sc1\n\t"
               "s_waitcnt vmcnt(0)\n\tv_max_u32 %[t], v100, v101\n\tv_max3_u32 %[t], %[t], v102, v103\n\t"
               "v_max3_u32 %[t], %[t], v104, v105\n\tv_max3_u32 %[t], %[t], v106, v107\n\t" BOX_LOOP_END
               : "=&{v[100:103]}"(a), "=&{v[104:107]}"(b), [t] "=&v"(tmp), [b] "+&s"(budget)
               : [o0] "v"(off0), [o1] "v"(off1), [rs] "s"(rs), [slp] "i"(SLEEP) : "vcc", "scc", "memory");
}
__device__ __forceinline__ void poll_lds_o(unsigned addr, v4u& a, v4u& b, int& budget) {
  unsigned tmp;
  budget = __builtin_amdgcn_readfirstlane(budget);
  asm volatile("1:\n\tds_read_b64 v[100:101], %[o]\n\tds_read_b64 v[102:103], %[o] offset:32\n\tds_read_b64 v[104:105], %[o] offset:64\n\t"
               "ds_read_b64 v[106:107], %[o] offset:96\n\ts_waitcnt lgkmcnt(0)\n\tv_max_u32 %[t], v100, v101\n\tv_max3_u32 %[t], %[t], v102, v103\n\t"
               "v_max3_u32 %[t], %[t], v104, v105\n\tv_max3_u32 %[t], %[t], v106, v107\n\t" BOX_LOOP_END
               : "=&{v[100:103]}"(a), "=&{v[104:107]}"(b), [t] "=&v"(tmp), [b] "+&s"(budget) : [o] "v"(addr), [slp] "i"(0) : "vcc", "scc", "memory");
}

typedef unsigned v16u __attribute__((ext_vector_type(16)));
__device__ __forceinline__ char* as_ptr(unsigned lo, unsigned hi) { return (char*)(((u64)hi << 32) | lo); }

__global__ void __launch_bounds__(448, 2) fused_decode(const char*, const char*, const char*, const char*, const char*, const char*, const char*,
                                                    char*, char*) {
  v16u ka16;
  v2u ka2;
  asm volatile("s_load_dwordx16 s[36:51], %2, 0x0\n\ts_load_dwordx2 s[52:53], %2, 0x40"
               : "={s[36:51]}"(ka16), "={s[52:53]}"(ka2) : "s"(__builtin_amdgcn_kernarg_segment_ptr()));
  __builtin_amdgcn_sched_barrier(0);
  extern __shared__ __attribute__((aligned(16))) char smem[];
  LAS char* L = (LAS char*)smem;
  const unsigned lbase = (unsigned)(uintptr_t)L;
  ctrl_t ctrl = (ctrl_t)(L + L_CTRL);
  const int lane = threadIdx.x & 63, bid = blockIdx.x;
  int wave = __builtin_amdgcn_readfirstlane(threadIdx.x >> 6);
  const int xg = bid & 7, tg = bid >> 3, kvh = tg >> 2, ii = tg & 3, gi = 4 * xg + ii, wid = kvh * 32 + gi, bb = gi >> 4;
  const int nt = ii == 0 ? 33 : ii == 1 ? 31 : 32, tile0 = (xg & 3) * 128 + (ii == 1 ? 33 : 32 * ii);
  const int g_kv = 8 * nt, g_wo = g_kv + 4;
  const int g = lane >> 4, col = lane & 15, q4 = (lane >> 2) & 3, p4 = lane & 3;
  const int ffn = xg * 8 + kvh;
  const bool has_ffn = ii == 1;
  unsigned vk[4], vx = lane * 16, vq = (lane >> 1) * 8192 + (lane & 1) * 16;
#pragma unroll
  for (int j = 0; j < 4; ++j) vk[j] = g * 256 + ((col ^ (4 * j + g)) * 16);
  unsigned kvoff = ((bb * 8 + kvh) * SK + tile0 * 64) * 256, qoff = (kvh * 512 + gi * 16) * 2;
  asm volatile("" : "+v"(vk[0]), "+v"(vk[1]), "+v"(vk[2]), "+v"(vk[3]), "+v"(vx), "+v"(vq));
  asm volatile("" : "+s"(kvoff), "+s"(qoff), "+s"(wave));
  ctrl[lane] = 0u;
  ctrl[C_LANDED + lane] = 0u;
  *(volatile LAS unsigned*)(L + L_O + lane * 4) = SENT;
  asm volatile("s_waitcnt lgkmcnt(0)\n\ts_barrier" : "+{s[36:51]}"(ka16), "+{s[52:53]}"(ka2) :: "memory");
  const char *x = as_ptr(ka16[0], ka16[1]), *kc = as_ptr(ka16[2], ka16[3]), *vc = as_ptr(ka16[4], ka16[5]), *wq = as_ptr(ka16[6], ka16[7]),
             *wo = as_ptr(ka16[8], ka16[9]), *w1 = as_ptr(ka16[10], ka16[11]), *w2 = as_ptr(ka16[12], ka16[13]);
  char *out = as_ptr(ka16[14], ka16[15]), *ws = as_ptr(ka2[0], ka2[1]);
  const __amdgpu_buffer_rsrc_t rs = __builtin_amdgcn_make_buffer_rsrc((void*)ws, 0, WS_END, 0x00020000);
  const unsigned t_start = (unsigned)__builtin_amdgcn_s_memrealtime();
  if (wave == 1) {
    __builtin_amdgcn_s_setprio(3);
    const u64 pk_ = (u64)(kc + kvoff), pv_ = (u64)(vc + kvoff), pq_ = (u64)(wq + qoff), pq2 = pq_ + 511 * 8192;
    asm volatile("v_writelane_b32 v100, %0, 0\n\tv_writelane_b32 v101, %1, 0\n\tv_writelane_b32 v100, %2, 1\n\tv_writelane_b32 v101, %3, 1\n\t"
                 "v_writelane_b32 v100, %4, 2\n\tv_writelane_b32 v101, %5, 2\n\tv_writelane_b32 v100, %6, 3\n\tv_writelane_b32 v101, %7, 3\n\t"
                 "v_writelane_b32 v100, %8, 4\n\tv_writelane_b32 v101, %9, 4\n\tv_writelane_b32 v100, %10, 5\n\tv_writelane_b32 v101, %11, 5\n\t"
                 "s_mov_b32 m0, %12\n\ts_mov_b64 exec, 63\n\tglobal_load_lds_dword v[100:101], off nt\n\ts_mov_b64 exec, -1"
                 :: "s"((unsigned)pk_), "s"((unsigned)(pk_ >> 32)), "s"((unsigned)pv_), "s"((unsigned)(pv_ >> 32)), "s"(ka16[0]), "s"(ka16[1]),
                    "s"(ka2[0]), "s"(ka2[1]), "s"((unsigned)pq_), "s"((unsigned)(pq_ >> 32)), "s"((unsigned)pq2), "s"((unsigned)(pq2 >> 32)),
                    "s"(lbase + L_NIL) : "v100", "v101", "memory");
#pragma unroll
    for (int j = 0; j < 4; ++j) dma4(vk[0], vk[1], vk[2], vk[3], kc + kvoff + j * 4096, lbase + j * 4096);
  }
  if (opaque(wave) == 0) {
    asm volatile("s_mov_b32 m0, %4\n\ts_nop 0\n\tglobal_load_lds_dwordx4 %0, %1\n\tglobal_load_lds_dwordx4 %0, %1 offset:1024\n\t"
                 "s_mov_b32 m0, %5\n\ts_nop 0\n\tglobal_load_lds_dwordx4 %2, %3 sc1"
                 :: "v"(vx), "s"(x), "v"(WS_EPOCH), "s"(ws), "s"(lbase + L_W), "s"(lbase + L_W + 2048) : "memory");
  }
  if (opaque(wave) == 6) {
#pragma nounroll
    for (int j = 0; j < 16; ++j) dma1(vq, wq + qoff + (size_t)j * (32 * 8192), lbase + L_S + j * 1024);
    wait_vm<0>();
    if (lane == 0) ctrl[C_WQ] = 1u;
  }
  const int role = opaque(wave);
  int budget = SPIN_BUDGET;

  if (__builtin_expect(role == 1, 1)) {
    unsigned vv[2];
#pragma unroll
    for (int j = 0; j < 2; ++j) vv[j] = g * 256 + ((col ^ ((4 * j + g) << 1)) * 16);
    const char* kb = kc + kvoff;
    const char* vb = vc + kvoff;
    const char* wb = wo + (size_t)(kvh * 512 + 32 * (gi >> 3) + 8 * ((gi >> 2) & 1)) * 1024 + ii * 256;
    const unsigned va_pub = lbase + L_CTRL + (C_LANDED + lane) * 4;
    const int w16 = W0 * 16 + (ii == 0 ? FF0 : ii == 1 ? FF1 : 0);
    int issued = 4, pub = 0, wl = W0 / 4, dith = 0;
#pragma nounroll
    for (int seq = 1; seq <= 2 * nt; ++seq) {
      {
        const v4u sc = *(volatile LAS v4u*)(ctrl + C_SCHED);
        v4u f = *(volatile LAS v4u*)(ctrl + C_FREED);
        const int el = (int)((unsigned)__builtin_amdgcn_s_memrealtime() - __builtin_amdgcn_readfirstlane(sc[0]));
        const int rate = __builtin_amdgcn_readfirstlane(sc[2]);
        const int lag16 = rate ? ((((int)__builtin_amdgcn_readfirstlane(sc[1]) + ((el * rate) >> 8)) * nt) >> 5) - pub * 16 : 0;
        const int wk = w16 + lag16 * KN / KD + dith;
        wl = __builtin_amdgcn_readfirstlane(min(max((wk + 32) >> 6, WMIN / 4), WMAX / 4));
        dith = min(max(wk - wl * 64, -32), 32);
        if ((int)__builtin_amdgcn_readfirstlane(min(min(f[0], f[1]), min(f[2], f[3]))) <= seq - NSLOT) {
          wait_vm<0>(); pub = issued; ctrl[C_LANDED + lane] = pub;
          do {
            __builtin_amdgcn_s_sleep(1);
            f = *(volatile LAS v4u*)(ctrl + C_FREED);
          } while ((int)__builtin_amdgcn_readfirstlane(min(min(f[0], f[1]), min(f[2], f[3]))) <= seq - NSLOT && --budget > 0);
        }
      }
      const bool is_wo = seq == 2 * nt, is_k = seq < nt;
#define DRAIN_TO(N) { wait_vm<4 * (N)>(); pub = max(pub, issued - (N)); ctrl[C_LANDED + lane] = pub; }
      if (is_wo) { DRAIN_TO(8) DRAIN_TO(7) DRAIN_TO(6) DRAIN_TO(5) DRAIN_TO(4) DRAIN_TO(3) DRAIN_TO(2) DRAIN_TO(1) DRAIN_TO(0) wl = 8; }
      unsigned v[4];
#pragma unroll
      for (int j = 0; j < 4; ++j) v[j] = is_k ? vk[j] : is_wo ? vv[j & 1] + g * 768 + j * (127 * 1024) : vv[j & 1];
      const char* src = is_wo ? wb : is_k ? kb + seq * SLOT : vb + (seq - nt) * SLOT;
      issue_slot(v[0], v[1], v[2], v[3], src, is_wo ? 12288 : 4096, lbase + (seq & (NSLOT - 1)) * SLOT, wl, issued - wl, pub, va_pub);
      issued += 4;
    }
    DRAIN_TO(8) DRAIN_TO(7) DRAIN_TO(6) DRAIN_TO(5) DRAIN_TO(4) DRAIN_TO(0)
#undef DRAIN_TO
    return;
  }

  if (role == 0) {
    u64 tag;
    v4u xb[16];
    {
      wait_vm<0>();
      tag = *(volatile LAS u64*)(L + L_W + 2048) + 1;
      const v4u xv0 = *(volatile LAS v4u*)(L + L_W + lane * 32), xv1 = *(volatile LAS v4u*)(L + L_W + lane * 32 + 16);
      float v[16];
#pragma unroll
      for (int e = 0; e < 4; ++e) { v[2 * e] = bf_lo(xv0[e]); v[2 * e + 1] = bf_hi(xv0[e]); v[8 + 2 * e] = bf_lo(xv1[e]); v[9 + 2 * e] = bf_hi(xv1[e]); }
      float s4[4], q4s[4];
#pragma unroll
      for (int e = 0; e < 4; ++e) s4[e] = (v[e] + v[4 + e]) + (v[8 + e] + v[12 + e]);
      const float mean = half_sum((s4[0] + s4[1]) + (s4[2] + s4[3])) * (1.f / D);
#pragma unroll
      for (int e = 0; e < 4; ++e) {
        const float d0 = v[e] - mean, d1 = v[4 + e] - mean, d2 = v[8 + e] - mean, d3 = v[12 + e] - mean;
        q4s[e] = fmaf(d0, d0, d1 * d1) + fmaf(d2, d2, d3 * d3);
      }
      const float var = half_sum((q4s[0] + q4s[1]) + (q4s[2] + q4s[3])) * (1.f / D);
      const float mb = rbf(mean), den = rbf(__builtin_amdgcn_sqrtf(rbf(rbf(var) + 1e-5f))), rden = __builtin_amdgcn_rcpf(den);
      unsigned o[8];
#pragma unroll
      for (int e = 0; e < 8; ++e) {
        const unsigned dp = pk_bf16(v[2 * e] - mb, v[2 * e + 1] - mb);
        const float d0 = bf_lo(dp), d1 = bf_hi(dp), q0 = d0 * rden, q1 = d1 * rden;
        o[e] = pk_bf16(fmaf(fmaf(-q0, den, d0), rden, q0), fmaf(fmaf(-q1, den, d1), rden, q1));
      }
      *(LAS v4u*)(L + L_XN + lane * 32) = v4u{o[0], o[1], o[2], o[3]};
      *(LAS v4u*)(L + L_XN + lane * 32 + 16) = v4u{o[4], o[5], o[6], o[7]};
      asm volatile("" ::: "memory");
      LAS char* xa = L + L_XN + (((col >> 1) & 1) * 512 + 4 * g) * 2;
#pragma unroll
      for (int ks = 0; ks < 16; ++ks) xb[ks] = cat(*(LAS v2u*)(xa + ks * 64), *(LAS v2u*)(xa + ks * 64 + 32));
      __builtin_amdgcn_sched_barrier(0);
      lds_wait_ge(ctrl, C_WQ, 1u, budget);
      f32x4 qp[2] = {{0.f, 0.f, 0.f, 0.f}, {0.f, 0.f, 0.f, 0.f}};
      LAS char* wa = L + L_S + 8 * lane;
      v4u fa[16];
#pragma unroll
      for (int ks = 0; ks < 16; ++ks) fa[ks] = cat(lds_tr(wa + ks * 1024), lds_tr(wa + ks * 1024 + 512));
      __builtin_amdgcn_sched_barrier(0);
#pragma unroll
      for (int ks = 0; ks < 16; ++ks) qp[ks & 1] = mfma32(fa[ks], xb[ks], qp[ks & 1]);
      const f32x4 qa = qp[0] + qp[1];
      if ((col & 13) == 0) *(LAS v2u*)(L + L_Q + (col >> 1) * 32 + g * 8) = v2u{pk_bf16(qa[0], qa[1]), pk_bf16(qa[2], qa[3])};
      asm volatile("" ::: "memory");
      const unsigned thi = (unsigned)(tag >> 16), tlo = (unsigned)tag << 16;
      const unsigned qv = *(LAS unsigned short*)(L + L_Q + (lane & 31) * 2);
      __builtin_amdgcn_raw_buffer_store_b64(v2u{tlo | qv, thi}, rs, lane < 32 ? WS_QG + ((kvh * 2 + g) * 512 + gi * 16 + col) * 8 : OOB, 0, AUX_SC1);
      if (lane == 0) *(volatile LAS u64*)(ctrl + C_TAG) = tag;
    }
    {
      const unsigned thi = (unsigned)(tag >> 16), tlo = (unsigned)tag << 16;
      while ((int)((unsigned)__builtin_amdgcn_s_memrealtime() - t_start) < QDELAY) __builtin_amdgcn_s_sleep(2);
      const int qo = WS_QG + (kvh * 2 + bb) * 4096 + lane * 8;
      v2u a[8];
#pragma unroll
      for (int k = 0; k < 8; ++k) a[k] = v2u{~tlo, ~thi};
      for (;;) {
        asm volatile("" ::: "memory");
        bool all = true;
        v2u nv[8];
#pragma unroll
        for (int k = 0; k < 8; ++k) {
          const bool ok = a[k][1] == thi && ((a[k][0] ^ tlo) >> 16) == 0;
          nv[k] = __builtin_amdgcn_raw_buffer_load_b64(rs, ok ? OOB : qo + 512 * k, 0, AUX_SC1);
        }
#pragma unroll
        for (int k = 0; k < 8; ++k) {
          const bool ok = a[k][1] == thi && ((a[k][0] ^ tlo) >> 16) == 0;
          a[k] = ok ? a[k] : nv[k];
          all = all && a[k][1] == thi && ((a[k][0] ^ tlo) >> 16) == 0;
        }
        if (__all(all)) break;
        if (--budget <= 0) break;
        __builtin_amdgcn_s_sleep(4);
      }
#pragma unroll
      for (int k = 0; k < 8; ++k) *(LAS unsigned short*)(L + L_Q + (64 * k + lane) * 2) = (unsigned short)a[k][0];
      wait_lgkm();
      if (lane == 0) ctrl[C_Q] = 1u;
    }
    auto ffn_slice = [&]() {
      const int u0 = (28 * ffn) & ~7, lo = 28 * ffn - u0;
      const unsigned vo1 = (lane >> 2) * (FF * 2) + ((p4 ^ ((lane >> 3) & 2)) * 16);
      LAS char* wa = L + L_W + (4 * g + q4) * 64 + 8 * p4;
      f32x4 h0 = {0.f, 0.f, 0.f, 0.f}, h1 = h0;
#pragma unroll
      for (int rd = 0; rd < 4; ++rd) {
#pragma unroll
        for (int j = 0; j < 8; ++j) dma1(vo1, w1 + u0 * 2 + (size_t)(rd * 128 + j * 16) * (FF * 2), lbase + L_W + j * 1024);
        wait_vm<0>();
        v4u fa[4][2];
#pragma unroll
        for (int ks = 0; ks < 4; ++ks)
#pragma unroll
          for (int nb = 0; nb < 2; ++nb) {
            LAS char* p = wa + ks * 2048 + ((nb ^ (g & 1)) * 32);
            fa[ks][nb] = cat(lds_tr(p), lds_tr(p + 1024));
          }
        __builtin_amdgcn_sched_barrier(0);
        wait_lgkm();
#pragma unroll
        for (int ks = 0; ks < 4; ++ks) { h0 = mfma32(fa[ks][0], xb[4 * rd + ks], h0); h1 = mfma32(fa[ks][1], xb[4 * rd + ks], h1); }
      }
      float hv[8];
#pragma unroll
      for (int j = 0; j < 8; ++j) {
        const int u = (j >> 2) * 16 + 4 * g + (j & 3);
        hv[j] = (u >= lo && u < lo + 28) ? silu(rbf(j < 4 ? h0[j & 3] : h1[j & 3])) : 0.f;
      }
      const v4u hb = {pk_bf16(hv[0], hv[1]), pk_bf16(hv[2], hv[3]), pk_bf16(hv[4], hv[5]), pk_bf16(hv[6], hv[7])};
      const unsigned vo2[2] = {(unsigned)(g * 1024 + ((col ^ (g << 1)) * 16)), (unsigned)(g * 1024 + ((col ^ ((4 + g) << 1)) * 16))};
      const int sw = (4 * (g & 1) + q4) << 1;
      LAS char* w2a = L + L_W + (4 * g + q4) * 256 + 8 * (p4 & 1);
      const int mine = 2 * (col >> 2) + (col & 1);
#pragma unroll
      for (int cb = 0; cb < 4; ++cb) {
#pragma unroll
        for (int j = 0; j < 8; ++j) dma1(vo2[j & 1], w2 + (size_t)(u0 + 4 * j) * 1024 + cb * 256, lbase + L_W + j * 1024);
        wait_vm<0>();
        v4u wf[8];
#pragma unroll
        for (int nb = 0; nb < 8; ++nb) {
          LAS char* p = w2a + (((2 * nb + (p4 >> 1)) ^ sw) * 16);
          wf[nb] = cat(lds_tr(p), lds_tr(p + 4096));
        }
        __builtin_amdgcn_sched_barrier(0);
        wait_lgkm();
        f32x4 fo = {0.f, 0.f, 0.f, 0.f};
#pragma unroll
        for (int nb = 0; nb < 8; ++nb) fo = mfma32(wf[nb], mine == nb ? hb : v4u{0u, 0u, 0u, 0u}, fo);
        __builtin_amdgcn_raw_buffer_store_b128(not_sent(__builtin_bit_cast(v4u, fo)), rs, WS_PF + ffn * 4096 + cb * 1024 + lane * 16, 0, AUX_SC1);
      }
    };
    if (has_ffn) {
      ffn_slice();
      wait_vm<0>();
      if (lane == 0) __hip_atomic_store((GAS u64*)(ws + WS_FOK) + ffn, tag, RLX_AGENT);
    }

    lds_wait_ge<8>(ctrl, C_ALL, 1u, budget);
    const int cb = xg >> 1, pc = (xg & 1) * 32 + tg, cw = pc >> 4, gw = (pc >> 2) & 3;
    const int jj = (g >> 1) | ((g & 1) << 1), oidx = ((pc >> 1) & 1) * 512 + cb * 128 + cw * 32 + (pc & 1) * 16 + gw * 4 + jj;
    const float xin = __uint_as_float((unsigned)*(const GAS unsigned short*)(x + oidx * 2) << 16);
    float gate = 0.f;
    {
      const unsigned t0 = *(const GAS unsigned short*)(out + oidx * 2), t1 = *(const GAS unsigned short*)(wo + (size_t)(kvh * 512 + g * 128 + 4 * gi) * 1024);
      asm volatile("" :: "v"(t0), "v"(t1));
    }
#pragma nounroll
    for (int pass = 0; pass < 2; ++pass) {
      const bool last = opaque(pass) != 0;
      if (last) { lds_wait_ge<8>(ctrl, C_LANDED, g_kv - 4, budget); lds_wait_ge(ctrl, C_PUB, 1u, budget); }
      else {
        lds_wait_ge<8>(ctrl, C_LANDED, PFAT, budget);
        while (!__all(__hip_atomic_load((GAS u64*)(ws + WS_FOK) + lane, RLX_AGENT) == tag) && --budget > 0) __builtin_amdgcn_s_sleep(8);
      }
      const int po = (last ? WS_P2 + pc * 16 : WS_PF + gw * 256 + cw * 64 + (pc & 3) * 16) + lane * 4096 + cb * 1024;
      const v4u sv = poll_box<0>(rs, po, budget);
      float t = rows_sum4(__uint_as_float(sv[0]), __uint_as_float(sv[1]), __uint_as_float(sv[2]), __uint_as_float(sv[3]));
      t += dpp<XOR1>(t); t += dpp<XOR2>(t); t += dpp<ROR8>(t); t += dpp<ROR4>(t);
      t = rbf(t);
      if (last) {
        const float y = xin + rbf(t * gate);
        const __amdgpu_buffer_rsrc_t ro = __builtin_amdgcn_make_buffer_rsrc((void*)out, 0, 2 * D * 2, 0x00020000);
        __builtin_amdgcn_raw_buffer_store_b16(budget <= 0 ? (unsigned short)0x7fc0 : (unsigned short)pk_bf16(y, y), ro, col == 0 ? oidx * 2 : OOB, 0, 0);
        break;
      }
      gate = rbf(silu(t));
    }
    return;
  }

  if (role == 6) {
    unsigned at_t = 0;
    int at_p = 0;
    lds_wait_ge<8>(ctrl, C_Q, 1u, budget);
    const u64 tag = *(volatile LAS u64*)(ctrl + C_TAG);
    lds_wait_ge<8>(ctrl, C_CLR, 4u, budget);
#pragma nounroll
    for (int k = 0; k < NROUND; ++k) {
      GAS u64* line = (GAS u64*)(ws + WS_TAU) + (k ? k * 256 : kvh * 32);
      const int at = MARK + k * MSTEP, mark = (at * nt) >> 5;
      lds_wait_ge<1>(ctrl, C_LANDED, mark, budget);
      const unsigned tau = (unsigned)__builtin_amdgcn_s_memrealtime();
      if (lane == 0 && (k == 0 || ii == 3)) __hip_atomic_store(line + (k ? ffn : gi), (tag << 16) | (tau & 0xffffu), RLX_AGENT);
      lds_wait_ge<4>(ctrl, C_LANDED, mark + MDEL, budget);
      const u64 v = __hip_atomic_load(line + (lane & (k ? 63 : 31)), RLX_AGENT);
      const unsigned seen_t = ((v ^ (tag << 16)) >> 16) == 0 ? (unsigned)v : (unsigned)__builtin_amdgcn_s_memrealtime();
      const float d = wave_sum((float)(short)(seen_t - tau));
      const unsigned mean = tau + __builtin_amdgcn_readfirstlane((int)(d * (1.f / 64)));
      constexpr int rate = 65536 / TG16;
      const int common = k ? at_p + (((int)(mean - at_t) * rate) >> 8) - at * 16 : LAG0 * 16;
      at_t = mean;
      at_p = at * 16 + min(max(common, 0), LAGHI * 16);
      if (lane == 0) *(volatile LAS v4u*)(ctrl + C_SCHED) = v4u{at_t, (unsigned)at_p, (unsigned)rate, 0u};
      if (k == 0) {
        while (!__all(((__hip_atomic_load(line + (lane & 31), RLX_AGENT) ^ (tag << 16)) >> 16) == 0) && --budget > 0) __builtin_amdgcn_s_sleep(8);
        if (lane == 0) ctrl[C_FLAG] = 1u;
        if (gi == 0 && lane == 0) __hip_atomic_store((GAS u64*)(ws + WS_GOK) + kvh, tag, RLX_AGENT);
      } else if (k == 1) {
        while (!__all(__hip_atomic_load((GAS u64*)(ws + WS_GOK) + (lane & 7), RLX_AGENT) == tag) && --budget > 0) __builtin_amdgcn_s_sleep(8);
        if (lane == 0) ctrl[C_ALL] = 1u;
        if (bid == 0 && lane == 0) __hip_atomic_store((GAS u64*)(ws + WS_EPOCH), tag, RLX_AGENT);
      }
    }
    return;
  }

  const int c = role - 2;
  const int blk = col >> 2;
  constexpr float SCALE = 0.08838834764831845f, LOG2E = 1.4426950408889634f;
  unsigned seen = 0u;
  auto need = [&](unsigned want) {
    if (seen < want) seen = lds_wait_ge(ctrl, C_LANDED, want, budget);
    asm volatile("" ::: "memory");
  };
  float ms;
  {
    unsigned ka[4];
#pragma unroll
    for (int k = 0; k < 4; ++k) ka[k] = col * 256 + (((4 * k + g) ^ col) * 16);
    v4u kf[4][4];
    auto fetch_k = [&](int t) {
      need(4 * t + 4);
      LAS char* sb = L + (t & (NSLOT - 1)) * SLOT;
#pragma unroll
      for (int k = 0; k < 4; ++k)
#pragma unroll
        for (int a = 0; a < 4; ++a) kf[a][k] = *(LAS v4u*)(sb + ka[k] + a * 4096);
      __builtin_amdgcn_sched_barrier(0);
      wait_lgkm();
      if (lane == 0) ctrl[C_FREED + c] = min(t + 4, nt);
      __builtin_amdgcn_sched_barrier(0);
    };
    fetch_k(c);
    const v4u sv = {SENT, SENT, SENT, SENT};
    if (c < 3) __builtin_amdgcn_raw_buffer_store_b128(sv, rs, (c < 2 ? WS_P1 + wid * 2048 + c * 1024 : WS_P2 + wid * 1024) + lane * 16, 0, AUX_SC1);
    wait_vm<0>();
    if (lane == 0) __hip_atomic_fetch_add((LAS unsigned*)(ctrl + C_CLR), 1u, __ATOMIC_RELAXED, __HIP_MEMORY_SCOPE_WORKGROUP);
    lds_wait_ge<2>(ctrl, C_Q, 1u, budget);
    v4u qf[4][4];
#pragma unroll
    for (int k = 0; k < 4; ++k) {
      const v4u qv = *(LAS v4u*)(L + L_Q + ((col & 3) * 128 + 32 * k + 8 * g) * 2);
#pragma unroll
      for (int a = 0; a < 4; ++a) qf[a][k] = blk == a ? qv : v4u{0u, 0u, 0u, 0u};
    }
    float mx = -3.0e38f;
#pragma nounroll
    for (int t = c;; t += 4) {
      f32x4 s0 = {0.f, 0.f, 0.f, 0.f}, s1 = {0.f, 0.f, 0.f, 0.f};
#pragma unroll
      for (int k = 0; k < 4; ++k) {
        s0 = mfma32(kf[0][k], qf[0][k], s0); s1 = mfma32(kf[1][k], qf[1][k], s1);
        s0 = mfma32(kf[2][k], qf[2][k], s0); s1 = mfma32(kf[3][k], qf[3][k], s1);
      }
      s0 += s1;
      const unsigned a0 = pk_bf16(s0[0], s0[1]), a1 = pk_bf16(s0[2], s0[3]);
      const unsigned b0 = pk_bf16(bf_lo(a0) * SCALE, bf_hi(a0) * SCALE), b1 = pk_bf16(bf_lo(a1) * SCALE, bf_hi(a1) * SCALE);
      *(LAS v2u*)(L + L_S + t * 512 + lane * 8) = v2u{b0, b1};
      mx = max2(max2(mx, max2(bf_lo(b0), bf_hi(b0))), max2(bf_lo(b1), bf_hi(b1)));
      if (t + 4 >= nt) break;
      fetch_k(t + 4);
    }
    mx = max2(mx, dpp<ROR8>(mx)); mx = max2(mx, dpp<ROR4>(mx)); mx = xrow_max(mx);
    if (lane < 4) *(LAS float*)(L + L_M + lane * 16 + c * 4) = mx;
    wait_lgkm();
    if (lane == 0) __hip_atomic_fetch_add((LAS unsigned*)(ctrl + C_MAX), 1u, __ATOMIC_RELAXED, __HIP_MEMORY_SCOPE_WORKGROUP);
    lds_wait_ge(ctrl, C_MAX, 4u, budget);
    const f32x4 m4 = *(LAS f32x4*)(L + L_M + (col & 3) * 16);
    ms = max2(max2(m4[0], m4[1]), max2(m4[2], m4[3])) * LOG2E;
  }
  unsigned va[2];
#pragma unroll
  for (int n = 0; n < 2; ++n)
    va[n] = (4 * g + q4) * 256 + (((4 * c + 2 * n + (p4 >> 1)) ^ ((4 * (g & 1) + q4) << 1)) * 16) + 8 * (p4 & 1);
  float lsum = 0.f, linv = 0.f;
  unsigned lam = 0;
  f32x4 acc0 = {0.f, 0.f, 0.f, 0.f}, acc1 = {0.f, 0.f, 0.f, 0.f};
#pragma nounroll
  for (int t = 0; t < nt; ++t) {
    const int seq = nt + t;
    const v2u sc = *(LAS v2u*)(L + L_S + t * 512 + lane * 8);
    const float e0 = __builtin_amdgcn_exp2f(fmaf(bf_lo(sc[0]), LOG2E, -ms)), e1 = __builtin_amdgcn_exp2f(fmaf(bf_hi(sc[0]), LOG2E, -ms));
    const float e2 = __builtin_amdgcn_exp2f(fmaf(bf_lo(sc[1]), LOG2E, -ms)), e3 = __builtin_amdgcn_exp2f(fmaf(bf_hi(sc[1]), LOG2E, -ms));
    lsum += (e0 + e1) + (e2 + e3);
    const unsigned p0 = pk_bf16(e0, e1), p1 = pk_bf16(e2, e3);
    v4u pA = {blk == 0 ? p0 : 0u, blk == 0 ? p1 : 0u, blk == 1 ? p0 : 0u, blk == 1 ? p1 : 0u};
    v4u pB = {blk == 2 ? p0 : 0u, blk == 2 ? p1 : 0u, blk == 3 ? p0 : 0u, blk == 3 ? p1 : 0u};
    if (t == nt - 1) {
      lds_wait_ge(ctrl, C_FLAG, 1u, budget);
      const float lt = quad_stride_sum(lsum);
      linv = __builtin_amdgcn_rcpf(lt);
      lam = __float_as_uint(ms + __builtin_amdgcn_logf(lt));
    }
    asm volatile("" : "+v"(pA), "+v"(pB), "+v"(lsum), "+v"(linv), "+v"(lam));
    LAS char* sb = L + (seq & (NSLOT - 1)) * SLOT;
    need(4 * seq + 2);
    acc0 = mfma32(cat(lds_tr(sb + va[0]), lds_tr(sb + va[0] + 4096)), pA, acc0);
    acc1 = mfma32(cat(lds_tr(sb + va[1]), lds_tr(sb + va[1] + 4096)), pA, acc1);
    need(4 * seq + 4);
    v4u v0 = cat(lds_tr(sb + va[0] + 8192), lds_tr(sb + va[0] + 12288)), v1 = cat(lds_tr(sb + va[1] + 8192), lds_tr(sb + va[1] + 12288));
    wait_lgkm();
    if (lane == 0) ctrl[C_FREED + c] = seq + 1;
    asm volatile("" : "+v"(v0), "+v"(v1) :: "memory");
    acc0 = mfma32(v0, pB, acc0);
    acc1 = mfma32(v1, pB, acc1);
  }

  v4u pk;
#pragma unroll
  for (int j = 0; j < 4; ++j) {
    float a = ((col & 4) ? acc1[j] : acc0[j]) + dpp<ROR4>((col & 4) ? acc0[j] : acc1[j]);
    a += dpp<ROR8>(a);
    pk[j] = ((__float_as_uint(a * linv) + 0x80u) & 0xffffff00u) | ((lam >> (8 * j)) & 0xffu);
  }
  __builtin_amdgcn_raw_buffer_store_b128(not_sent(pk), rs, col < 8 ? WS_P1 + wid * 2048 + c * 512 + g * 128 + col * 16 : OOB, 0, AUX_SC1);
  const int rec = WS_P1 + (kvh * 32 + (c >> 1) * 16 + (lane >> 3)) * 2048 + (gi >> 2) * 256 + (c & 1) * 128 + (lane & 7) * 16;
  v4u a0, a1, wa[4];
  need(g_wo);
  LAS char* wp = L + ((2 * nt) & (NSLOT - 1)) * SLOT;
#pragma unroll
  for (int n = 0; n < 4; ++n) wa[n] = cat(lds_tr(wp + va[n & 1] + (n >> 1) * 4096), lds_tr(wp + va[n & 1] + (n >> 1) * 4096 + 8192));
  __builtin_amdgcn_sched_barrier(0);
  poll_box2<0>(rs, rec, rec + 8 * 2048, a0, a1, budget);
  const float lA = __uint_as_float(__builtin_amdgcn_perm(__builtin_amdgcn_perm(a0[3], a0[2], 0x04000c0cu), __builtin_amdgcn_perm(a0[1], a0[0], 0x0c0c0400u), 0x07060100u));
  const float lB = __uint_as_float(__builtin_amdgcn_perm(__builtin_amdgcn_perm(a1[3], a1[2], 0x04000c0cu), __builtin_amdgcn_perm(a1[1], a1[0], 0x0c0c0400u), 0x07060100u));
  float M = max2(lA, lB);
  M = xrow_max(max2(M, dpp<ROR8>(M)));
  const float wA = __builtin_amdgcn_exp2f(lA - M), wB = __builtin_amdgcn_exp2f(lB - M);
  float r[4];
#pragma unroll
  for (int j = 0; j < 4; ++j) r[j] = fmaf(wA, __uint_as_float(a0[j] & 0xffffff00u), wB * __uint_as_float(a1[j] & 0xffffff00u));
  float wsum = xrow_sum(wA + wB), ov = rows_sum4(r[0], r[1], r[2], r[3]);
  wsum += dpp<ROR8>(wsum); ov += dpp<ROR8>(ov);
  ov *= __builtin_amdgcn_rcpf(wsum);
  const auto pr = __builtin_amdgcn_permlane32_swap(__float_as_uint(ov), __float_as_uint(ov), false, false);
  if ((lane & 40) == 0) *(LAS unsigned*)(L + L_O + c * 64 + col * 8 + g * 4) = min(pk_bf16(__uint_as_float(pr[0]), __uint_as_float(pr[1])), SENT - 1);
  v4u o0, o1;
  poll_lds_o(lbase + L_O + ((col >> 1) & 1) * 128 + g * 8, o0, o1, budget);
  const f32x4 zero = {0.f, 0.f, 0.f, 0.f};
  const f32x4 sp0 = mfma32(wa[2], o1, mfma32(wa[0], o0, zero)), sp1 = mfma32(wa[3], o1, mfma32(wa[1], o0, zero));
  const f32x4 sp = (col & 1) ? sp1 : sp0;
  __builtin_amdgcn_raw_buffer_store_b128(not_sent(__builtin_bit_cast(v4u, sp)), rs, col < 4 ? WS_P2 + wid * 1024 + c * 256 + g * 64 + col * 16 : OOB, 0, AUX_SC1);
  if (lane == 0) ctrl[C_PUB] = 1u;
}

extern "C" void kernel_launch(void* const* d_in, const int* in_sizes, int n_in, void* d_out, int out_size, void* d_ws, size_t ws_size,
                              hipStream_t stream) {
  (void)hipFuncSetAttribute((const void*)fused_decode, hipFuncAttributeMaxDynamicSharedMemorySize, L_BYTES);
  hipLaunchKernelGGL(fused_decode, dim3(NWG), dim3(448), L_BYTES, stream, (const char*)d_in[0], (const char*)d_in[1], (const char*)d_in[2],
                     (const char*)d_in[3], (const char*)d_in[4], (const char*)d_in[6], (const char*)d_in[7], (char*)d_out, (char*)d_ws);
}
```

```cpp
#include <hip/hip_runtime.h>
#include <hip/hip_bf16.h>
#include <stdint.h>

#define GAS __attribute__((address_space(1)))
#define LAS __attribute__((address_space(3)))
#define RLX_AGENT __ATOMIC_RELAXED, __HIP_MEMORY_SCOPE_AGENT
typedef unsigned long long u64;
typedef unsigned v4u __attribute__((ext_vector_type(4)));
typedef unsigned v2u __attribute__((ext_vector_type(2)));
typedef float f32x4 __attribute__((ext_vector_type(4)));
typedef float f32x2 __attribute__((ext_vector_type(2)));
typedef __bf16 bf16x8 __attribute__((ext_vector_type(8)));
typedef __bf16 bf16x4 __attribute__((ext_vector_type(4)));
typedef __bf16 bf16x2 __attribute__((ext_vector_type(2)));

constexpr int W0 = 16, WMIN = 12, WMAX = 32;
constexpr int KN = 3, KD = 2;
constexpr int FF0 = 32, FF1 = -16;
constexpr int TG16 = 218;
constexpr int NROUND = 4, MARK = 64, MSTEP = 48, MDEL = 16;
constexpr int LAG0 = 2, LAGHI = 3;
constexpr int QDELAY = 370, PFAT = 176;

constexpr int D = 512, SK = 32768, FF = 1792, NWG = 256, NSLOT = 8, SLOT = 16384;
constexpr unsigned SENT = 0xFFFFFFFFu;
constexpr int AUX_SC1 = 16, OOB = 0x7ffffff0;

constexpr int WS_EPOCH = 0, WS_GOK = 4096, WS_FOK = 4608, WS_TAU = 8192, WS_QG = 32768, WS_P1 = WS_QG + 65536, WS_P2 = WS_P1 + NWG * 2048,
              WS_PF = WS_P2 + NWG * 1024, WS_END = WS_PF + 64 * 4096;

constexpr int L_S = NSLOT * SLOT, L_CTRL = L_S + 33 * 512, L_XN = L_CTRL + 512, L_Q = L_XN + 2048, L_O = L_Q + 1024, L_M = L_O + 256,
              L_NIL = L_M + 64, L_W = L_NIL + 256, L_BYTES = L_W + 8192;
constexpr int C_LANDED = 64, C_ALL = 0, C_WQ = 1, C_Q = 2, C_CLR = 3, C_FREED = 4, C_PUB = 8, C_FLAG = 9, C_TAG = 10, C_SCHED = 12, C_MAX = 16;
static_assert(NROUND >= 2 && WS_TAU + NROUND * 2048 <= WS_QG && MARK + (NROUND - 1) * MSTEP + MDEL <= 248 && L_BYTES <= 163840, "");
constexpr int SPIN_BUDGET = 1 << 20;

__device__ __forceinline__ unsigned pk_bf16(float lo, float hi) {
  return __builtin_bit_cast(unsigned, __builtin_convertvector(f32x2{lo, hi}, bf16x2));
}
__device__ __forceinline__ float bf_lo(unsigned u) { return __uint_as_float(u << 16); }
__device__ __forceinline__ float bf_hi(unsigned u) { return __uint_as_float(u & 0xffff0000u); }
__device__ __forceinline__ float rbf(float f) { return bf_lo(pk_bf16(f, f)); }
template <int CTRL> __device__ __forceinline__ float dpp(float x) {
  return __builtin_bit_cast(float, __builtin_amdgcn_mov_dpp(__builtin_bit_cast(int, x), CTRL, 0xf, 0xf, true));
}
constexpr int XOR1 = 0xB1, XOR2 = 0x4E, ROR4 = 0x124, ROR8 = 0x128;
__device__ __forceinline__ float xrow_sum(float x) {
  auto s = __builtin_amdgcn_permlane16_swap(__float_as_uint(x), __float_as_uint(x), false, false);
  x = __uint_as_float(s[0]) + __uint_as_float(s[1]);
  auto t = __builtin_amdgcn_permlane32_swap(__float_as_uint(x), __float_as_uint(x), false, false);
  return __uint_as_float(t[0]) + __uint_as_float(t[1]);
}
__device__ __forceinline__ float quad_stride_sum(float x) {
  x += dpp<ROR8>(x); x += dpp<ROR4>(x);
  return xrow_sum(x);
}
__device__ __forceinline__ float rows_sum4(float a, float b, float c, float d) {
  const auto s = __builtin_amdgcn_permlane32_swap(__float_as_uint(a), __float_as_uint(b), false, false);
  const auto t = __builtin_amdgcn_permlane32_swap(__float_as_uint(c), __float_as_uint(d), false, false);
  const float ab = __uint_as_float(s[0]) + __uint_as_float(s[1]), cd = __uint_as_float(t[0]) + __uint_as_float(t[1]);
  const auto u = __builtin_amdgcn_permlane16_swap(__float_as_uint(ab), __float_as_uint(cd), false, false);
  return __uint_as_float(u[0]) + __uint_as_float(u[1]);
}
__device__ __forceinline__ float max2(float a, float b) {
  float top = 3.4028235e38f;
  asm("" : "+s"(top));
  return __builtin_amdgcn_fmed3f(a, b, top);
}
__device__ __forceinline__ float xrow_max(float x) {
  const auto s = __builtin_amdgcn_permlane16_swap(__float_as_uint(x), __float_as_uint(x), false, false);
  x = max2(__uint_as_float(s[0]), __uint_as_float(s[1]));
  const auto t = __builtin_amdgcn_permlane32_swap(__float_as_uint(x), __float_as_uint(x), false, false);
  return max2(__uint_as_float(t[0]), __uint_as_float(t[1]));
}
__device__ __forceinline__ float wave_sum(float x) {
  x += dpp<XOR1>(x); x += dpp<XOR2>(x);
  return quad_stride_sum(x);
}
__device__ __forceinline__ float half_sum(float x) {
  x += dpp<XOR1>(x); x += dpp<XOR2>(x); x += dpp<ROR8>(x); x += dpp<ROR4>(x);
  const auto s = __builtin_amdgcn_permlane16_swap(__float_as_uint(x), __float_as_uint(x), false, false);
  return __uint_as_float(s[0]) + __uint_as_float(s[1]);
}
__device__ __forceinline__ f32x4 mfma32(v4u a, v4u b, f32x4 c) {
  return __builtin_amdgcn_mfma_f32_16x16x32_bf16(__builtin_bit_cast(bf16x8, a), __builtin_bit_cast(bf16x8, b), c, 0, 0, 0);
}
__device__ __forceinline__ v2u lds_tr(LAS char* p) {
  return __builtin_bit_cast(v2u, __builtin_amdgcn_ds_read_tr16_b64_v4bf16((LAS bf16x4*)p));
}
__device__ __forceinline__ v4u cat(v2u a, v2u b) { return v4u{a[0], a[1], b[0], b[1]}; }
__device__ __forceinline__ v4u not_sent(v4u u) {
  return v4u{min(u[0], SENT - 1), min(u[1], SENT - 1), min(u[2], SENT - 1), min(u[3], SENT - 1)};
}
__device__ __forceinline__ float silu(float v) { return v / (1.f + __expf(-v)); }

__device__ __forceinline__ void dma1(unsigned voff, const char* sbase, unsigned ldsaddr) {
  asm volatile("s_mov_b32 m0, %2\n\ts_nop 0\n\tglobal_load_lds_dwordx4 %0, %1" :: "v"(voff), "s"(sbase), "s"(ldsaddr) : "memory");
}
__device__ __forceinline__ void dma4(unsigned v0, unsigned v1, unsigned v2, unsigned v3, const char* sbase, unsigned ldsaddr) {
  asm volatile("s_mov_b32 m0, %5\n\ts_nop 0\n\t"
               "global_load_lds_dwordx4 %0, %4 nt\n\tglobal_load_lds_dwordx4 %1, %4 offset:1024 nt\n\t"
               "global_load_lds_dwordx4 %2, %4 offset:2048 nt\n\tglobal_load_lds_dwordx4 %3, %4 offset:3072 nt"
               :: "v"(v0), "v"(v1), "v"(v2), "v"(v3), "s"(sbase), "s"(ldsaddr) : "memory");
}
template <int N> __device__ __forceinline__ void wait_vm() { asm volatile("s_waitcnt vmcnt(%0)" :: "i"(N) : "memory"); }
#define SLOT_GROUP(W, STEP) \
  "s_max_i32 %[pub], %[pub], %[cand]\n\tv_mov_b32 v99, %[pub]\n\ts_mov_b32 m0, %[lds]\n\ts_add_u32 %[lds], %[lds], 0x1000\n\t" \
  "s_add_i32 %[cand], %[cand], 1\n\ts_waitcnt vmcnt(" #W ")\n\t" \
  "global_load_lds_dwordx4 %[v0], vcc nt\n\tds_write_b32 %[va], v99\n\tglobal_load_lds_dwordx4 %[v1], vcc offset:1024 nt\n\t" \
  "global_load_lds_dwordx4 %[v2], vcc offset:2048 nt\n\tglobal_load_lds_dwordx4 %[v3], vcc offset:3072 nt\n\t" \
  "s_add_u32 vcc_lo, vcc_lo, " STEP "\n\ts_addc_u32 vcc_hi, vcc_hi, 0\n\t"
#define SLOT_LEVEL(W) \
  "s_mov_b64 vcc, %[src]\n\t" SLOT_GROUP(W, "0x1000") SLOT_GROUP(W, "%[mid]") SLOT_GROUP(W, "0x1000") SLOT_GROUP(W, "0") "s_branch 9f\n\t"
__device__ __forceinline__ void issue_slot(unsigned v0, unsigned v1, unsigned v2, unsigned v3, const char* src, int mid, unsigned lds,
                                           int w, int cand, int& pub, unsigned va) {
  int jump;
  asm volatile("s_mul_i32 %[jmp], %[lvl], 8f-7f\n\ts_getpc_b64 vcc\n6:\n\ts_add_u32 %[jmp], %[jmp], 7f-6b\n\t"
               "s_add_u32 vcc_lo, vcc_lo, %[jmp]\n\ts_addc_u32 vcc_hi, vcc_hi, 0\n\ts_setpc_b64 vcc\n7:\n\t"
               SLOT_LEVEL(12) "8:\n\t" SLOT_LEVEL(16) SLOT_LEVEL(20) SLOT_LEVEL(24) SLOT_LEVEL(28) SLOT_LEVEL(32) "9:"
               : [lds] "+&s"(lds), [cand] "+&s"(cand), [pub] "+&s"(pub), [jmp] "=&s"(jump)
               : [v0] "v"(v0), [v1] "v"(v1), [v2] "v"(v2), [v3] "v"(v3), [src] "s"(src), [mid] "s"(mid), [lvl] "s"(w - 3), [va] "v"(va)
               : "v99", "vcc", "scc", "memory");
}
#define SLOTC_GROUP(W, STEP) \
  "s_max_i32 %[pub], %[pub], %[cand]\n\tv_mov_b32 v99, %[pub]\n\ts_mov_b32 m0, %[lds]\n\ts_add_u32 %[lds], %[lds], 0x1000\n\t" \
  "s_add_i32 %[cand], %[cand], 1\n\ts_waitcnt vmcnt(" #W ")\n\t" \
  "global_load_lds_dwordx4 %[v0], vcc\n\tds_write_b32 %[va], v99\n\tglobal_load_lds_dwordx4 %[v1], vcc offset:1024\n\t" \
  "global_load_lds_dwordx4 %[v2], vcc offset:2048\n\tglobal_load_lds_dwordx4 %[v3], vcc offset:3072\n\t" \
  "s_add_u32 vcc_lo, vcc_lo, " STEP "\n\ts_addc_u32 vcc_hi, vcc_hi, 0\n\t"
#define SLOTC_LEVEL(W) \
  "s_mov_b64 vcc, %[src]\n\t" SLOTC_GROUP(W, "0x1000") SLOTC_GROUP(W, "%[mid]") SLOTC_GROUP(W, "0x1000") SLOTC_GROUP(W, "0") "s_branch 9f\n\t"
__device__ __forceinline__ void issue_slot_c(unsigned v0, unsigned v1, unsigned v2, unsigned v3, const char* src, int mid, unsigned lds,
                                           int w, int cand, int& pub, unsigned va) {
  int jump;
  asm volatile("s_mul_i32 %[jmp], %[lvl], 8f-7f\n\ts_getpc_b64 vcc\n6:\n\ts_add_u32 %[jmp], %[jmp], 7f-6b\n\t"
               "s_add_u32 vcc_lo, vcc_lo, %[jmp]\n\ts_addc_u32 vcc_hi, vcc_hi, 0\n\ts_setpc_b64 vcc\n7:\n\t"
               SLOTC_LEVEL(12) "8:\n\t" SLOTC_LEVEL(16) SLOTC_LEVEL(20) SLOTC_LEVEL(24) SLOTC_LEVEL(28) SLOTC_LEVEL(32) "9:"
               : [lds] "+&s"(lds), [cand] "+&s"(cand), [pub] "+&s"(pub), [jmp] "=&s"(jump)
               : [v0] "v"(v0), [v1] "v"(v1), [v2] "v"(v2), [v3] "v"(v3), [src] "s"(src), [mid] "s"(mid), [lvl] "s"(w - 3), [va] "v"(va)
               : "v99", "vcc", "scc", "memory");
}
__device__ __forceinline__ int opaque(int v) { asm volatile("" : "+s"(v)); return v; }
__device__ __forceinline__ void wait_lgkm() { asm volatile("s_waitcnt lgkmcnt(0)" ::: "memory"); }

typedef volatile LAS unsigned* ctrl_t;
template <int SLEEP = 0> __device__ __forceinline__ unsigned lds_wait_ge(ctrl_t ctrl, int word, unsigned want, int& budget) {
  unsigned v, tmp;
  int go;
  budget = __builtin_amdgcn_readfirstlane(budget);
  want = __builtin_amdgcn_readfirstlane(want);
  asm volatile("1:\n\tds_read_b32 %[t], %[a]\n\t.if %[slp]\n\ts_sleep %[slp]\n\t.endif\n\ts_waitcnt lgkmcnt(0)\n\tv_readfirstlane_b32 %[v], %[t]\n\t"
               "s_cmp_lt_u32 %[v], %[w]\n\ts_cselect_b32 %[g], %[b], 0\n\ts_subb_u32 %[b], %[b], 0\n\ts_cmp_gt_i32 %[g], 0\n\ts_cbranch_scc1 1b"
               : [v] "=&s"(v), [t] "=&v"(tmp), [g] "=&s"(go), [b] "+&s"(budget)
               : [a] "v"((unsigned)(uintptr_t)(ctrl + word)), [w] "s"(want), [slp] "i"(SLEEP) : "scc", "memory");
  return v;
}
#define BOX_LOOP_END \
  "v_cmpx_eq_u32 vcc, -1, %[t]\n\ts_sub_i32 %[b], %[b], 1\n\ts_cmp_gt_i32 %[b], 0\n\ts_cselect_b64 vcc, exec, 0\n\t" \
  "s_cmp_lg_u64 vcc, 0\n\ts_cbranch_scc0 2f\n\t.if %[slp]\n\ts_sleep %[slp]\n\t.endif\n\ts_branch 1b\n2:\n\ts_mov_b64 exec, -1"
template <int SLEEP> __device__ __forceinline__ v4u poll_box(__amdgpu_buffer_rsrc_t rs, int off, int& budget) {
  v4u a;
  unsigned tmp;
  budget = __builtin_amdgcn_readfirstlane(budget);
  asm volatile("1:\n\tbuffer_load_dwordx4 v[100:103], %[o], %[rs], 0 offen sc1\n\ts_waitcnt vmcnt(0)\n\t"
               "v_max_u32 %[t], v100, v101\n\tv_max3_u32 %[t], %[t], v102, v103\n\t" BOX_LOOP_END
               : "=&{v[100:103]}"(a), [t] "=&v"(tmp), [b] "+&s"(budget) : [o] "v"(off), [rs] "s"(rs), [slp] "i"(SLEEP) : "vcc", "scc", "memory");
  return a;
}
template <int SLEEP> __device__ __forceinline__ void poll_box2(__amdgpu_buffer_rsrc_t rs, int off0, int off1, v4u& a, v4u& b, int& budget) {
  unsigned tmp;
  budget = __builtin_amdgcn_readfirstlane(budget);
  asm volatile("1:\n\tbuffer_load_dwordx4 v[100:103], %[o0], %[rs], 0 offen sc1\n\tbuffer_load_dwordx4 v[104:107], %[o1], %[rs], 0 offen sc1\n\t"
               "s_waitcnt vmcnt(0)\n\tv_max_u32 %[t], v100, v101\n\tv_max3_u32 %[t], %[t], v102, v103\n\t"
               "v_max3_u32 %[t], %[t], v104, v105\n\tv_max3_u32 %[t], %[t], v106, v107\n\t" BOX_LOOP_END
               : "=&{v[100:103]}"(a), "=&{v[104:107]}"(b), [t] "=&v"(tmp), [b] "+&s"(budget)
               : [o0] "v"(off0), [o1] "v"(off1), [rs] "s"(rs), [slp] "i"(SLEEP) : "vcc", "scc", "memory");
}
__device__ __forceinline__ void poll_lds_o(unsigned addr, v4u& a, v4u& b, int& budget) {
  unsigned tmp;
  budget = __builtin_amdgcn_readfirstlane(budget);
  asm volatile("1:\n\tds_read_b64 v[100:101], %[o]\n\tds_read_b64 v[102:103], %[o] offset:32\n\tds_read_b64 v[104:105], %[o] offset:64\n\t"
               "ds_read_b64 v[106:107], %[o] offset:96\n\ts_waitcnt lgkmcnt(0)\n\tv_max_u32 %[t], v100, v101\n\tv_max3_u32 %[t], %[t], v102, v103\n\t"
               "v_max3_u32 %[t], %[t], v104, v105\n\tv_max3_u32 %[t], %[t], v106, v107\n\t" BOX_LOOP_END
               : "=&{v[100:103]}"(a), "=&{v[104:107]}"(b), [t] "=&v"(tmp), [b] "+&s"(budget) : [o] "v"(addr), [slp] "i"(0) : "vcc", "scc", "memory");
}

typedef unsigned v16u __attribute__((ext_vector_type(16)));
__device__ __forceinline__ char* as_ptr(unsigned lo, unsigned hi) { return (char*)(((u64)hi << 32) | lo); }

__global__ void __launch_bounds__(448, 2) fused_decode(const char*, const char*, const char*, const char*, const char*, const char*, const char*,
                                                    char*, char*) {
  v16u ka16;
  v2u ka2;
  asm volatile("s_load_dwordx16 s[36:51], %2, 0x0\n\ts_load_dwordx2 s[52:53], %2, 0x40"
               : "={s[36:51]}"(ka16), "={s[52:53]}"(ka2) : "s"(__builtin_amdgcn_kernarg_segment_ptr()));
  __builtin_amdgcn_sched_barrier(0);
  extern __shared__ __attribute__((aligned(16))) char smem[];
  LAS char* L = (LAS char*)smem;
  const unsigned lbase = (unsigned)(uintptr_t)L;
  ctrl_t ctrl = (ctrl_t)(L + L_CTRL);
  const int lane = threadIdx.x & 63, bid = blockIdx.x;
  int wave = __builtin_amdgcn_readfirstlane(threadIdx.x >> 6);
  const int xg = bid & 7, tg = bid >> 3, kvh = tg >> 2, ii = tg & 3, gi = 4 * xg + ii, wid = kvh * 32 + gi, bb = gi >> 4;
  const int nt = ii == 0 ? 33 : ii == 1 ? 31 : 32, tile0 = (xg & 3) * 128 + (ii == 1 ? 33 : 32 * ii);
  const int g_kv = 8 * nt, g_wo = g_kv + 4;
  const int g = lane >> 4, col = lane & 15, q4 = (lane >> 2) & 3, p4 = lane & 3;
  const int ffn = xg * 8 + kvh;
  const bool has_ffn = ii == 1;
  unsigned vk[4], vx = lane * 16, vq = (lane >> 1) * 8192 + (lane & 1) * 16;
#pragma unroll
  for (int j = 0; j < 4; ++j) vk[j] = g * 256 + ((col ^ (4 * j + g)) * 16);
  unsigned kvoff = ((bb * 8 + kvh) * SK + tile0 * 64) * 256, qoff = (kvh * 512 + gi * 16) * 2;
  asm volatile("" : "+v"(vk[0]), "+v"(vk[1]), "+v"(vk[2]), "+v"(vk[3]), "+v"(vx), "+v"(vq));
  asm volatile("" : "+s"(kvoff), "+s"(qoff), "+s"(wave));
  ctrl[lane] = 0u;
  ctrl[C_LANDED + lane] = 0u;
  *(volatile LAS unsigned*)(L + L_O + lane * 4) = SENT;
  asm volatile("s_waitcnt lgkmcnt(0)\n\ts_barrier" : "+{s[36:51]}"(ka16), "+{s[52:53]}"(ka2) :: "memory");
  const char *x = as_ptr(ka16[0], ka16[1]), *kc = as_ptr(ka16[2], ka16[3]), *vc = as_ptr(ka16[4], ka16[5]), *wq = as_ptr(ka16[6], ka16[7]),
             *wo = as_ptr(ka16[8], ka16[9]), *w1 = as_ptr(ka16[10], ka16[11]), *w2 = as_ptr(ka16[12], ka16[13]);
  char *out = as_ptr(ka16[14], ka16[15]), *ws = as_ptr(ka2[0], ka2[1]);
  const __amdgpu_buffer_rsrc_t rs = __builtin_amdgcn_make_buffer_rsrc((void*)ws, 0, WS_END, 0x00020000);
  const unsigned t_start = (unsigned)__builtin_amdgcn_s_memrealtime();
  if (wave == 1) {
    __builtin_amdgcn_s_setprio(3);
    const u64 pk_ = (u64)(kc + kvoff), pv_ = (u64)(vc + kvoff), pq_ = (u64)(wq + qoff), pq2 = pq_ + 511 * 8192;
    asm volatile("v_writelane_b32 v100, %0, 0\n\tv_writelane_b32 v101, %1, 0\n\tv_writelane_b32 v100, %2, 1\n\tv_writelane_b32 v101, %3, 1\n\t"
                 "v_writelane_b32 v100, %4, 2\n\tv_writelane_b32 v101, %5, 2\n\tv_writelane_b32 v100, %6, 3\n\tv_writelane_b32 v101, %7, 3\n\t"
                 "v_writelane_b32 v100, %8, 4\n\tv_writelane_b32 v101, %9, 4\n\tv_writelane_b32 v100, %10, 5\n\tv_writelane_b32 v101, %11, 5\n\t"
                 "s_mov_b32 m0, %12\n\ts_mov_b64 exec, 63\n\tglobal_load_lds_dword v[100:101], off nt\n\ts_mov_b64 exec, -1"
                 :: "s"((unsigned)pk_), "s"((unsigned)(pk_ >> 32)), "s"((unsigned)pv_), "s"((unsigned)(pv_ >> 32)), "s"(ka16[0]), "s"(ka16[1]),
                    "s"(ka2[0]), "s"(ka2[1]), "s"((unsigned)pq_), "s"((unsigned)(pq_ >> 32)), "s"((unsigned)pq2), "s"((unsigned)(pq2 >> 32)),
                    "s"(lbase + L_NIL) : "v100", "v101", "memory");
#pragma unroll
    for (int j = 0; j < 4; ++j) dma4(vk[0], vk[1], vk[2], vk[3], kc + kvoff + j * 4096, lbase + j * 4096);
  }
  if (opaque(wave) == 0) {
    asm volatile("s_mov_b32 m0, %4\n\ts_nop 0\n\tglobal_load_lds_dwordx4 %0, %1\n\tglobal_load_lds_dwordx4 %0, %1 offset:1024\n\t"
                 "s_mov_b32 m0, %5\n\ts_nop 0\n\tglobal_load_lds_dwordx4 %2, %3 sc1"
                 :: "v"(vx), "s"(x), "v"(WS_EPOCH), "s"(ws), "s"(lbase + L_W), "s"(lbase + L_W + 2048) : "memory");
  }
  if (opaque(wave) == 6) {
#pragma nounroll
    for (int j = 0; j < 16; ++j) dma1(vq, wq + qoff + (size_t)j * (32 * 8192), lbase + L_S + j * 1024);
    wait_vm<0>();
    if (lane == 0) ctrl[C_WQ] = 1u;
  }
  const int role = opaque(wave);
  int budget = SPIN_BUDGET;

  if (__builtin_expect(role == 1, 1)) {
    unsigned vv[2];
#pragma unroll
    for (int j = 0; j < 2; ++j) vv[j] = g * 256 + ((col ^ ((4 * j + g) << 1)) * 16);
    const char* kb = kc + kvoff;
    const char* vb = vc + kvoff;
    const char* wb = wo + (size_t)(kvh * 512 + 32 * (gi >> 3) + 8 * ((gi >> 2) & 1)) * 1024 + ii * 256;
    const unsigned va_pub = lbase + L_CTRL + (C_LANDED + lane) * 4;
    const int w16 = W0 * 16 + (ii == 0 ? FF0 : ii == 1 ? FF1 : 0);
    int issued = 4, pub = 0, wl = W0 / 4, dith = 0;
#pragma nounroll
    for (int seq = 1; seq <= 2 * nt; ++seq) {
      {
        const v4u sc = *(volatile LAS v4u*)(ctrl + C_SCHED);
        v4u f = *(volatile LAS v4u*)(ctrl + C_FREED);
        const int el = (int)((unsigned)__builtin_amdgcn_s_memrealtime() - __builtin_amdgcn_readfirstlane(sc[0]));
        const int rate = __builtin_amdgcn_readfirstlane(sc[2]);
        const int lag16 = rate ? ((((int)__builtin_amdgcn_readfirstlane(sc[1]) + ((el * rate) >> 8)) * nt) >> 5) - pub * 16 : 0;
        const int wk = w16 + lag16 * KN / KD + dith;
        wl = __builtin_amdgcn_readfirstlane(min(max((wk + 32) >> 6, WMIN / 4), WMAX / 4));
        dith = min(max(wk - wl * 64, -32), 32);
        if ((int)__builtin_amdgcn_readfirstlane(min(min(f[0], f[1]), min(f[2], f[3]))) <= seq - NSLOT) {
          wait_vm<0>(); pub = issued; ctrl[C_LANDED + lane] = pub;
          do {
            __builtin_amdgcn_s_sleep(1);
            f = *(volatile LAS v4u*)(ctrl + C_FREED);
          } while ((int)__builtin_amdgcn_readfirstlane(min(min(f[0], f[1]), min(f[2], f[3]))) <= seq - NSLOT && --budget > 0);
        }
      }
      const bool is_wo = seq == 2 * nt, is_k = seq < nt;
#define DRAIN_TO(N) { wait_vm<4 * (N)>(); pub = max(pub, issued - (N)); ctrl[C_LANDED + lane] = pub; }
      if (is_wo) { DRAIN_TO(8) DRAIN_TO(7) DRAIN_TO(6) DRAIN_TO(5) DRAIN_TO(4) DRAIN_TO(3) DRAIN_TO(2) DRAIN_TO(1) DRAIN_TO(0) wl = 8; }
      unsigned v[4];
#pragma unroll
      for (int j = 0; j < 4; ++j) v[j] = is_k ? vk[j] : is_wo ? vv[j & 1] + g * 768 + j * (127 * 1024) : vv[j & 1];
      const char* src = is_wo ? wb : is_k ? kb + seq * SLOT : vb + (seq - nt) * SLOT;
      if (is_wo) issue_slot_c(v[0], v[1], v[2], v[3], src, is_wo ? 12288 : 4096, lbase + (seq & (NSLOT - 1)) * SLOT, wl, issued - wl, pub, va_pub);
      else
      issue_slot(v[0], v[1], v[2], v[3], src, is_wo ? 12288 : 4096, lbase + (seq & (NSLOT - 1)) * SLOT, wl, issued - wl, pub, va_pub);
      issued += 4;
    }
    DRAIN_TO(8) DRAIN_TO(7) DRAIN_TO(6) DRAIN_TO(5) DRAIN_TO(4) DRAIN_TO(0)
#undef DRAIN_TO
    return;
  }

  if (role == 0) {
    u64 tag;
    v4u xb[16];
    {
      wait_vm<0>();
      tag = *(volatile LAS u64*)(L + L_W + 2048) + 1;
      const v4u xv0 = *(volatile LAS v4u*)(L + L_W + lane * 32), xv1 = *(volatile LAS v4u*)(L + L_W + lane * 32 + 16);
      float v[16];
#pragma unroll
      for (int e = 0; e < 4; ++e) { v[2 * e] = bf_lo(xv0[e]); v[2 * e + 1] = bf_hi(xv0[e]); v[8 + 2 * e] = bf_lo(xv1[e]); v[9 + 2 * e] = bf_hi(xv1[e]); }
      float s4[4], q4s[4];
#pragma unroll
      for (int e = 0; e < 4; ++e) s4[e] = (v[e] + v[4 + e]) + (v[8 + e] + v[12 + e]);
      const float mean = half_sum((s4[0] + s4[1]) + (s4[2] + s4[3])) * (1.f / D);
#pragma unroll
      for (int e = 0; e < 4; ++e) {
        const float d0 = v[e] - mean, d1 = v[4 + e] - mean, d2 = v[8 + e] - mean, d3 = v[12 + e] - mean;
        q4s[e] = fmaf(d0, d0, d1 * d1) + fmaf(d2, d2, d3 * d3);
      }
      const float var = half_sum((q4s[0] + q4s[1]) + (q4s[2] + q4s[3])) * (1.f / D);
      const float mb = rbf(mean), den = rbf(__builtin_amdgcn_sqrtf(rbf(rbf(var) + 1e-5f))), rden = __builtin_amdgcn_rcpf(den);
      unsigned o[8];
#pragma unroll
      for (int e = 0; e < 8; ++e) {
        const unsigned dp = pk_bf16(v[2 * e] - mb, v[2 * e + 1] - mb);
        const float d0 = bf_lo(dp), d1 = bf_hi(dp), q0 = d0 * rden, q1 = d1 * rden;
        o[e] = pk_bf16(fmaf(fmaf(-q0, den, d0), rden, q0), fmaf(fmaf(-q1, den, d1), rden, q1));
      }
      *(LAS v4u*)(L + L_XN + lane * 32) = v4u{o[0], o[1], o[2], o[3]};
      *(LAS v4u*)(L + L_XN + lane * 32 + 16) = v4u{o[4], o[5], o[6], o[7]};
      asm volatile("" ::: "memory");
      LAS char* xa = L + L_XN + (((col >> 1) & 1) * 512 + 4 * g) * 2;
#pragma unroll
      for (int ks = 0; ks < 16; ++ks) xb[ks] = cat(*(LAS v2u*)(xa + ks * 64), *(LAS v2u*)(xa + ks * 64 + 32));
      __builtin_amdgcn_sched_barrier(0);
      lds_wait_ge(ctrl, C_WQ, 1u, budget);
      f32x4 qp[2] = {{0.f, 0.f, 0.f, 0.f}, {0.f, 0.f, 0.f, 0.f}};
      LAS char* wa = L + L_S + 8 * lane;
      v4u fa[16];
#pragma unroll
      for (int ks = 0; ks < 16; ++ks) fa[ks] = cat(lds_tr(wa + ks * 1024), lds_tr(wa + ks * 1024 + 512));
      __builtin_amdgcn_sched_barrier(0);
#pragma unroll
      for (int ks = 0; ks < 16; ++ks) qp[ks & 1] = mfma32(fa[ks], xb[ks], qp[ks & 1]);
      const f32x4 qa = qp[0] + qp[1];
      if ((col & 13) == 0) *(LAS v2u*)(L + L_Q + (col >> 1) * 32 + g * 8) = v2u{pk_bf16(qa[0], qa[1]), pk_bf16(qa[2], qa[3])};
      asm volatile("" ::: "memory");
      const unsigned thi = (unsigned)(tag >> 16), tlo = (unsigned)tag << 16;
      const unsigned qv = *(LAS unsigned short*)(L + L_Q + (lane & 31) * 2);
      __builtin_amdgcn_raw_buffer_store_b64(v2u{tlo | qv, thi}, rs, lane < 32 ? WS_QG + ((kvh * 2 + g) * 512 + gi * 16 + col) * 8 : OOB, 0, AUX_SC1);
      if (lane == 0) *(volatile LAS u64*)(ctrl + C_TAG) = tag;
    }
    {
      const unsigned thi = (unsigned)(tag >> 16), tlo = (unsigned)tag << 16;
      while ((int)((unsigned)__builtin_amdgcn_s_memrealtime() - t_start) < QDELAY) __builtin_amdgcn_s_sleep(2);
      const int qo = WS_QG + (kvh * 2 + bb) * 4096 + lane * 8;
      v2u a[8];
#pragma unroll
      for (int k = 0; k < 8; ++k) a[k] = v2u{~tlo, ~thi};
      for (;;) {
        asm volatile("" ::: "memory");
        bool all = true;
        v2u nv[8];
#pragma unroll
        for (int k = 0; k < 8; ++k) {
          const bool ok = a[k][1] == thi && ((a[k][0] ^ tlo) >> 16) == 0;
          nv[k] = __builtin_amdgcn_raw_buffer_load_b64(rs, ok ? OOB : qo + 512 * k, 0, AUX_SC1);
        }
#pragma unroll
        for (int k = 0; k < 8; ++k) {
          const bool ok = a[k][1] == thi && ((a[k][0] ^ tlo) >> 16) == 0;
          a[k] = ok ? a[k] : nv[k];
          all = all && a[k][1] == thi && ((a[k][0] ^ tlo) >> 16) == 0;
        }
        if (__all(all)) break;
        if (--budget <= 0) break;
        __builtin_amdgcn_s_sleep(4);
      }
#pragma unroll
      for (int k = 0; k < 8; ++k) *(LAS unsigned short*)(L + L_Q + (64 * k + lane) * 2) = (unsigned short)a[k][0];
      wait_lgkm();
      if (lane == 0) ctrl[C_Q] = 1u;
    }
    auto ffn_slice = [&]() {
      const int u0 = (28 * ffn) & ~7, lo = 28 * ffn - u0;
      const unsigned vo1 = (lane >> 2) * (FF * 2) + ((p4 ^ ((lane >> 3) & 2)) * 16);
      LAS char* wa = L + L_W + (4 * g + q4) * 64 + 8 * p4;
      f32x4 h0 = {0.f, 0.f, 0.f, 0.f}, h1 = h0;
#pragma unroll
      for (int rd = 0; rd < 4; ++rd) {
#pragma unroll
        for (int j = 0; j < 8; ++j) dma1(vo1, w1 + u0 * 2 + (size_t)(rd * 128 + j * 16) * (FF * 2), lbase + L_W + j * 1024);
        wait_vm<0>();
        v4u fa[4][2];
#pragma unroll
        for (int ks = 0; ks < 4; ++ks)
#pragma unroll
          for (int nb = 0; nb < 2; ++nb) {
            LAS char* p = wa + ks * 2048 + ((nb ^ (g & 1)) * 32);
            fa[ks][nb] = cat(lds_tr(p), lds_tr(p + 1024));
          }
        __builtin_amdgcn_sched_barrier(0);
        wait_lgkm();
#pragma unroll
        for (int ks = 0; ks < 4; ++ks) { h0 = mfma32(fa[ks][0], xb[4 * rd + ks], h0); h1 = mfma32(fa[ks][1], xb[4 * rd + ks], h1); }
      }
      float hv[8];
#pragma unroll
      for (int j = 0; j < 8; ++j) {
        const int u = (j >> 2) * 16 + 4 * g + (j & 3);
        hv[j] = (u >= lo && u < lo + 28) ? silu(rbf(j < 4 ? h0[j & 3] : h1[j & 3])) : 0.f;
      }
      const v4u hb = {pk_bf16(hv[0], hv[1]), pk_bf16(hv[2], hv[3]), pk_bf16(hv[4], hv[5]), pk_bf16(hv[6], hv[7])};
      const unsigned vo2[2] = {(unsigned)(g * 1024 + ((col ^ (g << 1)) * 16)), (unsigned)(g * 1024 + ((col ^ ((4 + g) << 1)) * 16))};
      const int sw = (4 * (g & 1) + q4) << 1;
      LAS char* w2a = L + L_W + (4 * g + q4) * 256 + 8 * (p4 & 1);
      const int mine = 2 * (col >> 2) + (col & 1);
#pragma unroll
      for (int cb = 0; cb < 4; ++cb) {
#pragma unroll
        for (int j = 0; j < 8; ++j) dma1(vo2[j & 1], w2 + (size_t)(u0 + 4 * j) * 1024 + cb * 256, lbase + L_W + j * 1024);
        wait_vm<0>();
        v4u wf[8];
#pragma unroll
        for (int nb = 0; nb < 8; ++nb) {
          LAS char* p = w2a + (((2 * nb + (p4 >> 1)) ^ sw) * 16);
          wf[nb] = cat(lds_tr(p), lds_tr(p + 4096));
        }
        __builtin_amdgcn_sched_barrier(0);
        wait_lgkm();
        f32x4 fo = {0.f, 0.f, 0.f, 0.f};
#pragma unroll
        for (int nb = 0; nb < 8; ++nb) fo = mfma32(wf[nb], mine == nb ? hb : v4u{0u, 0u, 0u, 0u}, fo);
        __builtin_amdgcn_raw_buffer_store_b128(not_sent(__builtin_bit_cast(v4u, fo)), rs, WS_PF + ffn * 4096 + cb * 1024 + lane * 16, 0, AUX_SC1);
      }
    };
    if (has_ffn) {
      ffn_slice();
      wait_vm<0>();
      if (lane == 0) __hip_atomic_store((GAS u64*)(ws + WS_FOK) + ffn, tag, RLX_AGENT);
    }

    lds_wait_ge<8>(ctrl, C_ALL, 1u, budget);
    const int cb = xg >> 1, pc = (xg & 1) * 32 + tg, cw = pc >> 4, gw = (pc >> 2) & 3;
    const int jj = (g >> 1) | ((g & 1) << 1), oidx = ((pc >> 1) & 1) * 512 + cb * 128 + cw * 32 + (pc & 1) * 16 + gw * 4 + jj;
    const float xin = __uint_as_float((unsigned)*(const GAS unsigned short*)(x + oidx * 2) << 16);
    float gate = 0.f;
    {
      const unsigned t0 = *(const GAS unsigned short*)(out + oidx * 2), t1 = *(const GAS unsigned short*)(wo + (size_t)(kvh * 512 + g * 128 + 4 * gi) * 1024);
      asm volatile("" :: "v"(t0), "v"(t1));
    }
#pragma nounroll
    for (int pass = 0; pass < 2; ++pass) {
      const bool last = opaque(pass) != 0;
      if (last) { lds_wait_ge<8>(ctrl, C_LANDED, g_kv - 4, budget); lds_wait_ge(ctrl, C_PUB, 1u, budget); }
      else {
        lds_wait_ge<8>(ctrl, C_LANDED, PFAT, budget);
        while (!__all(__hip_atomic_load((GAS u64*)(ws + WS_FOK) + lane, RLX_AGENT) == tag) && --budget > 0) __builtin_amdgcn_s_sleep(8);
      }
      const int po = (last ? WS_P2 + pc * 16 : WS_PF + gw * 256 + cw * 64 + (pc & 3) * 16) + lane * 4096 + cb * 1024;
      const v4u sv = poll_box<0>(rs, po, budget);
      float t = rows_sum4(__uint_as_float(sv[0]), __uint_as_float(sv[1]), __uint_as_float(sv[2]), __uint_as_float(sv[3]));
      t += dpp<XOR1>(t); t += dpp<XOR2>(t); t += dpp<ROR8>(t); t += dpp<ROR4>(t);
      t = rbf(t);
      if (last) {
        const float y = xin + rbf(t * gate);
        const __amdgpu_buffer_rsrc_t ro = __builtin_amdgcn_make_buffer_rsrc((void*)out, 0, 2 * D * 2, 0x00020000);
        __builtin_amdgcn_raw_buffer_store_b16(budget <= 0 ? (unsigned short)0x7fc0 : (unsigned short)pk_bf16(y, y), ro, col == 0 ? oidx * 2 : OOB, 0, 0);
        break;
      }
      gate = rbf(silu(t));
    }
    return;
  }

  if (role == 6) {
    unsigned at_t = 0;
    int at_p = 0;
    lds_wait_ge<8>(ctrl, C_Q, 1u, budget);
    const u64 tag = *(volatile LAS u64*)(ctrl + C_TAG);
    lds_wait_ge<8>(ctrl, C_CLR, 4u, budget);
#pragma nounroll
    for (int k = 0; k < NROUND; ++k) {
      GAS u64* line = (GAS u64*)(ws + WS_TAU) + (k ? k * 256 : kvh * 32);
      const int at = MARK + k * MSTEP, mark = (at * nt) >> 5;
      lds_wait_ge<1>(ctrl, C_LANDED, mark, budget);
      const unsigned tau = (unsigned)__builtin_amdgcn_s_memrealtime();
      if (lane == 0 && (k == 0 || ii == 3)) __hip_atomic_store(line + (k ? ffn : gi), (tag << 16) | (tau & 0xffffu), RLX_AGENT);
      lds_wait_ge<4>(ctrl, C_LANDED, mark + MDEL, budget);
      const u64 v = __hip_atomic_load(line + (lane & (k ? 63 : 31)), RLX_AGENT);
      const unsigned seen_t = ((v ^ (tag << 16)) >> 16) == 0 ? (unsigned)v : (unsigned)__builtin_amdgcn_s_memrealtime();
      const float d = wave_sum((float)(short)(seen_t - tau));
      const unsigned mean = tau + __builtin_amdgcn_readfirstlane((int)(d * (1.f / 64)));
      constexpr int rate = 65536 / TG16;
      const int common = k ? at_p + (((int)(mean - at_t) * rate) >> 8) - at * 16 : LAG0 * 16;
      at_t = mean;
      at_p = at * 16 + min(max(common, 0), LAGHI * 16);
      if (lane == 0) *(volatile LAS v4u*)(ctrl + C_SCHED) = v4u{at_t, (unsigned)at_p, (unsigned)rate, 0u};
      if (k == 0) {
        while (!__all(((__hip_atomic_load(line + (lane & 31), RLX_AGENT) ^ (tag << 16)) >> 16) == 0) && --budget > 0) __builtin_amdgcn_s_sleep(8);
        if (lane == 0) ctrl[C_FLAG] = 1u;
        if (gi == 0 && lane == 0) __hip_atomic_store((GAS u64*)(ws + WS_GOK) + kvh, tag, RLX_AGENT);
      } else if (k == 1) {
        while (!__all(__hip_atomic_load((GAS u64*)(ws + WS_GOK) + (lane & 7), RLX_AGENT) == tag) && --budget > 0) __builtin_amdgcn_s_sleep(8);
        if (lane == 0) ctrl[C_ALL] = 1u;
        if (bid == 0 && lane == 0) __hip_atomic_store((GAS u64*)(ws + WS_EPOCH), tag, RLX_AGENT);
      }
    }
    return;
  }

  const int c = role - 2;
  const int blk = col >> 2;
  constexpr float SCALE = 0.08838834764831845f, LOG2E = 1.4426950408889634f;
  unsigned seen = 0u;
  auto need = [&](unsigned want) {
    if (seen < want) seen = lds_wait_ge(ctrl, C_LANDED, want, budget);
    asm volatile("" ::: "memory");
  };
  float ms;
  {
    unsigned ka[4];
#pragma unroll
    for (int k = 0; k < 4; ++k) ka[k] = col * 256 + (((4 * k + g) ^ col) * 16);
    v4u kf[4][4];
    auto fetch_k = [&](int t) {
      need(4 * t + 4);
      LAS char* sb = L + (t & (NSLOT - 1)) * SLOT;
#pragma unroll
      for (int k = 0; k < 4; ++k)
#pragma unroll
        for (int a = 0; a < 4; ++a) kf[a][k] = *(LAS v4u*)(sb + ka[k] + a * 4096);
      __builtin_amdgcn_sched_barrier(0);
      wait_lgkm();
      if (lane == 0) ctrl[C_FREED + c] = min(t + 4, nt);
      __builtin_amdgcn_sched_barrier(0);
    };
    fetch_k(c);
    const v4u sv = {SENT, SENT, SENT, SENT};
    if (c < 3) __builtin_amdgcn_raw_buffer_store_b128(sv, rs, (c < 2 ? WS_P1 + wid * 2048 + c * 1024 : WS_P2 + wid * 1024) + lane * 16, 0, AUX_SC1);
    wait_vm<0>();
    if (lane == 0) __hip_atomic_fetch_add((LAS unsigned*)(ctrl + C_CLR), 1u, __ATOMIC_RELAXED, __HIP_MEMORY_SCOPE_WORKGROUP);
    lds_wait_ge<2>(ctrl, C_Q, 1u, budget);
    v4u qf[4][4];
#pragma unroll
    for (int k = 0; k < 4; ++k) {
      const v4u qv = *(LAS v4u*)(L + L_Q + ((col & 3) * 128 + 32 * k + 8 * g) * 2);
#pragma unroll
      for (int a = 0; a < 4; ++a) qf[a][k] = blk == a ? qv : v4u{0u, 0u, 0u, 0u};
    }
    float mx = -3.0e38f;
#pragma nounroll
    for (int t = c;; t += 4) {
      f32x4 s0 = {0.f, 0.f, 0.f, 0.f}, s1 = {0.f, 0.f, 0.f, 0.f};
#pragma unroll
      for (int k = 0; k < 4; ++k) {
        s0 = mfma32(kf[0][k], qf[0][k], s0); s1 = mfma32(kf[1][k], qf[1][k], s1);
        s0 = mfma32(kf[2][k], qf[2][k], s0); s1 = mfma32(kf[3][k], qf[3][k], s1);
      }
      s0 += s1;
      const unsigned a0 = pk_bf16(s0[0], s0[1]), a1 = pk_bf16(s0[2], s0[3]);
      const unsigned b0 = pk_bf16(bf_lo(a0) * SCALE, bf_hi(a0) * SCALE), b1 = pk_bf16(bf_lo(a1) * SCALE, bf_hi(a1) * SCALE);
      *(LAS v2u*)(L + L_S + t * 512 + lane * 8) = v2u{b0, b1};
      mx = max2(max2(mx, max2(bf_lo(b0), bf_hi(b0))), max2(bf_lo(b1), bf_hi(b1)));
      if (t + 4 >= nt) break;
      fetch_k(t + 4);
    }
    mx = max2(mx, dpp<ROR8>(mx)); mx = max2(mx, dpp<ROR4>(mx)); mx = xrow_max(mx);
    if (lane < 4) *(LAS float*)(L + L_M + lane * 16 + c * 4) = mx;
    wait_lgkm();
    if (lane == 0) __hip_atomic_fetch_add((LAS unsigned*)(ctrl + C_MAX), 1u, __ATOMIC_RELAXED, __HIP_MEMORY_SCOPE_WORKGROUP);
    lds_wait_ge(ctrl, C_MAX, 4u, budget);
    const f32x4 m4 = *(LAS f32x4*)(L + L_M + (col & 3) * 16);
    ms = max2(max2(m4[0], m4[1]), max2(m4[2], m4[3])) * LOG2E;
  }
  unsigned va[2];
#pragma unroll
  for (int n = 0; n < 2; ++n)
    va[n] = (4 * g + q4) * 256 + (((4 * c + 2 * n + (p4 >> 1)) ^ ((4 * (g & 1) + q4) << 1)) * 16) + 8 * (p4 & 1);
  float lsum = 0.f, linv = 0.f;
  unsigned lam = 0;
  f32x4 acc0 = {0.f, 0.f, 0.f, 0.f}, acc1 = {0.f, 0.f, 0.f, 0.f};
#pragma nounroll
  for (int t = 0; t < nt; ++t) {
    const int seq = nt + t;
    const v2u sc = *(LAS v2u*)(L + L_S + t * 512 + lane * 8);
    const float e0 = __builtin_amdgcn_exp2f(fmaf(bf_lo(sc[0]), LOG2E, -ms)), e1 = __builtin_amdgcn_exp2f(fmaf(bf_hi(sc[0]), LOG2E, -ms));
    const float e2 = __builtin_amdgcn_exp2f(fmaf(bf_lo(sc[1]), LOG2E, -ms)), e3 = __builtin_amdgcn_exp2f(fmaf(bf_hi(sc[1]), LOG2E, -ms));
    lsum += (e0 + e1) + (e2 + e3);
    const unsigned p0 = pk_bf16(e0, e1), p1 = pk_bf16(e2, e3);
    v4u pA = {blk == 0 ? p0 : 0u, blk == 0 ? p1 : 0u, blk == 1 ? p0 : 0u, blk == 1 ? p1 : 0u};
    v4u pB = {blk == 2 ? p0 : 0u, blk == 2 ? p1 : 0u, blk == 3 ? p0 : 0u, blk == 3 ? p1 : 0u};
    if (t == nt - 1) {
      lds_wait_ge(ctrl, C_FLAG, 1u, budget);
      const float lt = quad_stride_sum(lsum);
      linv = __builtin_amdgcn_rcpf(lt);
      lam = __float_as_uint(ms + __builtin_amdgcn_logf(lt));
    }
    asm volatile("" : "+v"(pA), "+v"(pB), "+v"(lsum), "+v"(linv), "+v"(lam));
    LAS char* sb = L + (seq & (NSLOT - 1)) * SLOT;
    need(4 * seq + 2);
    acc0 = mfma32(cat(lds_tr(sb + va[0]), lds_tr(sb + va[0] + 4096)), pA, acc0);
    acc1 = mfma32(cat(lds_tr(sb + va[1]), lds_tr(sb + va[1] + 4096)), pA, acc1);
    need(4 * seq + 4);
    v4u v0 = cat(lds_tr(sb + va[0] + 8192), lds_tr(sb + va[0] + 12288)), v1 = cat(lds_tr(sb + va[1] + 8192), lds_tr(sb + va[1] + 12288));
    wait_lgkm();
    if (lane == 0) ctrl[C_FREED + c] = seq + 1;
    asm volatile("" : "+v"(v0), "+v"(v1) :: "memory");
    acc0 = mfma32(v0, pB, acc0);
    acc1 = mfma32(v1, pB, acc1);
  }

  v4u pk;
#pragma unroll
  for (int j = 0; j < 4; ++j) {
    float a = ((col & 4) ? acc1[j] : acc0[j]) + dpp<ROR4>((col & 4) ? acc0[j] : acc1[j]);
    a += dpp<ROR8>(a);
    pk[j] = ((__float_as_uint(a * linv) + 0x80u) & 0xffffff00u) | ((lam >> (8 * j)) & 0xffu);
  }
  __builtin_amdgcn_raw_buffer_store_b128(not_sent(pk), rs, col < 8 ? WS_P1 + wid * 2048 + c * 512 + g * 128 + col * 16 : OOB, 0, AUX_SC1);
  const int rec = WS_P1 + (kvh * 32 + (c >> 1) * 16 + (lane >> 3)) * 2048 + (gi >> 2) * 256 + (c & 1) * 128 + (lane & 7) * 16;
  v4u a0, a1, wa[4];
  need(g_wo);
  LAS char* wp = L + ((2 * nt) & (NSLOT - 1)) * SLOT;
#pragma unroll
  for (int n = 0; n < 4; ++n) wa[n] = cat(lds_tr(wp + va[n & 1] + (n >> 1) * 4096), lds_tr(wp + va[n & 1] + (n >> 1) * 4096 + 8192));
  __builtin_amdgcn_sched_barrier(0);
  poll_box2<0>(rs, rec, rec + 8 * 2048, a0, a1, budget);
  const float lA = __uint_as_float(__builtin_amdgcn_perm(__builtin_amdgcn_perm(a0[3], a0[2], 0x04000c0cu), __builtin_amdgcn_perm(a0[1], a0[0], 0x0c0c0400u), 0x07060100u));
  const float lB = __uint_as_float(__builtin_amdgcn_perm(__builtin_amdgcn_perm(a1[3], a1[2], 0x04000c0cu), __builtin_amdgcn_perm(a1[1], a1[0], 0x0c0c0400u), 0x07060100u));
  float M = max2(lA, lB);
  M = xrow_max(max2(M, dpp<ROR8>(M)));
  const float wA = __builtin_amdgcn_exp2f(lA - M), wB = __builtin_amdgcn_exp2f(lB - M);
  float r[4];
#pragma unroll
  for (int j = 0; j < 4; ++j) r[j] = fmaf(wA, __uint_as_float(a0[j] & 0xffffff00u), wB * __uint_as_float(a1[j] & 0xffffff00u));
  float wsum = xrow_sum(wA + wB), ov = rows_sum4(r[0], r[1], r[2], r[3]);
  wsum += dpp<ROR8>(wsum); ov += dpp<ROR8>(ov);
  ov *= __builtin_amdgcn_rcpf(wsum);
  const auto pr = __builtin_amdgcn_permlane32_swap(__float_as_uint(ov), __float_as_uint(ov), false, false);
  if ((lane & 40) == 0) *(LAS unsigned*)(L + L_O + c * 64 + col * 8 + g * 4) = min(pk_bf16(__uint_as_float(pr[0]), __uint_as_float(pr[1])), SENT - 1);
  v4u o0, o1;
  poll_lds_o(lbase + L_O + ((col >> 1) & 1) * 128 + g * 8, o0, o1, budget);
  const f32x4 zero = {0.f, 0.f, 0.f, 0.f};
  const f32x4 sp0 = mfma32(wa[2], o1, mfma32(wa[0], o0, zero)), sp1 = mfma32(wa[3], o1, mfma32(wa[1], o0, zero));
  const f32x4 sp = (col & 1) ? sp1 : sp0;
  __builtin_amdgcn_raw_buffer_store_b128(not_sent(__builtin_bit_cast(v4u, sp)), rs, col < 4 ? WS_P2 + wid * 1024 + c * 256 + g * 64 + col * 16 : OOB, 0, AUX_SC1);
  if (lane == 0) ctrl[C_PUB] = 1u;
}

extern "C" void kernel_launch(void* const* d_in, const int* in_sizes, int n_in, void* d_out, int out_size, void* d_ws, size_t ws_size,
                              hipStream_t stream) {
  (void)hipFuncSetAttribute((const void*)fused_decode, hipFuncAttributeMaxDynamicSharedMemorySize, L_BYTES);
  hipLaunchKernelGGL(fused_decode, dim3(NWG), dim3(448), L_BYTES, stream, (const char*)d_in[0], (const char*)d_in[1], (const char*)d_in[2],
                     (const char*)d_in[3], (const char*)d_in[4], (const char*)d_in[6], (const char*)d_in[7], (char*)d_out, (char*)d_ws);
}
```

```cpp
#include <hip/hip_runtime.h>
#include <hip/hip_bf16.h>
#include <stdint.h>

#define GAS __attribute__((address_space(1)))
#define LAS __attribute__((address_space(3)))
#define RLX_AGENT __ATOMIC_RELAXED, __HIP_MEMORY_SCOPE_AGENT
typedef unsigned long long u64;
typedef unsigned v4u __attribute__((ext_vector_type(4)));
typedef unsigned v2u __attribute__((ext_vector_type(2)));
typedef float f32x4 __attribute__((ext_vector_type(4)));
typedef float f32x2 __attribute__((ext_vector_type(2)));
typedef __bf16 bf16x8 __attribute__((ext_vector_type(8)));
typedef __bf16 bf16x4 __attribute__((ext_vector_type(4)));
typedef __bf16 bf16x2 __attribute__((ext_vector_type(2)));

constexpr int W0 = 16, WMIN = 12, WMAX = 32;
constexpr int KN = 3, KD = 2;
constexpr int FF0 = 32, FF1 = -16;
constexpr int TG16 = 218;
constexpr int NROUND = 4, MARK = 32, MSTEP = 58, MDEL = 14;
constexpr int LAG0 = 0, LAGHI = 2;
constexpr int QDELAY = 370, PFAT = 176;

constexpr int D = 512, SK = 32768, FF = 1792, NWG = 256, NSLOT = 8, SLOT = 16384;
constexpr unsigned SENT = 0xFFFFFFFFu;
constexpr int AUX_SC1 = 16, OOB = 0x7ffffff0;

constexpr int WS_EPOCH = 0, WS_GOK = 4096, WS_FOK = 4608, WS_TAU = 8192, WS_QG = 32768, WS_P1 = WS_QG + 65536, WS_P2 = WS_P1 + NWG * 2048,
              WS_PF = WS_P2 + NWG * 1024, WS_END = WS_PF + 64 * 4096;

constexpr int L_S = NSLOT * SLOT, L_CTRL = L_S + 33 * 512, L_XN = L_CTRL + 512, L_Q = L_XN + 2048, L_O = L_Q + 1024, L_M = L_O + 256,
              L_NIL = L_M + 64, L_W = L_NIL + 256, L_BYTES = L_W + 8192;
constexpr int C_LANDED = 64, C_ALL = 0, C_WQ = 1, C_Q = 2, C_CLR = 3, C_FREED = 4, C_PUB = 8, C_FLAG = 9, C_TAG = 10, C_SCHED = 12, C_MAX = 16;
static_assert(NROUND >= 2 && WS_TAU + NROUND * 2048 <= WS_QG && MARK + (NROUND - 1) * MSTEP + MDEL <= 248 && L_BYTES <= 163840, "");
constexpr int SPIN_BUDGET = 1 << 20;

__device__ __forceinline__ unsigned pk_bf16(float lo, float hi) {
  return __builtin_bit_cast(unsigned, __builtin_convertvector(f32x2{lo, hi}, bf16x2));
}
__device__ __forceinline__ float bf_lo(unsigned u) { return __uint_as_float(u << 16); }
__device__ __forceinline__ float bf_hi(unsigned u) { return __uint_as_float(u & 0xffff0000u); }
__device__ __forceinline__ float rbf(float f) { return bf_lo(pk_bf16(f, f)); }
template <int CTRL> __device__ __forceinline__ float dpp(float x) {
  return __builtin_bit_cast(float, __builtin_amdgcn_mov_dpp(__builtin_bit_cast(int, x), CTRL, 0xf, 0xf, true));
}
constexpr int XOR1 = 0xB1, XOR2 = 0x4E, ROR4 = 0x124, ROR8 = 0x128;
__device__ __forceinline__ float xrow_sum(float x) {
  auto s = __builtin_amdgcn_permlane16_swap(__float_as_uint(x), __float_as_uint(x), false, false);
  x = __uint_as_float(s[0]) + __uint_as_float(s[1]);
  auto t = __builtin_amdgcn_permlane32_swap(__float_as_uint(x), __float_as_uint(x), false, false);
  return __uint_as_float(t[0]) + __uint_as_float(t[1]);
}
__device__ __forceinline__ float quad_stride_sum(float x) {
  x += dpp<ROR8>(x); x += dpp<ROR4>(x);
  return xrow_sum(x);
}
__device__ __forceinline__ float rows_sum4(float a, float b, float c, float d) {
  const auto s = __builtin_amdgcn_permlane32_swap(__float_as_uint(a), __float_as_uint(b), false, false);
  const auto t = __builtin_amdgcn_permlane32_swap(__float_as_uint(c), __float_as_uint(d), false, false);
  const float ab = __uint_as_float(s[0]) + __uint_as_float(s[1]), cd = __uint_as_float(t[0]) + __uint_as_float(t[1]);
  const auto u = __builtin_amdgcn_permlane16_swap(__float_as_uint(ab), __float_as_uint(cd), false, false);
  return __uint_as_float(u[0]) + __uint_as_float(u[1]);
}
__device__ __forceinline__ float max2(float a, float b) {
  float top = 3.4028235e38f;
  asm("" : "+s"(top));
  return __builtin_amdgcn_fmed3f(a, b, top);
}
__device__ __forceinline__ float xrow_max(float x) {
  const auto s = __builtin_amdgcn_permlane16_swap(__float_as_uint(x), __float_as_uint(x), false, false);
  x = max2(__uint_as_float(s[0]), __uint_as_float(s[1]));
  const auto t = __builtin_amdgcn_permlane32_swap(__float_as_uint(x), __float_as_uint(x), false, false);
  return max2(__uint_as_float(t[0]), __uint_as_float(t[1]));
}
__device__ __forceinline__ float wave_sum(float x) {
  x += dpp<XOR1>(x); x += dpp<XOR2>(x);
  return quad_stride_sum(x);
}
__device__ __forceinline__ float half_sum(float x) {
  x += dpp<XOR1>(x); x += dpp<XOR2>(x); x += dpp<ROR8>(x); x += dpp<ROR4>(x);
  const auto s = __builtin_amdgcn_permlane16_swap(__float_as_uint(x), __float_as_uint(x), false, false);
  return __uint_as_float(s[0]) + __uint_as_float(s[1]);
}
__device__ __forceinline__ f32x4 mfma32(v4u a, v4u b, f32x4 c) {
  return __builtin_amdgcn_mfma_f32_16x16x32_bf16(__builtin_bit_cast(bf16x8, a), __builtin_bit_cast(bf16x8, b), c, 0, 0, 0);
}
__device__ __forceinline__ v2u lds_tr(LAS char* p) {
  return __builtin_bit_cast(v2u, __builtin_amdgcn_ds_read_tr16_b64_v4bf16((LAS bf16x4*)p));
}
__device__ __forceinline__ v4u cat(v2u a, v2u b) { return v4u{a[0], a[1], b[0], b[1]}; }
__device__ __forceinline__ v4u not_sent(v4u u) {
  return v4u{min(u[0], SENT - 1), min(u[1], SENT - 1), min(u[2], SENT - 1), min(u[3], SENT - 1)};
}
__device__ __forceinline__ float silu(float v) { return v / (1.f + __expf(-v)); }

__device__ __forceinline__ void dma1(unsigned voff, const char* sbase, unsigned ldsaddr) {
  asm volatile("s_mov_b32 m0, %2\n\ts_nop 0\n\tglobal_load_lds_dwordx4 %0, %1 nt" :: "v"(voff), "s"(sbase), "s"(ldsaddr) : "memory");
}
__device__ __forceinline__ void dma4(unsigned v0, unsigned v1, unsigned v2, unsigned v3, const char* sbase, unsigned ldsaddr) {
  asm volatile("s_mov_b32 m0, %5\n\ts_nop 0\n\t"
               "global_load_lds_dwordx4 %0, %4 nt\n\tglobal_load_lds_dwordx4 %1, %4 offset:1024 nt\n\t"
               "global_load_lds_dwordx4 %2, %4 offset:2048 nt\n\tglobal_load_lds_dwordx4 %3, %4 offset:3072 nt"
               :: "v"(v0), "v"(v1), "v"(v2), "v"(v3), "s"(sbase), "s"(ldsaddr) : "memory");
}
template <int N> __device__ __forceinline__ void wait_vm() { asm volatile("s_waitcnt vmcnt(%0)" :: "i"(N) : "memory"); }
#define SLOT_GROUP(W, STEP) \
  "s_max_i32 %[pub], %[pub], %[cand]\n\tv_mov_b32 v99, %[pub]\n\ts_mov_b32 m0, %[lds]\n\ts_add_u32 %[lds], %[lds], 0x1000\n\t" \
  "s_add_i32 %[cand], %[cand], 1\n\ts_waitcnt vmcnt(" #W ")\n\t" \
  "global_load_lds_dwordx4 %[v0], vcc nt\n\tds_write_b32 %[va], v99\n\tglobal_load_lds_dwordx4 %[v1], vcc offset:1024 nt\n\t" \
  "global_load_lds_dwordx4 %[v2], vcc offset:2048 nt\n\tglobal_load_lds_dwordx4 %[v3], vcc offset:3072 nt\n\t" \
  "s_add_u32 vcc_lo, vcc_lo, " STEP "\n\ts_addc_u32 vcc_hi, vcc_hi, 0\n\t"
#define SLOT_LEVEL(W) \
  "s_mov_b64 vcc, %[src]\n\t" SLOT_GROUP(W, "0x1000") SLOT_GROUP(W, "%[mid]") SLOT_GROUP(W, "0x1000") SLOT_GROUP(W, "0") "s_branch 9f\n\t"
__device__ __forceinline__ void issue_slot(unsigned v0, unsigned v1, unsigned v2, unsigned v3, const char* src, int mid, unsigned lds,
                                           int w, int cand, int& pub, unsigned va) {
  int jump;
  asm volatile("s_mul_i32 %[jmp], %[lvl], 8f-7f\n\ts_getpc_b64 vcc\n6:\n\ts_add_u32 %[jmp], %[jmp], 7f-6b\n\t"
               "s_add_u32 vcc_lo, vcc_lo, %[jmp]\n\ts_addc_u32 vcc_hi, vcc_hi, 0\n\ts_setpc_b64 vcc\n7:\n\t"
               SLOT_LEVEL(12) "8:\n\t" SLOT_LEVEL(16) SLOT_LEVEL(20) SLOT_LEVEL(24) SLOT_LEVEL(28) SLOT_LEVEL(32) "9:"
               : [lds] "+&s"(lds), [cand] "+&s"(cand), [pub] "+&s"(pub), [jmp] "=&s"(jump)
               : [v0] "v"(v0), [v1] "v"(v1), [v2] "v"(v2), [v3] "v"(v3), [src] "s"(src), [mid] "s"(mid), [lvl] "s"(w - 3), [va] "v"(va)
               : "v99", "vcc", "scc", "memory");
}
__device__ __forceinline__ int opaque(int v) { asm volatile("" : "+s"(v)); return v; }
__device__ __forceinline__ void wait_lgkm() { asm volatile("s_waitcnt lgkmcnt(0)" ::: "memory"); }

typedef volatile LAS unsigned* ctrl_t;
template <int SLEEP = 0> __device__ __forceinline__ unsigned lds_wait_ge(ctrl_t ctrl, int word, unsigned want, int& budget) {
  unsigned v, tmp;
  int go;
  budget = __builtin_amdgcn_readfirstlane(budget);
  want = __builtin_amdgcn_readfirstlane(want);
  asm volatile("1:\n\tds_read_b32 %[t], %[a]\n\t.if %[slp]\n\ts_sleep %[slp]\n\t.endif\n\ts_waitcnt lgkmcnt(0)\n\tv_readfirstlane_b32 %[v], %[t]\n\t"
               "s_cmp_lt_u32 %[v], %[w]\n\ts_cselect_b32 %[g], %[b], 0\n\ts_subb_u32 %[b], %[b], 0\n\ts_cmp_gt_i32 %[g], 0\n\ts_cbranch_scc1 1b"
               : [v] "=&s"(v), [t] "=&v"(tmp), [g] "=&s"(go), [b] "+&s"(budget)
               : [a] "v"((unsigned)(uintptr_t)(ctrl + word)), [w] "s"(want), [slp] "i"(SLEEP) : "scc", "memory");
  return v;
}
#define BOX_LOOP_END \
  "v_cmpx_eq_u32 vcc, -1, %[t]\n\ts_sub_i32 %[b], %[b], 1\n\ts_cmp_gt_i32 %[b], 0\n\ts_cselect_b64 vcc, exec, 0\n\t" \
  "s_cmp_lg_u64 vcc, 0\n\ts_cbranch_scc0 2f\n\t.if %[slp]\n\ts_sleep %[slp]\n\t.endif\n\ts_branch 1b\n2:\n\ts_mov_b64 exec, -1"
template <int SLEEP> __device__ __forceinline__ v4u poll_box(__amdgpu_buffer_rsrc_t rs, int off, int& budget) {
  v4u a;
  unsigned tmp;
  budget = __builtin_amdgcn_readfirstlane(budget);
  asm volatile("1:\n\tbuffer_load_dwordx4 v[100:103], %[o], %[rs], 0 offen sc1\n\ts_waitcnt vmcnt(0)\n\t"
               "v_max_u32 %[t], v100, v101\n\tv_max3_u32 %[t], %[t], v102, v103\n\t" BOX_LOOP_END
               : "=&{v[100:103]}"(a), [t] "=&v"(tmp), [b] "+&s"(budget) : [o] "v"(off), [rs] "s"(rs), [slp] "i"(SLEEP) : "vcc", "scc", "memory");
  return a;
}
template <int SLEEP> __device__ __forceinline__ void poll_box2(__amdgpu_buffer_rsrc_t rs, int off0, int off1, v4u& a, v4u& b, int& budget) {
  unsigned tmp;
  budget = __builtin_amdgcn_readfirstlane(budget);
  asm volatile("1:\n\tbuffer_load_dwordx4 v[100:103], %[o0], %[rs], 0 offen sc1\n\tbuffer_load_dwordx4 v[104:107], %[o1], %[rs], 0 offen sc1\n\t"
               "s_waitcnt vmcnt(0)\n\tv_max_u32 %[t], v100, v101\n\tv_max3_u32 %[t], %[t], v102, v103\n\t"
               "v_max3_u32 %[t], %[t], v104, v105\n\tv_max3_u32 %[t], %[t], v106, v107\n\t" BOX_LOOP_END
               : "=&{v[100:103]}"(a), "=&{v[104:107]}"(b), [t] "=&v"(tmp), [b] "+&s"(budget)
               : [o0] "v"(off0), [o1] "v"(off1), [rs] "s"(rs), [slp] "i"(SLEEP) : "vcc", "scc", "memory");
}
__device__ __forceinline__ void poll_lds_o(unsigned addr, v4u& a, v4u& b, int& budget) {
  unsigned tmp;
  budget = __builtin_amdgcn_readfirstlane(budget);
  asm volatile("1:\n\tds_read_b64 v[100:101], %[o]\n\tds_read_b64 v[102:103], %[o] offset:32\n\tds_read_b64 v[104:105], %[o] offset:64\n\t"
               "ds_read_b64 v[106:107], %[o] offset:96\n\ts_waitcnt lgkmcnt(0)\n\tv_max_u32 %[t], v100, v101\n\tv_max3_u32 %[t], %[t], v102, v103\n\t"
               "v_max3_u32 %[t], %[t], v104, v105\n\tv_max3_u32 %[t], %[t], v106, v107\n\t" BOX_LOOP_END
               : "=&{v[100:103]}"(a), "=&{v[104:107]}"(b), [t] "=&v"(tmp), [b] "+&s"(budget) : [o] "v"(addr), [slp] "i"(0) : "vcc", "scc", "memory");
}

typedef unsigned v16u __attribute__((ext_vector_type(16)));
__device__ __forceinline__ char* as_ptr(unsigned lo, unsigned hi) { return (char*)(((u64)hi << 32) | lo); }

__global__ void __launch_bounds__(448, 2) fused_decode(const char*, const char*, const char*, const char*, const char*, const char*, const char*,
                                                    char*, char*) {
  v16u ka16;
  v2u ka2;
  asm volatile("s_load_dwordx16 s[36:51], %2, 0x0\n\ts_load_dwordx2 s[52:53], %2, 0x40"
               : "={s[36:51]}"(ka16), "={s[52:53]}"(ka2) : "s"(__builtin_amdgcn_kernarg_segment_ptr()));
  __builtin_amdgcn_sched_barrier(0);
  extern __shared__ __attribute__((aligned(16))) char smem[];
  LAS char* L = (LAS char*)smem;
  const unsigned lbase = (unsigned)(uintptr_t)L;
  ctrl_t ctrl = (ctrl_t)(L + L_CTRL);
  const int lane = threadIdx.x & 63, bid = blockIdx.x;
  int wave = __builtin_amdgcn_readfirstlane(threadIdx.x >> 6);
  const int xg = bid & 7, tg = bid >> 3, kvh = tg >> 2, ii = tg & 3, gi = 4 * xg + ii, wid = kvh * 32 + gi, bb = gi >> 4;
  const int nt = ii == 0 ? 33 : ii == 1 ? 31 : 32, tile0 = (xg & 3) * 128 + (ii == 1 ? 33 : 32 * ii);
  const int g_kv = 8 * nt, g_wo = g_kv + 4;
  const int g = lane >> 4, col = lane & 15, q4 = (lane >> 2) & 3, p4 = lane & 3;
  const int ffn = xg * 8 + kvh;
  const bool has_ffn = ii == 1;
  unsigned vk[4], vx = lane * 16, vq = (lane >> 1) * 8192 + (lane & 1) * 16;
#pragma unroll
  for (int j = 0; j < 4; ++j) vk[j] = g * 256 + ((col ^ (4 * j + g)) * 16);
  unsigned kvoff = ((bb * 8 + kvh) * SK + tile0 * 64) * 256, qoff = (kvh * 512 + gi * 16) * 2;
  asm volatile("" : "+v"(vk[0]), "+v"(vk[1]), "+v"(vk[2]), "+v"(vk[3]), "+v"(vx), "+v"(vq));
  asm volatile("" : "+s"(kvoff), "+s"(qoff), "+s"(wave));
  ctrl[lane] = 0u;
  ctrl[C_LANDED + lane] = 0u;
  *(volatile LAS unsigned*)(L + L_O + lane * 4) = SENT;
  asm volatile("s_waitcnt lgkmcnt(0)\n\ts_barrier" : "+{s[36:51]}"(ka16), "+{s[52:53]}"(ka2) :: "memory");
  const char *x = as_ptr(ka16[0], ka16[1]), *kc = as_ptr(ka16[2], ka16[3]), *vc = as_ptr(ka16[4], ka16[5]), *wq = as_ptr(ka16[6], ka16[7]),
             *wo = as_ptr(ka16[8], ka16[9]), *w1 = as_ptr(ka16[10], ka16[11]), *w2 = as_ptr(ka16[12], ka16[13]);
  char *out = as_ptr(ka16[14], ka16[15]), *ws = as_ptr(ka2[0], ka2[1]);
  const __amdgpu_buffer_rsrc_t rs = __builtin_amdgcn_make_buffer_rsrc((void*)ws, 0, WS_END, 0x00020000);
  const unsigned t_start = (unsigned)__builtin_amdgcn_s_memrealtime();
  if (wave == 1) {
    __builtin_amdgcn_s_setprio(3);
    const u64 pk_ = (u64)(kc + kvoff), pv_ = (u64)(vc + kvoff), pq_ = (u64)(wq + qoff), pq2 = pq_ + 511 * 8192;
    asm volatile("v_writelane_b32 v100, %0, 0\n\tv_writelane_b32 v101, %1, 0\n\tv_writelane_b32 v100, %2, 1\n\tv_writelane_b32 v101, %3, 1\n\t"
                 "v_writelane_b32 v100, %4, 2\n\tv_writelane_b32 v101, %5, 2\n\tv_writelane_b32 v100, %6, 3\n\tv_writelane_b32 v101, %7, 3\n\t"
                 "v_writelane_b32 v100, %8, 4\n\tv_writelane_b32 v101, %9, 4\n\tv_writelane_b32 v100, %10, 5\n\tv_writelane_b32 v101, %11, 5\n\t"
                 "s_mov_b32 m0, %12\n\ts_mov_b64 exec, 63\n\tglobal_load_lds_dword v[100:101], off nt\n\ts_mov_b64 exec, -1"
                 :: "s"((unsigned)pk_), "s"((unsigned)(pk_ >> 32)), "s"((unsigned)pv_), "s"((unsigned)(pv_ >> 32)), "s"(ka16[0]), "s"(ka16[1]),
                    "s"(ka2[0]), "s"(ka2[1]), "s"((unsigned)pq_), "s"((unsigned)(pq_ >> 32)), "s"((unsigned)pq2), "s"((unsigned)(pq2 >> 32)),
                    "s"(lbase + L_NIL) : "v100", "v101", "memory");
#pragma unroll
    for (int j = 0; j < 4; ++j) dma4(vk[0], vk[1], vk[2], vk[3], kc + kvoff + j * 4096, lbase + j * 4096);
  }
  if (opaque(wave) == 0) {
    asm volatile("s_mov_b32 m0, %4\n\ts_nop 0\n\tglobal_load_lds_dwordx4 %0, %1\n\tglobal_load_lds_dwordx4 %0, %1 offset:1024\n\t"
                 "s_mov_b32 m0, %5\n\ts_nop 0\n\tglobal_load_lds_dwordx4 %2, %3 sc1"
                 :: "v"(vx), "s"(x), "v"(WS_EPOCH), "s"(ws), "s"(lbase + L_W), "s"(lbase + L_W + 2048) : "memory");
  }
  if (opaque(wave) == 6) {
#pragma nounroll
    for (int j = 0; j < 16; ++j) dma1(vq, wq + qoff + (size_t)j * (32 * 8192), lbase + L_S + j * 1024);
    wait_vm<0>();
    if (lane == 0) ctrl[C_WQ] = 1u;
  }
  const int role = opaque(wave);
  int budget = SPIN_BUDGET;

  if (__builtin_expect(role == 1, 1)) {
    unsigned vv[2];
#pragma unroll
    for (int j = 0; j < 2; ++j) vv[j] = g * 256 + ((col ^ ((4 * j + g) << 1)) * 16);
    const char* kb = kc + kvoff;
    const char* vb = vc + kvoff;
    const char* wb = wo + (size_t)(kvh * 512 + 32 * (gi >> 3) + 8 * ((gi >> 2) & 1)) * 1024 + ii * 256;
    const unsigned va_pub = lbase + L_CTRL + (C_LANDED + lane) * 4;
    const int w16 = W0 * 16 + (ii == 0 ? FF0 : ii == 1 ? FF1 : 0);
    int issued = 4, pub = 0, wl = W0 / 4, dith = 0;
#pragma nounroll
    for (int seq = 1; seq <= 2 * nt; ++seq) {
      {
        const v4u sc = *(volatile LAS v4u*)(ctrl + C_SCHED);
        v4u f = *(volatile LAS v4u*)(ctrl + C_FREED);
        const int el = (int)((unsigned)__builtin_amdgcn_s_memrealtime() - __builtin_amdgcn_readfirstlane(sc[0]));
        const int rate = __builtin_amdgcn_readfirstlane(sc[2]);
        const int lag16 = rate ? ((((int)__builtin_amdgcn_readfirstlane(sc[1]) + ((el * rate) >> 8)) * nt) >> 5) - pub * 16 : 0;
        const int wk = w16 + lag16 * KN / KD + dith;
        wl = __builtin_amdgcn_readfirstlane(min(max((wk + 32) >> 6, WMIN / 4), WMAX / 4));
        dith = min(max(wk - wl * 64, -32), 32);
        if ((int)__builtin_amdgcn_readfirstlane(min(min(f[0], f[1]), min(f[2], f[3]))) <= seq - NSLOT) {
          wait_vm<0>(); pub = issued; ctrl[C_LANDED + lane] = pub;
          do {
            __builtin_amdgcn_s_sleep(1);
            f = *(volatile LAS v4u*)(ctrl + C_FREED);
          } while ((int)__builtin_amdgcn_readfirstlane(min(min(f[0], f[1]), min(f[2], f[3]))) <= seq - NSLOT && --budget > 0);
        }
      }
      const bool is_wo = seq == 2 * nt, is_k = seq < nt;
#define DRAIN_TO(N) { wait_vm<4 * (N)>(); pub = max(pub, issued - (N)); ctrl[C_LANDED + lane] = pub; }
      if (is_wo) { DRAIN_TO(8) DRAIN_TO(7) DRAIN_TO(6) DRAIN_TO(5) DRAIN_TO(4) DRAIN_TO(3) DRAIN_TO(2) DRAIN_TO(1) DRAIN_TO(0) wl = 8; }
      unsigned v[4];
#pragma unroll
      for (int j = 0; j < 4; ++j) v[j] = is_k ? vk[j] : is_wo ? vv[j & 1] + g * 768 + j * (127 * 1024) : vv[j & 1];
      const char* src = is_wo ? wb : is_k ? kb + seq * SLOT : vb + (seq - nt) * SLOT;
      issue_slot(v[0], v[1], v[2], v[3], src, is_wo ? 12288 : 4096, lbase + (seq & (NSLOT - 1)) * SLOT, wl, issued - wl, pub, va_pub);
      issued += 4;
    }
    DRAIN_TO(8) DRAIN_TO(7) DRAIN_TO(6) DRAIN_TO(5) DRAIN_TO(4) DRAIN_TO(0)
#undef DRAIN_TO
    return;
  }

  if (role == 0) {
    u64 tag;
    v4u xb[16];
    {
      wait_vm<0>();
      tag = *(volatile LAS u64*)(L + L_W + 2048) + 1;
      const v4u xv0 = *(volatile LAS v4u*)(L + L_W + lane * 32), xv1 = *(volatile LAS v4u*)(L + L_W + lane * 32 + 16);
      float v[16];
#pragma unroll
      for (int e = 0; e < 4; ++e) { v[2 * e] = bf_lo(xv0[e]); v[2 * e + 1] = bf_hi(xv0[e]); v[8 + 2 * e] = bf_lo(xv1[e]); v[9 + 2 * e] = bf_hi(xv1[e]); }
      float s4[4], q4s[4];
#pragma unroll
      for (int e = 0; e < 4; ++e) s4[e] = (v[e] + v[4 + e]) + (v[8 + e] + v[12 + e]);
      const float mean = half_sum((s4[0] + s4[1]) + (s4[2] + s4[3])) * (1.f / D);
#pragma unroll
      for (int e = 0; e < 4; ++e) {
        const float d0 = v[e] - mean, d1 = v[4 + e] - mean, d2 = v[8 + e] - mean, d3 = v[12 + e] - mean;
        q4s[e] = fmaf(d0, d0, d1 * d1) + fmaf(d2, d2, d3 * d3);
      }
      const float var = half_sum((q4s[0] + q4s[1]) + (q4s[2] + q4s[3])) * (1.f / D);
      const float mb = rbf(mean), den = rbf(__builtin_amdgcn_sqrtf(rbf(rbf(var) + 1e-5f))), rden = __builtin_amdgcn_rcpf(den);
      unsigned o[8];
#pragma unroll
      for (int e = 0; e < 8; ++e) {
        const unsigned dp = pk_bf16(v[2 * e] - mb, v[2 * e + 1] - mb);
        const float d0 = bf_lo(dp), d1 = bf_hi(dp), q0 = d0 * rden, q1 = d1 * rden;
        o[e] = pk_bf16(fmaf(fmaf(-q0, den, d0), rden, q0), fmaf(fmaf(-q1, den, d1), rden, q1));
      }
      *(LAS v4u*)(L + L_XN + lane * 32) = v4u{o[0], o[1], o[2], o[3]};
      *(LAS v4u*)(L + L_XN + lane * 32 + 16) = v4u{o[4], o[5], o[6], o[7]};
      asm volatile("" ::: "memory");
      LAS char* xa = L + L_XN + (((col >> 1) & 1) * 512 + 4 * g) * 2;
#pragma unroll
      for (int ks = 0; ks < 16; ++ks) xb[ks] = cat(*(LAS v2u*)(xa + ks * 64), *(LAS v2u*)(xa + ks * 64 + 32));
      __builtin_amdgcn_sched_barrier(0);
      lds_wait_ge(ctrl, C_WQ, 1u, budget);
      f32x4 qp[2] = {{0.f, 0.f, 0.f, 0.f}, {0.f, 0.f, 0.f, 0.f}};
      LAS char* wa = L + L_S + 8 * lane;
      v4u fa[16];
#pragma unroll
      for (int ks = 0; ks < 16; ++ks) fa[ks] = cat(lds_tr(wa + ks * 1024), lds_tr(wa + ks * 1024 + 512));
      __builtin_amdgcn_sched_barrier(0);
#pragma unroll
      for (int ks = 0; ks < 16; ++ks) qp[ks & 1] = mfma32(fa[ks], xb[ks], qp[ks & 1]);
      const f32x4 qa = qp[0] + qp[1];
      if ((col & 13) == 0) *(LAS v2u*)(L + L_Q + (col >> 1) * 32 + g * 8) = v2u{pk_bf16(qa[0], qa[1]), pk_bf16(qa[2], qa[3])};
      asm volatile("" ::: "memory");
      const unsigned thi = (unsigned)(tag >> 16), tlo = (unsigned)tag << 16;
      const unsigned qv = *(LAS unsigned short*)(L + L_Q + (lane & 31) * 2);
      __builtin_amdgcn_raw_buffer_store_b64(v2u{tlo | qv, thi}, rs, lane < 32 ? WS_QG + ((kvh * 2 + g) * 512 + gi * 16 + col) * 8 : OOB, 0, AUX_SC1);
      if (lane == 0) *(volatile LAS u64*)(ctrl + C_TAG) = tag;
    }
    {
      const unsigned thi = (unsigned)(tag >> 16), tlo = (unsigned)tag << 16;
      while ((int)((unsigned)__builtin_amdgcn_s_memrealtime() - t_start) < QDELAY) __builtin_amdgcn_s_sleep(2);
      const int qo = WS_QG + (kvh * 2 + bb) * 4096 + lane * 8;
      v2u a[8];
#pragma unroll
      for (int k = 0; k < 8; ++k) a[k] = v2u{~tlo, ~thi};
      for (;;) {
        asm volatile("" ::: "memory");
        bool all = true;
        v2u nv[8];
#pragma unroll
        for (int k = 0; k < 8; ++k) {
          const bool ok = a[k][1] == thi && ((a[k][0] ^ tlo) >> 16) == 0;
          nv[k] = __builtin_amdgcn_raw_buffer_load_b64(rs, ok ? OOB : qo + 512 * k, 0, AUX_SC1);
        }
#pragma unroll
        for (int k = 0; k < 8; ++k) {
          const bool ok = a[k][1] == thi && ((a[k][0] ^ tlo) >> 16) == 0;
          a[k] = ok ? a[k] : nv[k];
          all = all && a[k][1] == thi && ((a[k][0] ^ tlo) >> 16) == 0;
        }
        if (__all(all)) break;
        if (--budget <= 0) break;
        __builtin_amdgcn_s_sleep(4);
      }
#pragma unroll
      for (int k = 0; k < 8; ++k) *(LAS unsigned short*)(L + L_Q + (64 * k + lane) * 2) = (unsigned short)a[k][0];
      wait_lgkm();
      if (lane == 0) ctrl[C_Q] = 1u;
    }
    auto ffn_slice = [&]() {
      const int u0 = (28 * ffn) & ~7, lo = 28 * ffn - u0;
      const unsigned vo1 = (lane >> 2) * (FF * 2) + ((p4 ^ ((lane >> 3) & 2)) * 16);
      LAS char* wa = L + L_W + (4 * g + q4) * 64 + 8 * p4;
      f32x4 h0 = {0.f, 0.f, 0.f, 0.f}, h1 = h0;
#pragma unroll
      for (int rd = 0; rd < 4; ++rd) {
#pragma unroll
        for (int j = 0; j < 8; ++j) dma1(vo1, w1 + u0 * 2 + (size_t)(rd * 128 + j * 16) * (FF * 2), lbase + L_W + j * 1024);
        wait_vm<0>();
        v4u fa[4][2];
#pragma unroll
        for (int ks = 0; ks < 4; ++ks)
#pragma unroll
          for (int nb = 0; nb < 2; ++nb) {
            LAS char* p = wa + ks * 2048 + ((nb ^ (g & 1)) * 32);
            fa[ks][nb] = cat(lds_tr(p), lds_tr(p + 1024));
          }
        __builtin_amdgcn_sched_barrier(0);
        wait_lgkm();
#pragma unroll
        for (int ks = 0; ks < 4; ++ks) { h0 = mfma32(fa[ks][0], xb[4 * rd + ks], h0); h1 = mfma32(fa[ks][1], xb[4 * rd + ks], h1); }
      }
      float hv[8];
#pragma unroll
      for (int j = 0; j < 8; ++j) {
        const int u = (j >> 2) * 16 + 4 * g + (j & 3);
        hv[j] = (u >= lo && u < lo + 28) ? silu(rbf(j < 4 ? h0[j & 3] : h1[j & 3])) : 0.f;
      }
      const v4u hb = {pk_bf16(hv[0], hv[1]), pk_bf16(hv[2], hv[3]), pk_bf16(hv[4], hv[5]), pk_bf16(hv[6], hv[7])};
      const unsigned vo2[2] = {(unsigned)(g * 1024 + ((col ^ (g << 1)) * 16)), (unsigned)(g * 1024 + ((col ^ ((4 + g) << 1)) * 16))};
      const int sw = (4 * (g & 1) + q4) << 1;
      LAS char* w2a = L + L_W + (4 * g + q4) * 256 + 8 * (p4 & 1);
      const int mine = 2 * (col >> 2) + (col & 1);
#pragma unroll
      for (int cb = 0; cb < 4; ++cb) {
#pragma unroll
        for (int j = 0; j < 8; ++j) dma1(vo2[j & 1], w2 + (size_t)(u0 + 4 * j) * 1024 + cb * 256, lbase + L_W + j * 1024);
        wait_vm<0>();
        v4u wf[8];
#pragma unroll
        for (int nb = 0; nb < 8; ++nb) {
          LAS char* p = w2a + (((2 * nb + (p4 >> 1)) ^ sw) * 16);
          wf[nb] = cat(lds_tr(p), lds_tr(p + 4096));
        }
        __builtin_amdgcn_sched_barrier(0);
        wait_lgkm();
        f32x4 fo = {0.f, 0.f, 0.f, 0.f};
#pragma unroll
        for (int nb = 0; nb < 8; ++nb) fo = mfma32(wf[nb], mine == nb ? hb : v4u{0u, 0u, 0u, 0u}, fo);
        __builtin_amdgcn_raw_buffer_store_b128(not_sent(__builtin_bit_cast(v4u, fo)), rs, WS_PF + ffn * 4096 + cb * 1024 + lane * 16, 0, AUX_SC1);
      }
    };
    if (has_ffn) {
      ffn_slice();
      wait_vm<0>();
      if (lane == 0) __hip_atomic_store((GAS u64*)(ws + WS_FOK) + ffn, tag, RLX_AGENT);
    }

    lds_wait_ge<8>(ctrl, C_ALL, 1u, budget);
    const int cb = xg >> 1, pc = (xg & 1) * 32 + tg, cw = pc >> 4, gw = (pc >> 2) & 3;
    const int jj = (g >> 1) | ((g & 1) << 1), oidx = ((pc >> 1) & 1) * 512 + cb * 128 + cw * 32 + (pc & 1) * 16 + gw * 4 + jj;
    const float xin = __uint_as_float((unsigned)*(const GAS unsigned short*)(x + oidx * 2) << 16);
    float gate = 0.f;
    const __amdgpu_buffer_rsrc_t ro = __builtin_amdgcn_make_buffer_rsrc((void*)out, 0, 2 * D * 2, 0x00020000);
    int out_off = col == 0 ? oidx * 2 : OOB;
    __builtin_amdgcn_raw_buffer_store_b16((unsigned short)0, ro, out_off, 0, 0);
    const unsigned touch = *(const GAS unsigned short*)(wo + (size_t)(kvh * 512 + g * 128 + 4 * gi) * 1024);
    asm volatile("" : "+v"(out_off) : "v"(touch));
#pragma nounroll
    for (int pass = 0; pass < 2; ++pass) {
      const bool last = opaque(pass) != 0;
      if (last) { lds_wait_ge<8>(ctrl, C_LANDED, g_kv - 4, budget); lds_wait_ge(ctrl, C_PUB, 1u, budget); }
      else {
        lds_wait_ge<8>(ctrl, C_LANDED, PFAT, budget);
        while (!__all(__hip_atomic_load((GAS u64*)(ws + WS_FOK) + lane, RLX_AGENT) == tag) && --budget > 0) __builtin_amdgcn_s_sleep(8);
      }
      const int po = (last ? WS_P2 + pc * 16 : WS_PF + gw * 256 + cw * 64 + (pc & 3) * 16) + lane * 4096 + cb * 1024;
      const v4u sv = poll_box<0>(rs, po, budget);
      float t = rows_sum4(__uint_as_float(sv[0]), __uint_as_float(sv[1]), __uint_as_float(sv[2]), __uint_as_float(sv[3]));
      t += dpp<XOR1>(t); t += dpp<XOR2>(t); t += dpp<ROR8>(t); t += dpp<ROR4>(t);
      t = rbf(t);
      if (last) {
        const float y = xin + rbf(t * gate);
        __builtin_amdgcn_raw_buffer_store_b16(budget <= 0 ? (unsigned short)0x7fc0 : (unsigned short)pk_bf16(y, y), ro, out_off, 0, 0);
        break;
      }
      gate = rbf(silu(t));
    }
    return;
  }

  if (role == 6) {
    unsigned at_t = 0;
    int at_p = 0;
    lds_wait_ge<8>(ctrl, C_Q, 1u, budget);
    const u64 tag = *(volatile LAS u64*)(ctrl + C_TAG);
    lds_wait_ge<8>(ctrl, C_CLR, 4u, budget);
#pragma nounroll
    for (int k = 0; k < NROUND; ++k) {
      GAS u64* line = (GAS u64*)(ws + WS_TAU) + (k ? k * 256 : kvh * 32);
      const int at = MARK + k * MSTEP, mark = (at * nt) >> 5;
      lds_wait_ge<1>(ctrl, C_LANDED, mark, budget);
      const unsigned tau = (unsigned)__builtin_amdgcn_s_memrealtime();
      if (lane == 0 && (k == 0 || ii == 3)) __hip_atomic_store(line + (k ? ffn : gi), (tag << 16) | (tau & 0xffffu), RLX_AGENT);
      lds_wait_ge<4>(ctrl, C_LANDED, mark + MDEL, budget);
      const u64 v = __hip_atomic_load(line + (lane & (k ? 63 : 31)), RLX_AGENT);
      const unsigned seen_t = ((v ^ (tag << 16)) >> 16) == 0 ? (unsigned)v : (unsigned)__builtin_amdgcn_s_memrealtime();
      const float d = wave_sum((float)(short)(seen_t - tau));
      const unsigned mean = tau + __builtin_amdgcn_readfirstlane((int)(d * (1.f / 64)));
      constexpr int rate = 65536 / TG16;
      const int common = k ? at_p + (((int)(mean - at_t) * rate) >> 8) - at * 16 : LAG0 * 16;
      at_t = mean;
      at_p = at * 16 + min(max(common, 0), LAGHI * 16);
      if (lane == 0) *(volatile LAS v4u*)(ctrl + C_SCHED) = v4u{at_t, (unsigned)at_p, (unsigned)rate, 0u};
      if (k == 0) {
        while (!__all(((__hip_atomic_load(line + (lane & 31), RLX_AGENT) ^ (tag << 16)) >> 16) == 0) && --budget > 0) __builtin_amdgcn_s_sleep(8);
        if (lane == 0) ctrl[C_FLAG] = 1u;
        if (gi == 0 && lane == 0) __hip_atomic_store((GAS u64*)(ws + WS_GOK) + kvh, tag, RLX_AGENT);
      } else if (k == 1) {
        while (!__all(__hip_atomic_load((GAS u64*)(ws + WS_GOK) + (lane & 7), RLX_AGENT) == tag) && --budget > 0) __builtin_amdgcn_s_sleep(8);
        if (lane == 0) ctrl[C_ALL] = 1u;
        if (bid == 0 && lane == 0) __hip_atomic_store((GAS u64*)(ws + WS_EPOCH), tag, RLX_AGENT);
      }
    }
    return;
  }

  const int c = role - 2;
  const int blk = col >> 2;
  constexpr float SCALE = 0.08838834764831845f, LOG2E = 1.4426950408889634f;
  unsigned seen = 0u;
  auto need = [&](unsigned want) {
    if (seen < want) seen = lds_wait_ge(ctrl, C_LANDED, want, budget);
    asm volatile("" ::: "memory");
  };
  float ms;
  {
    unsigned ka[4];
#pragma unroll
    for (int k = 0; k < 4; ++k) ka[k] = col * 256 + (((4 * k + g) ^ col) * 16);
    v4u kf[4][4];
    auto fetch_k = [&](int t) {
      need(4 * t + 4);
      LAS char* sb = L + (t & (NSLOT - 1)) * SLOT;
#pragma unroll
      for (int k = 0; k < 4; ++k)
#pragma unroll
        for (int a = 0; a < 4; ++a) kf[a][k] = *(LAS v4u*)(sb + ka[k] + a * 4096);
      __builtin_amdgcn_sched_barrier(0);
      wait_lgkm();
      if (lane == 0) ctrl[C_FREED + c] = min(t + 4, nt);
      __builtin_amdgcn_sched_barrier(0);
    };
    fetch_k(c);
    const v4u sv = {SENT, SENT, SENT, SENT};
    if (c < 3) __builtin_amdgcn_raw_buffer_store_b128(sv, rs, (c < 2 ? WS_P1 + wid * 2048 + c * 1024 : WS_P2 + wid * 1024) + lane * 16, 0, AUX_SC1);
    wait_vm<0>();
    if (lane == 0) __hip_atomic_fetch_add((LAS unsigned*)(ctrl + C_CLR), 1u, __ATOMIC_RELAXED, __HIP_MEMORY_SCOPE_WORKGROUP);
    lds_wait_ge<2>(ctrl, C_Q, 1u, budget);
    v4u qf[4][4];
#pragma unroll
    for (int k = 0; k < 4; ++k) {
      const v4u qv = *(LAS v4u*)(L + L_Q + ((col & 3) * 128 + 32 * k + 8 * g) * 2);
#pragma unroll
      for (int a = 0; a < 4; ++a) qf[a][k] = blk == a ? qv : v4u{0u, 0u, 0u, 0u};
    }
    float mx = -3.0e38f;
#pragma nounroll
    for (int t = c;; t += 4) {
      f32x4 s0 = {0.f, 0.f, 0.f, 0.f}, s1 = {0.f, 0.f, 0.f, 0.f};
#pragma unroll
      for (int k = 0; k < 4; ++k) {
        s0 = mfma32(kf[0][k], qf[0][k], s0); s1 = mfma32(kf[1][k], qf[1][k], s1);
        s0 = mfma32(kf[2][k], qf[2][k], s0); s1 = mfma32(kf[3][k], qf[3][k], s1);
      }
      s0 += s1;
      const unsigned a0 = pk_bf16(s0[0], s0[1]), a1 = pk_bf16(s0[2], s0[3]);
      const unsigned b0 = pk_bf16(bf_lo(a0) * SCALE, bf_hi(a0) * SCALE), b1 = pk_bf16(bf_lo(a1) * SCALE, bf_hi(a1) * SCALE);
      *(LAS v2u*)(L + L_S + t * 512 + lane * 8) = v2u{b0, b1};
      mx = max2(max2(mx, max2(bf_lo(b0), bf_hi(b0))), max2(bf_lo(b1), bf_hi(b1)));
      if (t + 4 >= nt) break;
      fetch_k(t + 4);
    }
    mx = max2(mx, dpp<ROR8>(mx)); mx = max2(mx, dpp<ROR4>(mx)); mx = xrow_max(mx);
    if (lane < 4) *(LAS float*)(L + L_M + lane * 16 + c * 4) = mx;
    wait_lgkm();
    if (lane == 0) __hip_atomic_fetch_add((LAS unsigned*)(ctrl + C_MAX), 1u, __ATOMIC_RELAXED, __HIP_MEMORY_SCOPE_WORKGROUP);
    lds_wait_ge(ctrl, C_MAX, 4u, budget);
    const f32x4 m4 = *(LAS f32x4*)(L + L_M + (col & 3) * 16);
    ms = max2(max2(m4[0], m4[1]), max2(m4[2], m4[3])) * LOG2E;
  }
  unsigned va[2];
#pragma unroll
  for (int n = 0; n < 2; ++n)
    va[n] = (4 * g + q4) * 256 + (((4 * c + 2 * n + (p4 >> 1)) ^ ((4 * (g & 1) + q4) << 1)) * 16) + 8 * (p4 & 1);
  int p1_off = col < 8 ? WS_P1 + wid * 2048 + c * 512 + g * 128 + col * 16 : OOB;
  int p2_off = col < 4 ? WS_P2 + wid * 1024 + c * 256 + g * 64 + col * 16 : OOB;
  int rec0 = WS_P1 + (kvh * 32 + (c >> 1) * 16 + (lane >> 3)) * 2048 + (gi >> 2) * 256 + (c & 1) * 128 + (lane & 7) * 16, rec1 = rec0 + 8 * 2048;
  unsigned o_wr = (lane & 40) == 0 ? L_O + c * 64 + col * 8 + g * 4 : L_NIL + lane * 4, o_rd = lbase + L_O + ((col >> 1) & 1) * 128 + g * 8;
  unsigned wo0 = ((2 * nt) & (NSLOT - 1)) * SLOT + va[0], wo1 = ((2 * nt) & (NSLOT - 1)) * SLOT + va[1];
  asm volatile("" : "+v"(p1_off), "+v"(p2_off), "+v"(rec0), "+v"(rec1), "+v"(o_wr), "+v"(o_rd), "+v"(wo0), "+v"(wo1));
  float lsum = 0.f, linv = 0.f;
  unsigned lam = 0;
  f32x4 acc0 = {0.f, 0.f, 0.f, 0.f}, acc1 = {0.f, 0.f, 0.f, 0.f};
#pragma nounroll
  for (int t = 0; t < nt; ++t) {
    const int seq = nt + t;
    const v2u sc = *(LAS v2u*)(L + L_S + t * 512 + lane * 8);
    const float e0 = __builtin_amdgcn_exp2f(fmaf(bf_lo(sc[0]), LOG2E, -ms)), e1 = __builtin_amdgcn_exp2f(fmaf(bf_hi(sc[0]), LOG2E, -ms));
    const float e2 = __builtin_amdgcn_exp2f(fmaf(bf_lo(sc[1]), LOG2E, -ms)), e3 = __builtin_amdgcn_exp2f(fmaf(bf_hi(sc[1]), LOG2E, -ms));
    lsum += (e0 + e1) + (e2 + e3);
    const unsigned p0 = pk_bf16(e0, e1), p1 = pk_bf16(e2, e3);
    v4u pA = {blk == 0 ? p0 : 0u, blk == 0 ? p1 : 0u, blk == 1 ? p0 : 0u, blk == 1 ? p1 : 0u};
    v4u pB = {blk == 2 ? p0 : 0u, blk == 2 ? p1 : 0u, blk == 3 ? p0 : 0u, blk == 3 ? p1 : 0u};
    if (t == nt - 1) {
      lds_wait_ge(ctrl, C_FLAG, 1u, budget);
      const float lt = quad_stride_sum(lsum);
      linv = __builtin_amdgcn_rcpf(lt);
      lam = __float_as_uint(ms + __builtin_amdgcn_logf(lt));
    }
    asm volatile("" : "+v"(pA), "+v"(pB), "+v"(lsum), "+v"(linv), "+v"(lam));
    LAS char* sb = L + (seq & (NSLOT - 1)) * SLOT;
    need(4 * seq + 2);
    acc0 = mfma32(cat(lds_tr(sb + va[0]), lds_tr(sb + va[0] + 4096)), pA, acc0);
    acc1 = mfma32(cat(lds_tr(sb + va[1]), lds_tr(sb + va[1] + 4096)), pA, acc1);
    need(4 * seq + 4);
    v4u v0 = cat(lds_tr(sb + va[0] + 8192), lds_tr(sb + va[0] + 12288)), v1 = cat(lds_tr(sb + va[1] + 8192), lds_tr(sb + va[1] + 12288));
    wait_lgkm();
    if (lane == 0) ctrl[C_FREED + c] = seq + 1;
    asm volatile("" : "+v"(v0), "+v"(v1) :: "memory");
    acc0 = mfma32(v0, pB, acc0);
    acc1 = mfma32(v1, pB, acc1);
  }

  v4u pk;
#pragma unroll
  for (int j = 0; j < 4; ++j) {
    float a = ((col & 4) ? acc1[j] : acc0[j]) + dpp<ROR4>((col & 4) ? acc0[j] : acc1[j]);
    a += dpp<ROR8>(a);
    pk[j] = ((__float_as_uint(a * linv) + 0x80u) & 0xffffff00u) | ((lam >> (8 * j)) & 0xffu);
  }
  __builtin_amdgcn_raw_buffer_store_b128(not_sent(pk), rs, p1_off, 0, AUX_SC1);
  v4u a0, a1, wa[4];
  need(g_wo);
#pragma unroll
  for (int n = 0; n < 4; ++n) wa[n] = cat(lds_tr(L + ((n & 1) ? wo1 : wo0) + (n >> 1) * 4096), lds_tr(L + ((n & 1) ? wo1 : wo0) + (n >> 1) * 4096 + 8192));
  __builtin_amdgcn_sched_barrier(0);
  poll_box2<0>(rs, rec0, rec1, a0, a1, budget);
  const float lA = __uint_as_float(__builtin_amdgcn_perm(__builtin_amdgcn_perm(a0[3], a0[2], 0x04000c0cu), __builtin_amdgcn_perm(a0[1], a0[0], 0x0c0c0400u), 0x07060100u));
  const float lB = __uint_as_float(__builtin_amdgcn_perm(__builtin_amdgcn_perm(a1[3], a1[2], 0x04000c0cu), __builtin_amdgcn_perm(a1[1], a1[0], 0x0c0c0400u), 0x07060100u));
  float M = max2(lA, lB);
  M = xrow_max(max2(M, dpp<ROR8>(M)));
  const float wA = __builtin_amdgcn_exp2f(lA - M), wB = __builtin_amdgcn_exp2f(lB - M);
  float r[4];
#pragma unroll
  for (int j = 0; j < 4; ++j) r[j] = fmaf(wA, __uint_as_float(a0[j] & 0xffffff00u), wB * __uint_as_float(a1[j] & 0xffffff00u));
  float wsum = xrow_sum(wA + wB), ov = rows_sum4(r[0], r[1], r[2], r[3]);
  wsum += dpp<ROR8>(wsum); ov += dpp<ROR8>(ov);
  ov *= __builtin_amdgcn_rcpf(wsum);
  const auto pr = __builtin_amdgcn_permlane32_swap(__float_as_uint(ov), __float_as_uint(ov), false, false);
  *(LAS unsigned*)(L + o_wr) = min(pk_bf16(__uint_as_float(pr[0]), __uint_as_float(pr[1])), SENT - 1);
  v4u o0, o1;
  poll_lds_o(o_rd, o0, o1, budget);
  const f32x4 zero = {0.f, 0.f, 0.f, 0.f};
  const f32x4 sp0 = mfma32(wa[2], o1, mfma32(wa[0], o0, zero)), sp1 = mfma32(wa[3], o1, mfma32(wa[1], o0, zero));
  const f32x4 sp = (col & 1) ? sp1 : sp0;
  __builtin_amdgcn_raw_buffer_store_b128(not_sent(__builtin_bit_cast(v4u, sp)), rs, p2_off, 0, AUX_SC1);
  ctrl[C_PUB] = 1u;
}

extern "C" void kernel_launch(void* const* d_in, const int* in_sizes, int n_in, void* d_out, int out_size, void* d_ws, size_t ws_size,
                              hipStream_t stream) {
  (void)hipFuncSetAttribute((const void*)fused_decode, hipFuncAttributeMaxDynamicSharedMemorySize, L_BYTES);
  hipLaunchKernelGGL(fused_decode, dim3(NWG), dim3(448), L_BYTES, stream, (const char*)d_in[0], (const char*)d_in[1], (const char*)d_in[2],
                     (const char*)d_in[3], (const char*)d_in[4], (const char*)d_in[6], (const char*)d_in[7], (char*)d_out, (char*)d_ws);
}
```

```cpp
#include <hip/hip_runtime.h>
#include <hip/hip_bf16.h>
#include <stdint.h>

#define GAS __attribute__((address_space(1)))
#define LAS __attribute__((address_space(3)))
#define RLX_AGENT __ATOMIC_RELAXED, __HIP_MEMORY_SCOPE_AGENT
typedef unsigned long long u64;
typedef unsigned v4u __attribute__((ext_vector_type(4)));
typedef unsigned v2u __attribute__((ext_vector_type(2)));
typedef float f32x4 __attribute__((ext_vector_type(4)));
typedef float f32x2 __attribute__((ext_vector_type(2)));
typedef __bf16 bf16x8 __attribute__((ext_vector_type(8)));
typedef __bf16 bf16x4 __attribute__((ext_vector_type(4)));
typedef __bf16 bf16x2 __attribute__((ext_vector_type(2)));

constexpr int W0 = 16, WMIN = 12, WMAX = 32;
constexpr int KN = 3, KD = 2;
constexpr int FF0 = 32, FF1 = -16;
constexpr int TG16 = 216;
constexpr int NROUND = 4, MARK = 32, MSTEP = 58, MDEL = 14;
constexpr int LAG0 = 0, LAGHI = 2;
constexpr int QDELAY = 370, PFAT = 176;
constexpr int H2D = 12;

constexpr int D = 512, SK = 32768, FF = 1792, NWG = 256, NSLOT = 8, SLOT = 16384;
constexpr unsigned SENT = 0xFFFFFFFFu;
constexpr int AUX_SC1 = 16, OOB = 0x7ffffff0;

constexpr int WS_EPOCH = 0, WS_GOK = 4096, WS_FOK = 4608, WS_TAU = 8192, WS_QG = 32768, WS_P1 = WS_QG + 65536, WS_P2 = WS_P1 + NWG * 2048,
              WS_PF = WS_P2 + NWG * 1024, WS_END = WS_PF + 64 * 4096;

constexpr int L_S = NSLOT * SLOT, L_CTRL = L_S + 33 * 512, L_XN = L_CTRL + 512, L_Q = L_XN + 2048, L_O = L_Q + 1024, L_M = L_O + 256,
              L_NIL = L_M + 64, L_W = L_NIL + 256, L_BYTES = L_W + 8192;
constexpr int C_LANDED = 64, C_ALL = 0, C_WQ = 1, C_Q = 2, C_CLR = 3, C_FREED = 4, C_PUB = 8, C_FLAG = 9, C_TAG = 10, C_SCHED = 12, C_MAX = 16;
static_assert(NROUND >= 2 && WS_TAU + NROUND * 2048 <= WS_QG && MARK + (NROUND - 1) * MSTEP + MDEL <= 248 && L_BYTES <= 163840, "");
constexpr int SPIN_BUDGET = 1 << 20;

__device__ __forceinline__ unsigned pk_bf16(float lo, float hi) {
  return __builtin_bit_cast(unsigned, __builtin_convertvector(f32x2{lo, hi}, bf16x2));
}
__device__ __forceinline__ float bf_lo(unsigned u) { return __uint_as_float(u << 16); }
__device__ __forceinline__ float bf_hi(unsigned u) { return __uint_as_float(u & 0xffff0000u); }
__device__ __forceinline__ float rbf(float f) { return bf_lo(pk_bf16(f, f)); }
template <int CTRL> __device__ __forceinline__ float dpp(float x) {
  return __builtin_bit_cast(float, __builtin_amdgcn_mov_dpp(__builtin_bit_cast(int, x), CTRL, 0xf, 0xf, true));
}
constexpr int XOR1 = 0xB1, XOR2 = 0x4E, ROR4 = 0x124, ROR8 = 0x128;
__device__ __forceinline__ float xrow_sum(float x) {
  auto s = __builtin_amdgcn_permlane16_swap(__float_as_uint(x), __float_as_uint(x), false, false);
  x = __uint_as_float(s[0]) + __uint_as_float(s[1]);
  auto t = __builtin_amdgcn_permlane32_swap(__float_as_uint(x), __float_as_uint(x), false, false);
  return __uint_as_float(t[0]) + __uint_as_float(t[1]);
}
__device__ __forceinline__ float quad_stride_sum(float x) {
  x += dpp<ROR8>(x); x += dpp<ROR4>(x);
  return xrow_sum(x);
}
__device__ __forceinline__ float rows_sum4(float a, float b, float c, float d) {
  const auto s = __builtin_amdgcn_permlane32_swap(__float_as_uint(a), __float_as_uint(b), false, false);
  const auto t = __builtin_amdgcn_permlane32_swap(__float_as_uint(c), __float_as_uint(d), false, false);
  const float ab = __uint_as_float(s[0]) + __uint_as_float(s[1]), cd = __uint_as_float(t[0]) + __uint_as_float(t[1]);
  const auto u = __builtin_amdgcn_permlane16_swap(__float_as_uint(ab), __float_as_uint(cd), false, false);
  return __uint_as_float(u[0]) + __uint_as_float(u[1]);
}
__device__ __forceinline__ float max2(float a, float b) {
  float top = 3.4028235e38f;
  asm("" : "+s"(top));
  return __builtin_amdgcn_fmed3f(a, b, top);
}
__device__ __forceinline__ float xrow_max(float x) {
  const auto s = __builtin_amdgcn_permlane16_swap(__float_as_uint(x), __float_as_uint(x), false, false);
  x = max2(__uint_as_float(s[0]), __uint_as_float(s[1]));
  const auto t = __builtin_amdgcn_permlane32_swap(__float_as_uint(x), __float_as_uint(x), false, false);
  return max2(__uint_as_float(t[0]), __uint_as_float(t[1]));
}
__device__ __forceinline__ float wave_sum(float x) {
  x += dpp<XOR1>(x); x += dpp<XOR2>(x);
  return quad_stride_sum(x);
}
__device__ __forceinline__ float half_sum(float x) {
  x += dpp<XOR1>(x); x += dpp<XOR2>(x); x += dpp<ROR8>(x); x += dpp<ROR4>(x);
  const auto s = __builtin_amdgcn_permlane16_swap(__float_as_uint(x), __float_as_uint(x), false, false);
  return __uint_as_float(s[0]) + __uint_as_float(s[1]);
}
__device__ __forceinline__ f32x4 mfma32(v4u a, v4u b, f32x4 c) {
  return __builtin_amdgcn_mfma_f32_16x16x32_bf16(__builtin_bit_cast(bf16x8, a), __builtin_bit_cast(bf16x8, b), c, 0, 0, 0);
}
__device__ __forceinline__ v2u lds_tr(LAS char* p) {
  return __builtin_bit_cast(v2u, __builtin_amdgcn_ds_read_tr16_b64_v4bf16((LAS bf16x4*)p));
}
__device__ __forceinline__ v4u cat(v2u a, v2u b) { return v4u{a[0], a[1], b[0], b[1]}; }
__device__ __forceinline__ v4u not_sent(v4u u) {
  return v4u{min(u[0], SENT - 1), min(u[1], SENT - 1), min(u[2], SENT - 1), min(u[3], SENT - 1)};
}
__device__ __forceinline__ float silu(float v) { return v / (1.f + __expf(-v)); }

__device__ __forceinline__ void dma1(unsigned voff, const char* sbase, unsigned ldsaddr) {
  asm volatile("s_mov_b32 m0, %2\n\ts_nop 0\n\tglobal_load_lds_dwordx4 %0, %1 nt" :: "v"(voff), "s"(sbase), "s"(ldsaddr) : "memory");
}
__device__ __forceinline__ void dma4(unsigned v0, unsigned v1, unsigned v2, unsigned v3, const char* sbase, unsigned ldsaddr) {
  asm volatile("s_mov_b32 m0, %5\n\ts_nop 0\n\t"
               "global_load_lds_dwordx4 %0, %4 nt\n\tglobal_load_lds_dwordx4 %1, %4 offset:1024 nt\n\t"
               "global_load_lds_dwordx4 %2, %4 offset:2048 nt\n\tglobal_load_lds_dwordx4 %3, %4 offset:3072 nt"
               :: "v"(v0), "v"(v1), "v"(v2), "v"(v3), "s"(sbase), "s"(ldsaddr) : "memory");
}
template <int N> __device__ __forceinline__ void wait_vm() { asm volatile("s_waitcnt vmcnt(%0)" :: "i"(N) : "memory"); }
#define RUN_FETCH "ds_read_b128 v[100:103], %[ac] offset:48\n\tds_read_b128 v[104:107], %[ac] offset:16\n\ts_memrealtime s[86:87]\n\t"
#define RUN_GROUP(W, STEP, PRE) \
  "s_max_i32 %[pub], %[pub], %[t0]\n\tv_mov_b32 v99, %[pub]\n\ts_mov_b32 m0, %[t1]\n\ts_add_u32 %[t1], %[t1], 0x1000\n\t" \
  "s_add_i32 %[t0], %[t0], 1\n\t" PRE "s_waitcnt vmcnt(" #W ")\n\t" \
  "global_load_lds_dwordx4 %[v0], vcc nt\n\tds_write_b32 %[va], v99\n\tglobal_load_lds_dwordx4 %[v1], vcc offset:1024 nt\n\t" \
  "global_load_lds_dwordx4 %[v2], vcc offset:2048 nt\n\tglobal_load_lds_dwordx4 %[v3], vcc offset:3072 nt\n\t" \
  "s_add_u32 vcc_lo, vcc_lo, " STEP "\n\ts_addc_u32 vcc_hi, vcc_hi, 0\n\t"
#define RUN_LEVEL(W) \
  RUN_GROUP(W, "0x1000", "") RUN_GROUP(W, "%[mid]", "") RUN_GROUP(W, "0x1000", "") RUN_GROUP(W, "0x1000", RUN_FETCH) \
  "s_add_i32 %[seq], %[seq], 1\n\ts_cmp_lt_i32 %[seq], %[end]\n\ts_cbranch_scc1 5b\n\ts_branch 9f\n\t"
__device__ __forceinline__ void run_slots(unsigned v0, unsigned v1, unsigned v2, unsigned v3, const char* src, int mid, int& seq, int end,
                                          int& pub, int& dith, int w16, int ntn, unsigned lbase, unsigned actrl, unsigned va) {
  int t0, t1, t2;
  asm volatile("s_getpc_b64 s[84:85]\n4:\n\ts_add_u32 s84, s84, 7f-4b\n\ts_addc_u32 s85, s85, 0\n\t"
               "s_mov_b64 vcc, %[src]\n\t" RUN_FETCH "5:\n\ts_waitcnt lgkmcnt(0)\n\t"
               "v_min_u32 v104, v104, v105\n\tv_min3_u32 v104, v104, v106, v107\n\tv_readfirstlane_b32 %[t0], v100\n\t"
               "v_readfirstlane_b32 %[t1], v102\n\tv_readfirstlane_b32 %[t2], v101\n\t"
               "s_sub_i32 %[t0], s86, %[t0]\n\tv_readfirstlane_b32 s87, v104\n\ts_mul_i32 %[t0], %[t0], %[t1]\n\ts_ashr_i32 %[t0], %[t0], 8\n\t"
               "s_add_i32 %[t0], %[t0], %[t2]\n\ts_mul_i32 %[t0], %[t0], %[ntn]\n\ts_ashr_i32 %[t0], %[t0], 5\n\ts_lshl_b32 %[t2], %[pub], 4\n\t"
               "s_sub_i32 %[t0], %[t0], %[t2]\n\ts_mul_i32 %[t0], %[t0], %[kn]\n\ts_ashr_i32 %[t0], %[t0], %[kd]\n\t"
               "s_cmp_eq_u32 %[t1], 0\n\ts_cselect_b32 %[t0], 0, %[t0]\n\ts_add_i32 %[dith], %[dith], %[w16]\n\ts_add_i32 %[dith], %[dith], %[t0]\n\t"
               "s_add_i32 %[t2], %[dith], 32\n\ts_ashr_i32 %[t2], %[t2], 6\n\ts_max_i32 %[t2], %[t2], %[lo]\n\ts_min_i32 %[t2], %[t2], %[hi]\n\t"
               "s_lshl_b32 %[t0], %[t2], 6\n\ts_sub_i32 %[dith], %[dith], %[t0]\n\ts_max_i32 %[dith], %[dith], -32\n\ts_min_i32 %[dith], %[dith], 32\n\t"
               "s_add_i32 %[t0], %[seq], -8\n\ts_cmp_le_i32 s87, %[t0]\n\ts_cbranch_scc1 9f\n\t"
               "s_add_i32 s86, %[t2], -3\n\ts_mul_i32 s86, s86, 8f-7f\n\ts_lshl_b32 %[t0], %[seq], 2\n\ts_sub_i32 %[t0], %[t0], %[t2]\n\t"
               "s_and_b32 %[t1], %[seq], 7\n\ts_lshl_b32 %[t1], %[t1], 14\n\ts_add_u32 %[t1], %[t1], %[lb]\n\t"
               "s_add_u32 s86, s84, s86\n\ts_addc_u32 s87, s85, 0\n\ts_setpc_b64 s[86:87]\n7:\n\t"
               RUN_LEVEL(12) "8:\n\t" RUN_LEVEL(16) RUN_LEVEL(20) RUN_LEVEL(24) RUN_LEVEL(28) RUN_LEVEL(32) "9:\n\ts_waitcnt lgkmcnt(0)"
               : [seq] "+&s"(seq), [pub] "+&s"(pub), [dith] "+&s"(dith), [t0] "=&s"(t0), [t1] "=&s"(t1), [t2] "=&s"(t2)
               : [v0] "v"(v0), [v1] "v"(v1), [v2] "v"(v2), [v3] "v"(v3), [src] "s"(src), [mid] "s"(mid), [end] "s"(end),
                 [w16] "s"(w16), [ntn] "s"(ntn), [lb] "s"(lbase), [ac] "v"(actrl), [va] "v"(va), [kn] "i"(KN), [kd] "i"(KD / 2),
                 [lo] "i"(WMIN / 4), [hi] "i"(WMAX / 4)
               : "v99", "v100", "v101", "v102", "v103", "v104", "v105", "v106", "v107", "s84", "s85", "s86", "s87", "vcc", "scc", "memory");
}
static_assert(NSLOT == 8 && SLOT == 16384 && C_FREED == 4 && C_SCHED == 12 && (KD == 1 || KD == 2) && WMIN >= 12 && WMAX <= 32, "");
__device__ __forceinline__ int opaque(int v) { asm volatile("" : "+s"(v)); return v; }
__device__ __forceinline__ void wait_lgkm() { asm volatile("s_waitcnt lgkmcnt(0)" ::: "memory"); }

typedef volatile LAS unsigned* ctrl_t;
template <int SLEEP = 0> __device__ __forceinline__ unsigned lds_wait_ge(ctrl_t ctrl, int word, unsigned want, int& budget) {
  unsigned v, tmp;
  int go;
  budget = __builtin_amdgcn_readfirstlane(budget);
  want = __builtin_amdgcn_readfirstlane(want);
  asm volatile("1:\n\tds_read_b32 %[t], %[a]\n\t.if %[slp]\n\ts_sleep %[slp]\n\t.endif\n\ts_waitcnt lgkmcnt(0)\n\tv_readfirstlane_b32 %[v], %[t]\n\t"
               "s_cmp_lt_u32 %[v], %[w]\n\ts_cselect_b32 %[g], %[b], 0\n\ts_subb_u32 %[b], %[b], 0\n\ts_cmp_gt_i32 %[g], 0\n\ts_cbranch_scc1 1b"
               : [v] "=&s"(v), [t] "=&v"(tmp), [g] "=&s"(go), [b] "+&s"(budget)
               : [a] "v"((unsigned)(uintptr_t)(ctrl + word)), [w] "s"(want), [slp] "i"(SLEEP) : "scc", "memory");
  return v;
}
#define BOX_LOOP_END \
  "v_cmpx_eq_u32 vcc, -1, %[t]\n\ts_sub_i32 %[b], %[b], 1\n\ts_cmp_gt_i32 %[b], 0\n\ts_cselect_b64 vcc, exec, 0\n\t" \
  "s_cmp_lg_u64 vcc, 0\n\ts_cbranch_scc1 1b\n\ts_mov_b64 exec, -1"
__device__ __forceinline__ v4u poll_box(__amdgpu_buffer_rsrc_t rs, int off, int& budget) {
  v4u a;
  unsigned tmp;
  budget = __builtin_amdgcn_readfirstlane(budget);
  asm volatile("1:\n\tbuffer_load_dwordx4 v[100:103], %[o], %[rs], 0 offen sc1\n\ts_waitcnt vmcnt(0)\n\t"
               "v_max_u32 %[t], v100, v101\n\tv_max3_u32 %[t], %[t], v102, v103\n\t" BOX_LOOP_END
               : "=&{v[100:103]}"(a), [t] "=&v"(tmp), [b] "+&s"(budget) : [o] "v"(off), [rs] "s"(rs) : "vcc", "scc", "memory");
  return a;
}
__device__ __forceinline__ void poll_box2(__amdgpu_buffer_rsrc_t rs, int off0, int off1, v4u& a, v4u& b, int& budget) {
  unsigned tmp;
  budget = __builtin_amdgcn_readfirstlane(budget);
  asm volatile("1:\n\tbuffer_load_dwordx4 v[100:103], %[o0], %[rs], 0 offen sc1\n\tbuffer_load_dwordx4 v[104:107], %[o1], %[rs], 0 offen sc1\n\t"
               "s_waitcnt vmcnt(0)\n\tv_max_u32 %[t], v100, v101\n\tv_max3_u32 %[t], %[t], v102, v103\n\t"
               "v_max3_u32 %[t], %[t], v104, v105\n\tv_max3_u32 %[t], %[t], v106, v107\n\t" BOX_LOOP_END
               : "=&{v[100:103]}"(a), "=&{v[104:107]}"(b), [t] "=&v"(tmp), [b] "+&s"(budget)
               : [o0] "v"(off0), [o1] "v"(off1), [rs] "s"(rs) : "vcc", "scc", "memory");
}
__device__ __forceinline__ void poll_lds_o(unsigned addr, v4u& a, v4u& b, int& budget) {
  unsigned tmp;
  budget = __builtin_amdgcn_readfirstlane(budget);
  asm volatile("1:\n\tds_read_b64 v[100:101], %[o]\n\tds_read_b64 v[102:103], %[o] offset:32\n\tds_read_b64 v[104:105], %[o] offset:64\n\t"
               "ds_read_b64 v[106:107], %[o] offset:96\n\ts_waitcnt lgkmcnt(0)\n\tv_max_u32 %[t], v100, v101\n\tv_max3_u32 %[t], %[t], v102, v103\n\t"
               "v_max3_u32 %[t], %[t], v104, v105\n\tv_max3_u32 %[t], %[t], v106, v107\n\t" BOX_LOOP_END
               : "=&{v[100:103]}"(a), "=&{v[104:107]}"(b), [t] "=&v"(tmp), [b] "+&s"(budget) : [o] "v"(addr) : "vcc", "scc", "memory");
}

typedef unsigned v16u __attribute__((ext_vector_type(16)));
__device__ __forceinline__ char* as_ptr(unsigned lo, unsigned hi) { return (char*)(((u64)hi << 32) | lo); }

__global__ void __launch_bounds__(448, 2) fused_decode(const char*, const char*, const char*, const char*, const char*, const char*, const char*,
                                                    char*, char*) {
  v16u ka16;
  v2u ka2;
  asm volatile("s_load_dwordx16 s[36:51], %2, 0x0\n\ts_load_dwordx2 s[52:53], %2, 0x40"
               : "={s[36:51]}"(ka16), "={s[52:53]}"(ka2) : "s"(__builtin_amdgcn_kernarg_segment_ptr()));
  __builtin_amdgcn_sched_barrier(0);
  extern __shared__ __attribute__((aligned(16))) char smem[];
  LAS char* L = (LAS char*)smem;
  const unsigned lbase = (unsigned)(uintptr_t)L;
  ctrl_t ctrl = (ctrl_t)(L + L_CTRL);
  const int lane = threadIdx.x & 63, bid = blockIdx.x;
  int wave = __builtin_amdgcn_readfirstlane(threadIdx.x >> 6);
  const int xg = bid & 7, tg = bid >> 3, kvh = tg >> 2, ii = tg & 3, gi = 4 * xg + ii, wid = kvh * 32 + gi, bb = gi >> 4;
  const int nt = ii == 0 ? 33 : ii == 1 ? 31 : 32, tile0 = (xg & 3) * 128 + (ii == 1 ? 33 : 32 * ii);
  const int g_kv = 8 * nt, g_wo = g_kv + 4;
  const int g = lane >> 4, col = lane & 15, q4 = (lane >> 2) & 3, p4 = lane & 3;
  const int ffn = xg * 8 + kvh;
  const bool has_ffn = ii == 1;
  unsigned vk[4], vx = lane * 16, vq = (lane >> 1) * 8192 + (lane & 1) * 16;
#pragma unroll
  for (int j = 0; j < 4; ++j) vk[j] = g * 256 + ((col ^ (4 * j + g)) * 16);
  unsigned kvoff = ((bb * 8 + kvh) * SK + tile0 * 64) * 256, qoff = (kvh * 512 + gi * 16) * 2;
  asm volatile("" : "+v"(vk[0]), "+v"(vk[1]), "+v"(vk[2]), "+v"(vk[3]), "+v"(vx), "+v"(vq));
  asm volatile("" : "+s"(kvoff), "+s"(qoff), "+s"(wave));
  ctrl[lane] = 0u;
  ctrl[C_LANDED + lane] = 0u;
  *(volatile LAS unsigned*)(L + L_O + lane * 4) = SENT;
  asm volatile("s_waitcnt lgkmcnt(0)\n\ts_barrier" : "+{s[36:51]}"(ka16), "+{s[52:53]}"(ka2) :: "memory");
  const char *x = as_ptr(ka16[0], ka16[1]), *kc = as_ptr(ka16[2], ka16[3]), *vc = as_ptr(ka16[4], ka16[5]), *wq = as_ptr(ka16[6], ka16[7]),
             *wo = as_ptr(ka16[8], ka16[9]), *w1 = as_ptr(ka16[10], ka16[11]), *w2 = as_ptr(ka16[12], ka16[13]);
  char *out = as_ptr(ka16[14], ka16[15]), *ws = as_ptr(ka2[0], ka2[1]);
  const __amdgpu_buffer_rsrc_t rs = __builtin_amdgcn_make_buffer_rsrc((void*)ws, 0, WS_END, 0x00020000);
  const unsigned t_start = (unsigned)__builtin_amdgcn_s_memrealtime();
  if (wave == 1) {
    __builtin_amdgcn_s_setprio(3);
    const u64 pk_ = (u64)(kc + kvoff), pv_ = (u64)(vc + kvoff), pq_ = (u64)(wq + qoff), pq2 = pq_ + 511 * 8192;
    asm volatile("v_writelane_b32 v100, %0, 0\n\tv_writelane_b32 v101, %1, 0\n\tv_writelane_b32 v100, %2, 1\n\tv_writelane_b32 v101, %3, 1\n\t"
                 "v_writelane_b32 v100, %4, 2\n\tv_writelane_b32 v101, %5, 2\n\tv_writelane_b32 v100, %6, 3\n\tv_writelane_b32 v101, %7, 3\n\t"
                 "v_writelane_b32 v100, %8, 4\n\tv_writelane_b32 v101, %9, 4\n\tv_writelane_b32 v100, %10, 5\n\tv_writelane_b32 v101, %11, 5\n\t"
                 "s_mov_b32 m0, %12\n\ts_mov_b64 exec, 63\n\tglobal_load_lds_dword v[100:101], off nt\n\ts_mov_b64 exec, -1"
                 :: "s"((unsigned)pk_), "s"((unsigned)(pk_ >> 32)), "s"((unsigned)pv_), "s"((unsigned)(pv_ >> 32)), "s"(ka16[0]), "s"(ka16[1]),
                    "s"(ka2[0]), "s"(ka2[1]), "s"((unsigned)pq_), "s"((unsigned)(pq_ >> 32)), "s"((unsigned)pq2), "s"((unsigned)(pq2 >> 32)),
                    "s"(lbase + L_NIL) : "v100", "v101", "memory");
#pragma unroll
    for (int j = 0; j < 4; ++j) dma4(vk[0], vk[1], vk[2], vk[3], kc + kvoff + j * 4096, lbase + j * 4096);
  }
  if (opaque(wave) == 0) {
    asm volatile("s_mov_b32 m0, %4\n\ts_nop 0\n\tglobal_load_lds_dwordx4 %0, %1\n\tglobal_load_lds_dwordx4 %0, %1 offset:1024\n\t"
                 "s_mov_b32 m0, %5\n\ts_nop 0\n\tglobal_load_lds_dwordx4 %2, %3 sc1"
                 :: "v"(vx), "s"(x), "v"(WS_EPOCH), "s"(ws), "s"(lbase + L_W), "s"(lbase + L_W + 2048) : "memory");
  }
  if (opaque(wave) == 6) {
#pragma nounroll
    for (int j = 0; j < 16; ++j) dma1(vq, wq + qoff + (size_t)j * (32 * 8192), lbase + L_S + j * 1024);
    wait_vm<0>();
    if (lane == 0) ctrl[C_WQ] = 1u;
  }
  const int role = opaque(wave);
  int budget = SPIN_BUDGET;

  if (__builtin_expect(role == 1, 1)) {
    unsigned vv[2];
#pragma unroll
    for (int j = 0; j < 2; ++j) vv[j] = g * 256 + ((col ^ ((4 * j + g) << 1)) * 16);
    const char* kb = kc + kvoff;
    const char* vb = vc + kvoff;
    const char* wb = wo + (size_t)(kvh * 512 + 32 * (gi >> 3) + 8 * ((gi >> 2) & 1)) * 1024 + ii * 256;
    const unsigned va_pub = lbase + L_CTRL + (C_LANDED + lane) * 4;
    const int w16 = W0 * 16 + (ii == 0 ? FF0 : ii == 1 ? FF1 : 0);
    int seq = 1, pub = 0, dith = 0;
#define DRAIN_TO(N) { wait_vm<4 * (N)>(); pub = max(pub, 4 * seq - (N)); ctrl[C_LANDED + lane] = pub; }
#pragma nounroll
    for (int ph = 0; ph < 3; ++ph) {
      unsigned v[4];
#pragma unroll
      for (int j = 0; j < 4; ++j) v[j] = ph == 0 ? vk[j] : vv[j & 1] + (ph == 2 ? g * 768 + j * (127 * 1024) : 0);
      const char* base = ph == 0 ? kb : ph == 1 ? vb : wb;
      const int first = ph * nt, end = ph == 2 ? first + 1 : first + nt;
      if (ph == 2) {
        DRAIN_TO(8) DRAIN_TO(7) DRAIN_TO(6) DRAIN_TO(5) DRAIN_TO(4) DRAIN_TO(3) DRAIN_TO(2) DRAIN_TO(1) DRAIN_TO(0)
      }
      for (;;) {
        run_slots(v[0], v[1], v[2], v[3], base + (seq - first) * SLOT, ph == 2 ? 12288 : 4096, seq, end, pub, dith, ph == 2 ? 1024 : w16, nt, lbase,
                  lbase + L_CTRL, va_pub);
        if (seq >= end) break;
        DRAIN_TO(0)
        int fr;
        do {
          __builtin_amdgcn_s_sleep(1);
          const v4u f = *(volatile LAS v4u*)(ctrl + C_FREED);
          fr = __builtin_amdgcn_readfirstlane(min(min(f[0], f[1]), min(f[2], f[3])));
        } while (fr <= seq - NSLOT && --budget > 0);
        if (budget <= 0) { ctrl[C_LANDED + lane] = g_wo; return; }
      }
    }
    DRAIN_TO(0)
#undef DRAIN_TO
    return;
  }

  if (role == 0) {
    u64 tag;
    v4u xb[16];
    {
      wait_vm<0>();
      tag = *(volatile LAS u64*)(L + L_W + 2048) + 1;
      const v4u xv0 = *(volatile LAS v4u*)(L + L_W + lane * 32), xv1 = *(volatile LAS v4u*)(L + L_W + lane * 32 + 16);
      float v[16];
#pragma unroll
      for (int e = 0; e < 4; ++e) { v[2 * e] = bf_lo(xv0[e]); v[2 * e + 1] = bf_hi(xv0[e]); v[8 + 2 * e] = bf_lo(xv1[e]); v[9 + 2 * e] = bf_hi(xv1[e]); }
      float s4[4], q4s[4];
#pragma unroll
      for (int e = 0; e < 4; ++e) s4[e] = (v[e] + v[4 + e]) + (v[8 + e] + v[12 + e]);
      const float mean = half_sum((s4[0] + s4[1]) + (s4[2] + s4[3])) * (1.f / D);
#pragma unroll
      for (int e = 0; e < 4; ++e) {
        const float d0 = v[e] - mean, d1 = v[4 + e] - mean, d2 = v[8 + e] - mean, d3 = v[12 + e] - mean;
        q4s[e] = fmaf(d0, d0, d1 * d1) + fmaf(d2, d2, d3 * d3);
      }
      const float var = half_sum((q4s[0] + q4s[1]) + (q4s[2] + q4s[3])) * (1.f / D);
      const float mb = rbf(mean), den = rbf(__builtin_amdgcn_sqrtf(rbf(rbf(var) + 1e-5f))), rden = __builtin_amdgcn_rcpf(den);
      unsigned o[8];
#pragma unroll
      for (int e = 0; e < 8; ++e) {
        const unsigned dp = pk_bf16(v[2 * e] - mb, v[2 * e + 1] - mb);
        const float d0 = bf_lo(dp), d1 = bf_hi(dp), q0 = d0 * rden, q1 = d1 * rden;
        o[e] = pk_bf16(fmaf(fmaf(-q0, den, d0), rden, q0), fmaf(fmaf(-q1, den, d1), rden, q1));
      }
      *(LAS v4u*)(L + L_XN + lane * 32) = v4u{o[0], o[1], o[2], o[3]};
      *(LAS v4u*)(L + L_XN + lane * 32 + 16) = v4u{o[4], o[5], o[6], o[7]};
      asm volatile("" ::: "memory");
      LAS char* xa = L + L_XN + (((col >> 1) & 1) * 512 + 4 * g) * 2;
#pragma unroll
      for (int ks = 0; ks < 16; ++ks) xb[ks] = cat(*(LAS v2u*)(xa + ks * 64), *(LAS v2u*)(xa + ks * 64 + 32));
      __builtin_amdgcn_sched_barrier(0);
      lds_wait_ge(ctrl, C_WQ, 1u, budget);
      f32x4 qp[2] = {{0.f, 0.f, 0.f, 0.f}, {0.f, 0.f, 0.f, 0.f}};
      LAS char* wa = L + L_S + 8 * lane;
      v4u fa[16];
#pragma unroll
      for (int ks = 0; ks < 16; ++ks) fa[ks] = cat(lds_tr(wa + ks * 1024), lds_tr(wa + ks * 1024 + 512));
      __builtin_amdgcn_sched_barrier(0);
#pragma unroll
      for (int ks = 0; ks < 16; ++ks) qp[ks & 1] = mfma32(fa[ks], xb[ks], qp[ks & 1]);
      const f32x4 qa = qp[0] + qp[1];
      if ((col & 13) == 0) *(LAS v2u*)(L + L_Q + (col >> 1) * 32 + g * 8) = v2u{pk_bf16(qa[0], qa[1]), pk_bf16(qa[2], qa[3])};
      asm volatile("" ::: "memory");
      const unsigned thi = (unsigned)(tag >> 16), tlo = (unsigned)tag << 16;
      const unsigned qv = *(LAS unsigned short*)(L + L_Q + (lane & 31) * 2);
      __builtin_amdgcn_raw_buffer_store_b64(v2u{tlo | qv, thi}, rs, lane < 32 ? WS_QG + ((kvh * 2 + g) * 512 + gi * 16 + col) * 8 : OOB, 0, AUX_SC1);
      if (lane == 0) *(volatile LAS u64*)(ctrl + C_TAG) = tag;
    }
    {
      const unsigned thi = (unsigned)(tag >> 16), tlo = (unsigned)tag << 16;
      while ((int)((unsigned)__builtin_amdgcn_s_memrealtime() - t_start) < QDELAY) __builtin_amdgcn_s_sleep(2);
      const int qo = WS_QG + (kvh * 2 + bb) * 4096 + lane * 8;
      v2u a[8];
#pragma unroll
      for (int k = 0; k < 8; ++k) a[k] = v2u{~tlo, ~thi};
      for (;;) {
        asm volatile("" ::: "memory");
        bool all = true;
        v2u nv[8];
#pragma unroll
        for (int k = 0; k < 8; ++k) {
          const bool ok = a[k][1] == thi && ((a[k][0] ^ tlo) >> 16) == 0;
          nv[k] = __builtin_amdgcn_raw_buffer_load_b64(rs, ok ? OOB : qo + 512 * k, 0, AUX_SC1);
        }
#pragma unroll
        for (int k = 0; k < 8; ++k) {
          const bool ok = a[k][1] == thi && ((a[k][0] ^ tlo) >> 16) == 0;
          a[k] = ok ? a[k] : nv[k];
          all = all && a[k][1] == thi && ((a[k][0] ^ tlo) >> 16) == 0;
        }
        if (__all(all)) break;
        if (--budget <= 0) break;
        __builtin_amdgcn_s_sleep(4);
      }
#pragma unroll
      for (int k = 0; k < 8; ++k) *(LAS unsigned short*)(L + L_Q + (64 * k + lane) * 2) = (unsigned short)a[k][0];
      wait_lgkm();
      if (lane == 0) ctrl[C_Q] = 1u;
    }
    auto ffn_slice = [&]() {
      const int u0 = (28 * ffn) & ~7, lo = 28 * ffn - u0;
      const unsigned vo1 = (lane >> 2) * (FF * 2) + ((p4 ^ ((lane >> 3) & 2)) * 16);
      LAS char* wa = L + L_W + (4 * g + q4) * 64 + 8 * p4;
      f32x4 h0 = {0.f, 0.f, 0.f, 0.f}, h1 = h0;
#pragma unroll
      for (int rd = 0; rd < 4; ++rd) {
#pragma unroll
        for (int j = 0; j < 8; ++j) dma1(vo1, w1 + u0 * 2 + (size_t)(rd * 128 + j * 16) * (FF * 2), lbase + L_W + j * 1024);
        wait_vm<0>();
        v4u fa[4][2];
#pragma unroll
        for (int ks = 0; ks < 4; ++ks)
#pragma unroll
          for (int nb = 0; nb < 2; ++nb) {
            LAS char* p = wa + ks * 2048 + ((nb ^ (g & 1)) * 32);
            fa[ks][nb] = cat(lds_tr(p), lds_tr(p + 1024));
          }
        __builtin_amdgcn_sched_barrier(0);
        wait_lgkm();
#pragma unroll
        for (int ks = 0; ks < 4; ++ks) { h0 = mfma32(fa[ks][0], xb[4 * rd + ks], h0); h1 = mfma32(fa[ks][1], xb[4 * rd + ks], h1); }
      }
      float hv[8];
#pragma unroll
      for (int j = 0; j < 8; ++j) {
        const int u = (j >> 2) * 16 + 4 * g + (j & 3);
        hv[j] = (u >= lo && u < lo + 28) ? silu(rbf(j < 4 ? h0[j & 3] : h1[j & 3])) : 0.f;
      }
      const v4u hb = {pk_bf16(hv[0], hv[1]), pk_bf16(hv[2], hv[3]), pk_bf16(hv[4], hv[5]), pk_bf16(hv[6], hv[7])};
      const unsigned vo2[2] = {(unsigned)(g * 1024 + ((col ^ (g << 1)) * 16)), (unsigned)(g * 1024 + ((col ^ ((4 + g) << 1)) * 16))};
      const int sw = (4 * (g & 1) + q4) << 1;
      LAS char* w2a = L + L_W + (4 * g + q4) * 256 + 8 * (p4 & 1);
      const int mine = 2 * (col >> 2) + (col & 1);
#pragma unroll
      for (int cb = 0; cb < 4; ++cb) {
#pragma unroll
        for (int j = 0; j < 8; ++j) dma1(vo2[j & 1], w2 + (size_t)(u0 + 4 * j) * 1024 + cb * 256, lbase + L_W + j * 1024);
        wait_vm<0>();
        v4u wf[8];
#pragma unroll
        for (int nb = 0; nb < 8; ++nb) {
          LAS char* p = w2a + (((2 * nb + (p4 >> 1)) ^ sw) * 16);
          wf[nb] = cat(lds_tr(p), lds_tr(p + 4096));
        }
        __builtin_amdgcn_sched_barrier(0);
        wait_lgkm();
        f32x4 fo = {0.f, 0.f, 0.f, 0.f};
#pragma unroll
        for (int nb = 0; nb < 8; ++nb) fo = mfma32(wf[nb], mine == nb ? hb : v4u{0u, 0u, 0u, 0u}, fo);
        __builtin_amdgcn_raw_buffer_store_b128(not_sent(__builtin_bit_cast(v4u, fo)), rs, WS_PF + ffn * 4096 + cb * 1024 + lane * 16, 0, AUX_SC1);
      }
    };
    if (has_ffn) {
      ffn_slice();
      wait_vm<0>();
      if (lane == 0) __hip_atomic_store((GAS u64*)(ws + WS_FOK) + ffn, tag, RLX_AGENT);
    }

    lds_wait_ge<8>(ctrl, C_ALL, 1u, budget);
    const int cb = xg >> 1, pc = (xg & 1) * 32 + tg, cw = pc >> 4, gw = (pc >> 2) & 3;
    const int jj = (g >> 1) | ((g & 1) << 1), oidx = ((pc >> 1) & 1) * 512 + cb * 128 + cw * 32 + (pc & 1) * 16 + gw * 4 + jj;
    const float xin = __uint_as_float((unsigned)*(const GAS unsigned short*)(x + oidx * 2) << 16);
    float gate = 0.f;
    const __amdgpu_buffer_rsrc_t ro = __builtin_amdgcn_make_buffer_rsrc((void*)out, 0, 2 * D * 2, 0x00020000);
    int out_off = col == 0 ? oidx * 2 : OOB;
    __builtin_amdgcn_raw_buffer_store_b16((unsigned short)0, ro, out_off, 0, 0);
    const unsigned touch = *(const GAS unsigned short*)(wo + (size_t)(kvh * 512 + g * 128 + 4 * gi) * 1024);
    asm volatile("" : "+v"(out_off) : "v"(touch));
#pragma nounroll
    for (int pass = 0; pass < 2; ++pass) {
      const bool last = opaque(pass) != 0;
      if (last) { lds_wait_ge<8>(ctrl, C_LANDED, g_kv - 4, budget); lds_wait_ge(ctrl, C_PUB, 1u, budget); __builtin_amdgcn_s_sleep(H2D); }
      else {
        lds_wait_ge<8>(ctrl, C_LANDED, PFAT, budget);
        while (!__all(__hip_atomic_load((GAS u64*)(ws + WS_FOK) + lane, RLX_AGENT) == tag) && --budget > 0) __builtin_amdgcn_s_sleep(8);
      }
      const int po = (last ? WS_P2 + pc * 16 : WS_PF + gw * 256 + cw * 64 + (pc & 3) * 16) + lane * 4096 + cb * 1024;
      const v4u sv = poll_box(rs, po, budget);
      float t = rows_sum4(__uint_as_float(sv[0]), __uint_as_float(sv[1]), __uint_as_float(sv[2]), __uint_as_float(sv[3]));
      t += dpp<XOR1>(t); t += dpp<XOR2>(t); t += dpp<ROR8>(t); t += dpp<ROR4>(t);
      t = rbf(t);
      if (last) {
        const float y = xin + rbf(t * gate);
        __builtin_amdgcn_raw_buffer_store_b16(budget <= 0 ? (unsigned short)0x7fc0 : (unsigned short)pk_bf16(y, y), ro, out_off, 0, 0);
        break;
      }
      gate = rbf(silu(t));
    }
    return;
  }

  if (role == 6) {
    unsigned at_t = 0;
    int at_p = 0;
    lds_wait_ge<8>(ctrl, C_Q, 1u, budget);
    const u64 tag = *(volatile LAS u64*)(ctrl + C_TAG);
    lds_wait_ge<8>(ctrl, C_CLR, 4u, budget);
#pragma nounroll
    for (int k = 0; k < NROUND; ++k) {
      GAS u64* line = (GAS u64*)(ws + WS_TAU) + (k ? k * 256 : kvh * 32);
      const int at = MARK + k * MSTEP, mark = (at * nt) >> 5;
      lds_wait_ge<1>(ctrl, C_LANDED, mark, budget);
      const unsigned tau = (unsigned)__builtin_amdgcn_s_memrealtime();
      if (lane == 0 && (k == 0 || ii == 3)) __hip_atomic_store(line + (k ? ffn : gi), (tag << 16) | (tau & 0xffffu), RLX_AGENT);
      lds_wait_ge<4>(ctrl, C_LANDED, mark + MDEL, budget);
      const u64 v = __hip_atomic_load(line + (lane & (k ? 63 : 31)), RLX_AGENT);
      const unsigned seen_t = ((v ^ (tag << 16)) >> 16) == 0 ? (unsigned)v : (unsigned)__builtin_amdgcn_s_memrealtime();
      const float d = wave_sum((float)(short)(seen_t - tau));
      const unsigned mean = tau + __builtin_amdgcn_readfirstlane((int)(d * (1.f / 64)));
      constexpr int rate = 65536 / TG16;
      const int common = k ? at_p + (((int)(mean - at_t) * rate) >> 8) - at * 16 : LAG0 * 16;
      at_t = mean;
      at_p = at * 16 + min(max(common, 0), LAGHI * 16);
      if (lane == 0) *(volatile LAS v4u*)(ctrl + C_SCHED) = v4u{at_t, (unsigned)at_p, (unsigned)rate, 0u};
      if (k == 0) {
        while (!__all(((__hip_atomic_load(line + (lane & 31), RLX_AGENT) ^ (tag << 16)) >> 16) == 0) && --budget > 0) __builtin_amdgcn_s_sleep(8);
        if (lane == 0) ctrl[C_FLAG] = 1u;
        if (gi == 0 && lane == 0) __hip_atomic_store((GAS u64*)(ws + WS_GOK) + kvh, tag, RLX_AGENT);
      } else if (k == 1) {
        while (!__all(__hip_atomic_load((GAS u64*)(ws + WS_GOK) + (lane & 7), RLX_AGENT) == tag) && --budget > 0) __builtin_amdgcn_s_sleep(8);
        if (lane == 0) ctrl[C_ALL] = 1u;
        if (bid == 0 && lane == 0) __hip_atomic_store((GAS u64*)(ws + WS_EPOCH), tag, RLX_AGENT);
      }
    }
    return;
  }

  const int c = role - 2;
  const int blk = col >> 2;
  constexpr float SCALE = 0.08838834764831845f, LOG2E = 1.4426950408889634f;
  unsigned seen = 0u;
  auto need = [&](unsigned want) {
    if (seen < want) seen = lds_wait_ge(ctrl, C_LANDED, want, budget);
    asm volatile("" ::: "memory");
  };
  float ms;
  {
    unsigned ka[4];
#pragma unroll
    for (int k = 0; k < 4; ++k) ka[k] = col * 256 + (((4 * k + g) ^ col) * 16);
    v4u kf[4][4];
    auto fetch_k = [&](int t) {
      need(4 * t + 4);
      LAS char* sb = L + (t & (NSLOT - 1)) * SLOT;
#pragma unroll
      for (int k = 0; k < 4; ++k)
#pragma unroll
        for (int a = 0; a < 4; ++a) kf[a][k] = *(LAS v4u*)(sb + ka[k] + a * 4096);
      __builtin_amdgcn_sched_barrier(0);
      wait_lgkm();
      if (lane == 0) ctrl[C_FREED + c] = min(t + 4, nt);
      __builtin_amdgcn_sched_barrier(0);
    };
    fetch_k(c);
    const v4u sv = {SENT, SENT, SENT, SENT};
    if (c < 3) __builtin_amdgcn_raw_buffer_store_b128(sv, rs, (c < 2 ? WS_P1 + wid * 2048 + c * 1024 : WS_P2 + wid * 1024) + lane * 16, 0, AUX_SC1);
    wait_vm<0>();
    if (lane == 0) __hip_atomic_fetch_add((LAS unsigned*)(ctrl + C_CLR), 1u, __ATOMIC_RELAXED, __HIP_MEMORY_SCOPE_WORKGROUP);
    lds_wait_ge<2>(ctrl, C_Q, 1u, budget);
    v4u qf[4][4];
#pragma unroll
    for (int k = 0; k < 4; ++k) {
      const v4u qv = *(LAS v4u*)(L + L_Q + ((col & 3) * 128 + 32 * k + 8 * g) * 2);
#pragma unroll
      for (int a = 0; a < 4; ++a) qf[a][k] = blk == a ? qv : v4u{0u, 0u, 0u, 0u};
    }
    float mx = -3.0e38f;
#pragma nounroll
    for (int t = c;; t += 4) {
      f32x4 s0 = {0.f, 0.f, 0.f, 0.f}, s1 = {0.f, 0.f, 0.f, 0.f};
#pragma unroll
      for (int k = 0; k < 4; ++k) {
        s0 = mfma32(kf[0][k], qf[0][k], s0); s1 = mfma32(kf[1][k], qf[1][k], s1);
        s0 = mfma32(kf[2][k], qf[2][k], s0); s1 = mfma32(kf[3][k], qf[3][k], s1);
      }
      s0 += s1;
      const unsigned a0 = pk_bf16(s0[0], s0[1]), a1 = pk_bf16(s0[2], s0[3]);
      const unsigned b0 = pk_bf16(bf_lo(a0) * SCALE, bf_hi(a0) * SCALE), b1 = pk_bf16(bf_lo(a1) * SCALE, bf_hi(a1) * SCALE);
      *(LAS v2u*)(L + L_S + t * 512 + lane * 8) = v2u{b0, b1};
      mx = max2(max2(mx, max2(bf_lo(b0), bf_hi(b0))), max2(bf_lo(b1), bf_hi(b1)));
      if (t + 4 >= nt) break;
      fetch_k(t + 4);
    }
    mx = max2(mx, dpp<ROR8>(mx)); mx = max2(mx, dpp<ROR4>(mx)); mx = xrow_max(mx);
    if (lane < 4) *(LAS float*)(L + L_M + lane * 16 + c * 4) = mx;
    wait_lgkm();
    if (lane == 0) __hip_atomic_fetch_add((LAS unsigned*)(ctrl + C_MAX), 1u, __ATOMIC_RELAXED, __HIP_MEMORY_SCOPE_WORKGROUP);
    lds_wait_ge(ctrl, C_MAX, 4u, budget);
    const f32x4 m4 = *(LAS f32x4*)(L + L_M + (col & 3) * 16);
    ms = max2(max2(m4[0], m4[1]), max2(m4[2], m4[3])) * LOG2E;
  }
  unsigned va[2];
#pragma unroll
  for (int n = 0; n < 2; ++n)
    va[n] = (4 * g + q4) * 256 + (((4 * c + 2 * n + (p4 >> 1)) ^ ((4 * (g & 1) + q4) << 1)) * 16) + 8 * (p4 & 1);
  int p1_off = col < 8 ? WS_P1 + wid * 2048 + c * 512 + g * 128 + col * 16 : OOB;
  int p2_off = col < 4 ? WS_P2 + wid * 1024 + c * 256 + g * 64 + col * 16 : OOB;
  int rec0 = WS_P1 + (kvh * 32 + (c >> 1) * 16 + (lane >> 3)) * 2048 + (gi >> 2) * 256 + (c & 1) * 128 + (lane & 7) * 16, rec1 = rec0 + 8 * 2048;
  unsigned o_wr = (lane & 40) == 0 ? L_O + c * 64 + col * 8 + g * 4 : L_NIL + lane * 4, o_rd = lbase + L_O + ((col >> 1) & 1) * 128 + g * 8;
  unsigned wo0 = ((2 * nt) & (NSLOT - 1)) * SLOT + va[0], wo1 = ((2 * nt) & (NSLOT - 1)) * SLOT + va[1];
  asm volatile("" : "+v"(p1_off), "+v"(p2_off), "+v"(rec0), "+v"(rec1), "+v"(o_wr), "+v"(o_rd), "+v"(wo0), "+v"(wo1));
  float lsum = 0.f, linv = 0.f;
  unsigned lam = 0;
  f32x4 acc0 = {0.f, 0.f, 0.f, 0.f}, acc1 = {0.f, 0.f, 0.f, 0.f};
#pragma nounroll
  for (int t = 0; t < nt; ++t) {
    const int seq = nt + t;
    const v2u sc = *(LAS v2u*)(L + L_S + t * 512 + lane * 8);
    const float e0 = __builtin_amdgcn_exp2f(fmaf(bf_lo(sc[0]), LOG2E, -ms)), e1 = __builtin_amdgcn_exp2f(fmaf(bf_hi(sc[0]), LOG2E, -ms));
    const float e2 = __builtin_amdgcn_exp2f(fmaf(bf_lo(sc[1]), LOG2E, -ms)), e3 = __builtin_amdgcn_exp2f(fmaf(bf_hi(sc[1]), LOG2E, -ms));
    lsum += (e0 + e1) + (e2 + e3);
    const unsigned p0 = pk_bf16(e0, e1), p1 = pk_bf16(e2, e3);
    v4u pA = {blk == 0 ? p0 : 0u, blk == 0 ? p1 : 0u, blk == 1 ? p0 : 0u, blk == 1 ? p1 : 0u};
    v4u pB = {blk == 2 ? p0 : 0u, blk == 2 ? p1 : 0u, blk == 3 ? p0 : 0u, blk == 3 ? p1 : 0u};
    if (t == nt - 1) {
      lds_wait_ge(ctrl, C_FLAG, 1u, budget);
      const float lt = quad_stride_sum(lsum);
      linv = __builtin_amdgcn_rcpf(lt);
      lam = __float_as_uint(ms + __builtin_amdgcn_logf(lt));
    }
    asm volatile("" : "+v"(pA), "+v"(pB), "+v"(lsum), "+v"(linv), "+v"(lam));
    LAS char* sb = L + (seq & (NSLOT - 1)) * SLOT;
    need(4 * seq + 2);
    acc0 = mfma32(cat(lds_tr(sb + va[0]), lds_tr(sb + va[0] + 4096)), pA, acc0);
    acc1 = mfma32(cat(lds_tr(sb + va[1]), lds_tr(sb + va[1] + 4096)), pA, acc1);
    need(4 * seq + 4);
    v4u v0 = cat(lds_tr(sb + va[0] + 8192), lds_tr(sb + va[0] + 12288)), v1 = cat(lds_tr(sb + va[1] + 8192), lds_tr(sb + va[1] + 12288));
    wait_lgkm();
    if (lane == 0) ctrl[C_FREED + c] = seq + 1;
    asm volatile("" : "+v"(v0), "+v"(v1) :: "memory");
    acc0 = mfma32(v0, pB, acc0);
    acc1 = mfma32(v1, pB, acc1);
  }

  v4u pk;
#pragma unroll
  for (int j = 0; j < 4; ++j) {
    float a = ((col & 4) ? acc1[j] : acc0[j]) + dpp<ROR4>((col & 4) ? acc0[j] : acc1[j]);
    a += dpp<ROR8>(a);
    pk[j] = ((__float_as_uint(a * linv) + 0x80u) & 0xffffff00u) | ((lam >> (8 * j)) & 0xffu);
  }
  __builtin_amdgcn_raw_buffer_store_b128(not_sent(pk), rs, p1_off, 0, AUX_SC1);
  v4u a0, a1, wa[4];
  need(g_wo);
#pragma unroll
  for (int n = 0; n < 4; ++n) wa[n] = cat(lds_tr(L + ((n & 1) ? wo1 : wo0) + (n >> 1) * 4096), lds_tr(L + ((n & 1) ? wo1 : wo0) + (n >> 1) * 4096 + 8192));
  __builtin_amdgcn_sched_barrier(0);
  poll_box2(rs, rec0, rec1, a0, a1, budget);
  const float lA = __uint_as_float(__builtin_amdgcn_perm(__builtin_amdgcn_perm(a0[3], a0[2], 0x04000c0cu), __builtin_amdgcn_perm(a0[1], a0[0], 0x0c0c0400u), 0x07060100u));
  const float lB = __uint_as_float(__builtin_amdgcn_perm(__builtin_amdgcn_perm(a1[3], a1[2], 0x04000c0cu), __builtin_amdgcn_perm(a1[1], a1[0], 0x0c0c0400u), 0x07060100u));
  float M = max2(lA, lB);
  M = xrow_max(max2(M, dpp<ROR8>(M)));
  const float wA = __builtin_amdgcn_exp2f(lA - M), wB = __builtin_amdgcn_exp2f(lB - M);
  float r[4];
#pragma unroll
  for (int j = 0; j < 4; ++j) r[j] = fmaf(wA, __uint_as_float(a0[j] & 0xffffff00u), wB * __uint_as_float(a1[j] & 0xffffff00u));
  float wsum = xrow_sum(wA + wB), ov = rows_sum4(r[0], r[1], r[2], r[3]);
  wsum += dpp<ROR8>(wsum); ov += dpp<ROR8>(ov);
  ov *= __builtin_amdgcn_rcpf(wsum);
  const auto pr = __builtin_amdgcn_permlane32_swap(__float_as_uint(ov), __float_as_uint(ov), false, false);
  *(LAS unsigned*)(L + o_wr) = min(pk_bf16(__uint_as_float(pr[0]), __uint_as_float(pr[1])), SENT - 1);
  v4u o0, o1;
  poll_lds_o(o_rd, o0, o1, budget);
  const f32x4 zero = {0.f, 0.f, 0.f, 0.f};
  const f32x4 sp0 = mfma32(wa[2], o1, mfma32(wa[0], o0, zero)), sp1 = mfma32(wa[3], o1, mfma32(wa[1], o0, zero));
  const f32x4 sp = (col & 1) ? sp1 : sp0;
  __builtin_amdgcn_raw_buffer_store_b128(not_sent(__builtin_bit_cast(v4u, sp)), rs, p2_off, 0, AUX_SC1);
  ctrl[C_PUB] = 1u;
}

extern "C" void kernel_launch(void* const* d_in, const int* in_sizes, int n_in, void* d_out, int out_size, void* d_ws, size_t ws_size,
                              hipStream_t stream) {
  (void)hipFuncSetAttribute((const void*)fused_decode, hipFuncAttributeMaxDynamicSharedMemorySize, L_BYTES);
  hipLaunchKernelGGL(fused_decode, dim3(NWG), dim3(448), L_BYTES, stream, (const char*)d_in[0], (const char*)d_in[1], (const char*)d_in[2],
                     (const char*)d_in[3], (const char*)d_in[4], (const char*)d_in[6], (const char*)d_in[7], (char*)d_out, (char*)d_ws);
}
```

```cpp
#include <hip/hip_runtime.h>
#include <hip/hip_bf16.h>
#include <stdint.h>

#define GAS __attribute__((address_space(1)))
#define LAS __attribute__((address_space(3)))
#define RLX_AGENT __ATOMIC_RELAXED, __HIP_MEMORY_SCOPE_AGENT
typedef unsigned long long u64;
typedef unsigned v4u __attribute__((ext_vector_type(4)));
typedef unsigned v2u __attribute__((ext_vector_type(2)));
typedef float f32x4 __attribute__((ext_vector_type(4)));
typedef float f32x2 __attribute__((ext_vector_type(2)));
typedef __bf16 bf16x8 __attribute__((ext_vector_type(8)));
typedef __bf16 bf16x4 __attribute__((ext_vector_type(4)));
typedef __bf16 bf16x2 __attribute__((ext_vector_type(2)));

constexpr int W0 = 16, WMIN = 12, WMAX = 32;
constexpr int KN = 3, KD = 2;
constexpr int FF0 = 32, FF1 = -16;
constexpr int TG16 = 216;
constexpr int NROUND = 4, MARK = 32, MSTEP = 58, MDEL = 14;
constexpr int LAG0 = 0, LAGHI = 2;
constexpr int QDELAY = 370, PFAT = 176;
constexpr int H2D = 12;

constexpr int D = 512, SK = 32768, FF = 1792, NWG = 256, NSLOT = 8, SLOT = 16384;
constexpr unsigned SENT = 0xFFFFFFFFu;
constexpr int AUX_SC1 = 16, OOB = 0x7ffffff0;

constexpr int WS_EPOCH = 0, WS_GOK = 4096, WS_FOK = 4608, WS_TAU = 8192, WS_QG = 32768, WS_P1 = WS_QG + 65536, WS_P2 = WS_P1 + NWG * 2048,
              WS_PF = WS_P2 + NWG * 1024, WS_END = WS_PF + 64 * 4096;

constexpr int L_S = NSLOT * SLOT, L_CTRL = L_S + 33 * 512, L_XN = L_CTRL + 512, L_Q = L_XN + 2048, L_O = L_Q + 1024, L_M = L_O + 256,
              L_NIL = L_M + 64, L_W = L_NIL + 256, L_BYTES = L_W + 8192;
constexpr int C_LANDED = 64, C_ALL = 0, C_WQ = 1, C_Q = 2, C_CLR = 3, C_FREED = 4, C_PUB = 8, C_FLAG = 9, C_TAG = 10, C_SCHED = 12, C_MAX = 16;
static_assert(NROUND >= 2 && WS_TAU + NROUND * 2048 <= WS_QG && MARK + (NROUND - 1) * MSTEP + MDEL <= 248 && L_BYTES <= 163840, "");
constexpr int SPIN_BUDGET = 1 << 20;

__device__ __forceinline__ unsigned pk_bf16(float lo, float hi) {
  return __builtin_bit_cast(unsigned, __builtin_convertvector(f32x2{lo, hi}, bf16x2));
}
__device__ __forceinline__ float bf_lo(unsigned u) { return __uint_as_float(u << 16); }
__device__ __forceinline__ float bf_hi(unsigned u) { return __uint_as_float(u & 0xffff0000u); }
__device__ __forceinline__ float rbf(float f) { return bf_lo(pk_bf16(f, f)); }
template <int CTRL> __device__ __forceinline__ float dpp(float x) {
  return __builtin_bit_cast(float, __builtin_amdgcn_mov_dpp(__builtin_bit_cast(int, x), CTRL, 0xf, 0xf, true));
}
constexpr int XOR1 = 0xB1, XOR2 = 0x4E, ROR4 = 0x124, ROR8 = 0x128;
__device__ __forceinline__ float xrow_sum(float x) {
  auto s = __builtin_amdgcn_permlane16_swap(__float_as_uint(x), __float_as_uint(x), false, false);
  x = __uint_as_float(s[0]) + __uint_as_float(s[1]);
  auto t = __builtin_amdgcn_permlane32_swap(__float_as_uint(x), __float_as_uint(x), false, false);
  return __uint_as_float(t[0]) + __uint_as_float(t[1]);
}
__device__ __forceinline__ float quad_stride_sum(float x) {
  x += dpp<ROR8>(x); x += dpp<ROR4>(x);
  return xrow_sum(x);
}
__device__ __forceinline__ float rows_sum4(float a, float b, float c, float d) {
  const auto s = __builtin_amdgcn_permlane32_swap(__float_as_uint(a), __float_as_uint(b), false, false);
  const auto t = __builtin_amdgcn_permlane32_swap(__float_as_uint(c), __float_as_uint(d), false, false);
  const float ab = __uint_as_float(s[0]) + __uint_as_float(s[1]), cd = __uint_as_float(t[0]) + __uint_as_float(t[1]);
  const auto u = __builtin_amdgcn_permlane16_swap(__float_as_uint(ab), __float_as_uint(cd), false, false);
  return __uint_as_float(u[0]) + __uint_as_float(u[1]);
}
__device__ __forceinline__ float max2(float a, float b) {
  float top = 3.4028235e38f;
  asm("" : "+s"(top));
  return __builtin_amdgcn_fmed3f(a, b, top);
}
__device__ __forceinline__ float xrow_max(float x) {
  const auto s = __builtin_amdgcn_permlane16_swap(__float_as_uint(x), __float_as_uint(x), false, false);
  x = max2(__uint_as_float(s[0]), __uint_as_float(s[1]));
  const auto t = __builtin_amdgcn_permlane32_swap(__float_as_uint(x), __float_as_uint(x), false, false);
  return max2(__uint_as_float(t[0]), __uint_as_float(t[1]));
}
__device__ __forceinline__ float wave_sum(float x) {
  x += dpp<XOR1>(x); x += dpp<XOR2>(x);
  return quad_stride_sum(x);
}
__device__ __forceinline__ float half_sum(float x) {
  x += dpp<XOR1>(x); x += dpp<XOR2>(x); x += dpp<ROR8>(x); x += dpp<ROR4>(x);
  const auto s = __builtin_amdgcn_permlane16_swap(__float_as_uint(x), __float_as_uint(x), false, false);
  return __uint_as_float(s[0]) + __uint_as_float(s[1]);
}
__device__ __forceinline__ f32x4 mfma32(v4u a, v4u b, f32x4 c) {
  return __builtin_amdgcn_mfma_f32_16x16x32_bf16(__builtin_bit_cast(bf16x8, a), __builtin_bit_cast(bf16x8, b), c, 0, 0, 0);
}
__device__ __forceinline__ v2u lds_tr(LAS char* p) {
  return __builtin_bit_cast(v2u, __builtin_amdgcn_ds_read_tr16_b64_v4bf16((LAS bf16x4*)p));
}
__device__ __forceinline__ v4u cat(v2u a, v2u b) { return v4u{a[0], a[1], b[0], b[1]}; }
__device__ __forceinline__ v4u not_sent(v4u u) {
  return v4u{min(u[0], SENT - 1), min(u[1], SENT - 1), min(u[2], SENT - 1), min(u[3], SENT - 1)};
}
__device__ __forceinline__ float silu(float v) { return v / (1.f + __expf(-v)); }

template <bool NT = true> __device__ __forceinline__ void dma1(unsigned voff, const char* sbase, unsigned ldsaddr) {
  if (NT) asm volatile("s_mov_b32 m0, %2\n\ts_nop 0\n\tglobal_load_lds_dwordx4 %0, %1 nt" :: "v"(voff), "s"(sbase), "s"(ldsaddr) : "memory");
  else asm volatile("s_mov_b32 m0, %2\n\ts_nop 0\n\tglobal_load_lds_dwordx4 %0, %1" :: "v"(voff), "s"(sbase), "s"(ldsaddr) : "memory");
}
__device__ __forceinline__ void dma4(unsigned v0, unsigned v1, unsigned v2, unsigned v3, const char* sbase, unsigned ldsaddr) {
  asm volatile("s_mov_b32 m0, %5\n\ts_nop 0\n\t"
               "global_load_lds_dwordx4 %0, %4 nt\n\tglobal_load_lds_dwordx4 %1, %4 offset:1024 nt\n\t"
               "global_load_lds_dwordx4 %2, %4 offset:2048 nt\n\tglobal_load_lds_dwordx4 %3, %4 offset:3072 nt"
               :: "v"(v0), "v"(v1), "v"(v2), "v"(v3), "s"(sbase), "s"(ldsaddr) : "memory");
}
template <int N> __device__ __forceinline__ void wait_vm() { asm volatile("s_waitcnt vmcnt(%0)" :: "i"(N) : "memory"); }
#define RUN_FETCH "ds_read_b128 v[100:103], %[ac] offset:48\n\tds_read_b128 v[104:107], %[ac] offset:16\n\ts_memrealtime s[86:87]\n\t"
#define RUN_GROUP(W, STEP, PRE) \
  "s_max_i32 %[pub], %[pub], %[t0]\n\tv_mov_b32 v99, %[pub]\n\ts_mov_b32 m0, %[t1]\n\ts_add_u32 %[t1], %[t1], 0x1000\n\t" \
  "s_add_i32 %[t0], %[t0], 1\n\t" PRE "s_waitcnt vmcnt(" #W ")\n\t" \
  "global_load_lds_dwordx4 %[v0], vcc nt\n\tds_write_b32 %[va], v99\n\tglobal_load_lds_dwordx4 %[v1], vcc offset:1024 nt\n\t" \
  "global_load_lds_dwordx4 %[v2], vcc offset:2048 nt\n\tglobal_load_lds_dwordx4 %[v3], vcc offset:3072 nt\n\t" \
  "s_add_u32 vcc_lo, vcc_lo, " STEP "\n\ts_addc_u32 vcc_hi, vcc_hi, 0\n\t"
#define RUN_LEVEL(W) \
  RUN_GROUP(W, "0x1000", "") RUN_GROUP(W, "%[mid]", "") RUN_GROUP(W, "0x1000", "") RUN_GROUP(W, "0x1000", RUN_FETCH) \
  "s_add_i32 %[seq], %[seq], 1\n\ts_cmp_lt_i32 %[seq], %[end]\n\ts_cbranch_scc1 5b\n\ts_branch 9f\n\t"
__device__ __forceinline__ void run_slots(unsigned v0, unsigned v1, unsigned v2, unsigned v3, const char* src, int mid, int& seq, int end,
                                          int& pub, int& dith, int w16, int ntn, unsigned lbase, unsigned actrl, unsigned va) {
  int t0, t1, t2;
  asm volatile("s_getpc_b64 s[84:85]\n4:\n\ts_add_u32 s84, s84, 7f-4b\n\ts_addc_u32 s85, s85, 0\n\t"
               "s_mov_b64 vcc, %[src]\n\t" RUN_FETCH "5:\n\ts_waitcnt lgkmcnt(0)\n\t"
               "v_min_u32 v104, v104, v105\n\tv_min3_u32 v104, v104, v106, v107\n\tv_readfirstlane_b32 %[t0], v100\n\t"
               "v_readfirstlane_b32 %[t1], v102\n\tv_readfirstlane_b32 %[t2], v101\n\t"
               "s_sub_i32 %[t0], s86, %[t0]\n\tv_readfirstlane_b32 s87, v104\n\ts_mul_i32 %[t0], %[t0], %[t1]\n\ts_ashr_i32 %[t0], %[t0], 8\n\t"
               "s_add_i32 %[t0], %[t0], %[t2]\n\ts_mul_i32 %[t0], %[t0], %[ntn]\n\ts_ashr_i32 %[t0], %[t0], 5\n\ts_lshl_b32 %[t2], %[pub], 4\n\t"
               "s_sub_i32 %[t0], %[t0], %[t2]\n\ts_mul_i32 %[t0], %[t0], %[kn]\n\ts_ashr_i32 %[t0], %[t0], %[kd]\n\t"
               "s_cmp_eq_u32 %[t1], 0\n\ts_cselect_b32 %[t0], 0, %[t0]\n\ts_add_i32 %[dith], %[dith], %[w16]\n\ts_add_i32 %[dith], %[dith], %[t0]\n\t"
               "s_add_i32 %[t2], %[dith], 32\n\ts_ashr_i32 %[t2], %[t2], 6\n\ts_max_i32 %[t2], %[t2], %[lo]\n\ts_min_i32 %[t2], %[t2], %[hi]\n\t"
               "s_lshl_b32 %[t0], %[t2], 6\n\ts_sub_i32 %[dith], %[dith], %[t0]\n\ts_max_i32 %[dith], %[dith], -32\n\ts_min_i32 %[dith], %[dith], 32\n\t"
               "s_add_i32 %[t0], %[seq], -8\n\ts_cmp_le_i32 s87, %[t0]\n\ts_cbranch_scc1 9f\n\t"
               "s_add_i32 s86, %[t2], -3\n\ts_mul_i32 s86, s86, 8f-7f\n\ts_lshl_b32 %[t0], %[seq], 2\n\ts_sub_i32 %[t0], %[t0], %[t2]\n\t"
               "s_and_b32 %[t1], %[seq], 7\n\ts_lshl_b32 %[t1], %[t1], 14\n\ts_add_u32 %[t1], %[t1], %[lb]\n\t"
               "s_add_u32 s86, s84, s86\n\ts_addc_u32 s87, s85, 0\n\ts_setpc_b64 s[86:87]\n7:\n\t"
               RUN_LEVEL(12) "8:\n\t" RUN_LEVEL(16) RUN_LEVEL(20) RUN_LEVEL(24) RUN_LEVEL(28) RUN_LEVEL(32) "9:\n\ts_waitcnt lgkmcnt(0)"
               : [seq] "+&s"(seq), [pub] "+&s"(pub), [dith] "+&s"(dith), [t0] "=&s"(t0), [t1] "=&s"(t1), [t2] "=&s"(t2)
               : [v0] "v"(v0), [v1] "v"(v1), [v2] "v"(v2), [v3] "v"(v3), [src] "s"(src), [mid] "s"(mid), [end] "s"(end),
                 [w16] "s"(w16), [ntn] "s"(ntn), [lb] "s"(lbase), [ac] "v"(actrl), [va] "v"(va), [kn] "i"(KN), [kd] "i"(KD / 2),
                 [lo] "i"(WMIN / 4), [hi] "i"(WMAX / 4)
               : "v99", "v100", "v101", "v102", "v103", "v104", "v105", "v106", "v107", "s84", "s85", "s86", "s87", "vcc", "scc", "memory");
}
static_assert(NSLOT == 8 && SLOT == 16384 && C_FREED == 4 && C_SCHED == 12 && (KD == 1 || KD == 2) && WMIN >= 12 && WMAX <= 32, "");
__device__ __forceinline__ int opaque(int v) { asm volatile("" : "+s"(v)); return v; }
__device__ __forceinline__ void wait_lgkm() { asm volatile("s_waitcnt lgkmcnt(0)" ::: "memory"); }

typedef volatile LAS unsigned* ctrl_t;
template <int SLEEP = 0> __device__ __forceinline__ unsigned lds_wait_ge(ctrl_t ctrl, int word, unsigned want, int& budget) {
  unsigned v, tmp;
  int go;
  budget = __builtin_amdgcn_readfirstlane(budget);
  want = __builtin_amdgcn_readfirstlane(want);
  asm volatile("1:\n\tds_read_b32 %[t], %[a]\n\t.if %[slp]\n\ts_sleep %[slp]\n\t.endif\n\ts_waitcnt lgkmcnt(0)\n\tv_readfirstlane_b32 %[v], %[t]\n\t"
               "s_cmp_lt_u32 %[v], %[w]\n\ts_cselect_b32 %[g], %[b], 0\n\ts_subb_u32 %[b], %[b], 0\n\ts_cmp_gt_i32 %[g], 0\n\ts_cbranch_scc1 1b"
               : [v] "=&s"(v), [t] "=&v"(tmp), [g] "=&s"(go), [b] "+&s"(budget)
               : [a] "v"((unsigned)(uintptr_t)(ctrl + word)), [w] "s"(want), [slp] "i"(SLEEP) : "scc", "memory");
  return v;
}
#define BOX_LOOP_END \
  "v_cmpx_eq_u32 vcc, -1, %[t]\n\ts_sub_i32 %[b], %[b], 1\n\ts_cmp_gt_i32 %[b], 0\n\ts_cselect_b64 vcc, exec, 0\n\t" \
  "s_cmp_lg_u64 vcc, 0\n\ts_cbranch_scc1 1b\n\ts_mov_b64 exec, -1"
__device__ __forceinline__ v4u poll_box(__amdgpu_buffer_rsrc_t rs, int off, int& budget) {
  v4u a;
  unsigned tmp;
  budget = __builtin_amdgcn_readfirstlane(budget);
  asm volatile("1:\n\tbuffer_load_dwordx4 v[100:103], %[o], %[rs], 0 offen sc1\n\ts_waitcnt vmcnt(0)\n\t"
               "v_max_u32 %[t], v100, v101\n\tv_max3_u32 %[t], %[t], v102, v103\n\t" BOX_LOOP_END
               : "=&{v[100:103]}"(a), [t] "=&v"(tmp), [b] "+&s"(budget) : [o] "v"(off), [rs] "s"(rs) : "vcc", "scc", "memory");
  return a;
}
__device__ __forceinline__ void poll_box2(__amdgpu_buffer_rsrc_t rs, int off0, int off1, v4u& a, v4u& b, int& budget) {
  unsigned tmp;
  budget = __builtin_amdgcn_readfirstlane(budget);
  asm volatile("1:\n\tbuffer_load_dwordx4 v[100:103], %[o0], %[rs], 0 offen sc1\n\tbuffer_load_dwordx4 v[104:107], %[o1], %[rs], 0 offen sc1\n\t"
               "s_waitcnt vmcnt(0)\n\tv_max_u32 %[t], v100, v101\n\tv_max3_u32 %[t], %[t], v102, v103\n\t"
               "v_max3_u32 %[t], %[t], v104, v105\n\tv_max3_u32 %[t], %[t], v106, v107\n\t" BOX_LOOP_END
               : "=&{v[100:103]}"(a), "=&{v[104:107]}"(b), [t] "=&v"(tmp), [b] "+&s"(budget)
               : [o0] "v"(off0), [o1] "v"(off1), [rs] "s"(rs) : "vcc", "scc", "memory");
}
__device__ __forceinline__ void poll_lds_o(unsigned addr, v4u& a, v4u& b, int& budget) {
  unsigned tmp;
  budget = __builtin_amdgcn_readfirstlane(budget);
  asm volatile("1:\n\tds_read_b64 v[100:101], %[o]\n\tds_read_b64 v[102:103], %[o] offset:32\n\tds_read_b64 v[104:105], %[o] offset:64\n\t"
               "ds_read_b64 v[106:107], %[o] offset:96\n\ts_waitcnt lgkmcnt(0)\n\tv_max_u32 %[t], v100, v101\n\tv_max3_u32 %[t], %[t], v102, v103\n\t"
               "v_max3_u32 %[t], %[t], v104, v105\n\tv_max3_u32 %[t], %[t], v106, v107\n\t" BOX_LOOP_END
               : "=&{v[100:103]}"(a), "=&{v[104:107]}"(b), [t] "=&v"(tmp), [b] "+&s"(budget) : [o] "v"(addr) : "vcc", "scc", "memory");
}

typedef unsigned v16u __attribute__((ext_vector_type(16)));
__device__ __forceinline__ char* as_ptr(unsigned lo, unsigned hi) { return (char*)(((u64)hi << 32) | lo); }

__global__ void __launch_bounds__(448, 2) fused_decode(const char*, const char*, const char*, const char*, const char*, const char*, const char*,
                                                    char*, char*) {
  v16u ka16;
  v2u ka2;
  asm volatile("s_load_dwordx16 s[36:51], %2, 0x0\n\ts_load_dwordx2 s[52:53], %2, 0x40"
               : "={s[36:51]}"(ka16), "={s[52:53]}"(ka2) : "s"(__builtin_amdgcn_kernarg_segment_ptr()));
  __builtin_amdgcn_sched_barrier(0);
  extern __shared__ __attribute__((aligned(16))) char smem[];
  LAS char* L = (LAS char*)smem;
  const unsigned lbase = (unsigned)(uintptr_t)L;
  ctrl_t ctrl = (ctrl_t)(L + L_CTRL);
  const int lane = threadIdx.x & 63, bid = blockIdx.x;
  int wave = __builtin_amdgcn_readfirstlane(threadIdx.x >> 6);
  const int xg = bid & 7, tg = bid >> 3, kvh = tg >> 2, ii = tg & 3, gi = 4 * xg + ii, wid = kvh * 32 + gi, bb = gi >> 4;
  const bool has_ffn = ii == 1 && kvh < 7, has_33 = ii == 0 && kvh < 7;
  const int nt = has_33 ? 33 : has_ffn ? 31 : 32, tile0 = (xg & 3) * 128 + (has_ffn ? 33 : 32 * ii);
  const int g_kv = 8 * nt, g_wo = g_kv + 4;
  const int g = lane >> 4, col = lane & 15, q4 = (lane >> 2) & 3, p4 = lane & 3;
  const int ffn = xg * 7 + kvh;
  unsigned vk[4], vx = lane * 16, vq = (lane >> 1) * 8192 + (lane & 1) * 16;
#pragma unroll
  for (int j = 0; j < 4; ++j) vk[j] = g * 256 + ((col ^ (4 * j + g)) * 16);
  unsigned kvoff = ((bb * 8 + kvh) * SK + tile0 * 64) * 256, qoff = (kvh * 512 + gi * 16) * 2;
  asm volatile("" : "+v"(vk[0]), "+v"(vk[1]), "+v"(vk[2]), "+v"(vk[3]), "+v"(vx), "+v"(vq));
  asm volatile("" : "+s"(kvoff), "+s"(qoff), "+s"(wave));
  ctrl[lane] = 0u;
  ctrl[C_LANDED + lane] = 0u;
  *(volatile LAS unsigned*)(L + L_O + lane * 4) = SENT;
  asm volatile("s_waitcnt lgkmcnt(0)\n\ts_barrier" : "+{s[36:51]}"(ka16), "+{s[52:53]}"(ka2) :: "memory");
  const char *x = as_ptr(ka16[0], ka16[1]), *kc = as_ptr(ka16[2], ka16[3]), *vc = as_ptr(ka16[4], ka16[5]), *wq = as_ptr(ka16[6], ka16[7]),
             *wo = as_ptr(ka16[8], ka16[9]), *w1 = as_ptr(ka16[10], ka16[11]), *w2 = as_ptr(ka16[12], ka16[13]);
  char *out = as_ptr(ka16[14], ka16[15]), *ws = as_ptr(ka2[0], ka2[1]);
  const __amdgpu_buffer_rsrc_t rs = __builtin_amdgcn_make_buffer_rsrc((void*)ws, 0, WS_END, 0x00020000);
  const unsigned t_start = (unsigned)__builtin_amdgcn_s_memrealtime();
  if (wave == 1) {
    __builtin_amdgcn_s_setprio(3);
    const u64 pk_ = (u64)(kc + kvoff), pv_ = (u64)(vc + kvoff), pq_ = (u64)(wq + qoff), pq2 = pq_ + 511 * 8192;
    asm volatile("v_writelane_b32 v100, %0, 0\n\tv_writelane_b32 v101, %1, 0\n\tv_writelane_b32 v100, %2, 1\n\tv_writelane_b32 v101, %3, 1\n\t"
                 "v_writelane_b32 v100, %4, 2\n\tv_writelane_b32 v101, %5, 2\n\tv_writelane_b32 v100, %6, 3\n\tv_writelane_b32 v101, %7, 3\n\t"
                 "v_writelane_b32 v100, %8, 4\n\tv_writelane_b32 v101, %9, 4\n\tv_writelane_b32 v100, %10, 5\n\tv_writelane_b32 v101, %11, 5\n\t"
                 "s_mov_b32 m0, %12\n\ts_mov_b64 exec, 63\n\tglobal_load_lds_dword v[100:101], off nt\n\ts_mov_b64 exec, -1"
                 :: "s"((unsigned)pk_), "s"((unsigned)(pk_ >> 32)), "s"((unsigned)pv_), "s"((unsigned)(pv_ >> 32)), "s"(ka16[0]), "s"(ka16[1]),
                    "s"(ka2[0]), "s"(ka2[1]), "s"((unsigned)pq_), "s"((unsigned)(pq_ >> 32)), "s"((unsigned)pq2), "s"((unsigned)(pq2 >> 32)),
                    "s"(lbase + L_NIL) : "v100", "v101", "memory");
#pragma unroll
    for (int j = 0; j < 4; ++j) dma4(vk[0], vk[1], vk[2], vk[3], kc + kvoff + j * 4096, lbase + j * 4096);
  }
  if (opaque(wave) == 0) {
    asm volatile("s_mov_b32 m0, %4\n\ts_nop 0\n\tglobal_load_lds_dwordx4 %0, %1\n\tglobal_load_lds_dwordx4 %0, %1 offset:1024\n\t"
                 "s_mov_b32 m0, %5\n\ts_nop 0\n\tglobal_load_lds_dwordx4 %2, %3 sc1"
                 :: "v"(vx), "s"(x), "v"(WS_EPOCH), "s"(ws), "s"(lbase + L_W), "s"(lbase + L_W + 2048) : "memory");
  }
  if (opaque(wave) == 6) {
#pragma nounroll
    for (int j = 0; j < 16; ++j) dma1(vq, wq + qoff + (size_t)j * (32 * 8192), lbase + L_S + j * 1024);
    wait_vm<0>();
    if (lane == 0) ctrl[C_WQ] = 1u;
  }
  const int role = opaque(wave);
  int budget = SPIN_BUDGET;

  if (__builtin_expect(role == 1, 1)) {
    unsigned vv[2];
#pragma unroll
    for (int j = 0; j < 2; ++j) vv[j] = g * 256 + ((col ^ ((4 * j + g) << 1)) * 16);
    const char* kb = kc + kvoff;
    const char* vb = vc + kvoff;
    const char* wb = wo + (size_t)(kvh * 512 + 32 * (gi >> 3) + 8 * ((gi >> 2) & 1)) * 1024 + ii * 256;
    const unsigned va_pub = lbase + L_CTRL + (C_LANDED + lane) * 4;
    const int w16 = W0 * 16 + (has_33 ? FF0 : has_ffn ? FF1 : 0);
    int seq = 1, pub = 0, dith = 0;
#define DRAIN_TO(N) { wait_vm<4 * (N)>(); pub = max(pub, 4 * seq - (N)); ctrl[C_LANDED + lane] = pub; }
#pragma nounroll
    for (int ph = 0; ph < 3; ++ph) {
      unsigned v[4];
#pragma unroll
      for (int j = 0; j < 4; ++j) v[j] = ph == 0 ? vk[j] : vv[j & 1] + (ph == 2 ? g * 768 + j * (127 * 1024) : 0);
      const char* base = ph == 0 ? kb : ph == 1 ? vb : wb;
      const int first = ph * nt, end = ph == 2 ? first + 1 : first + nt;
      if (ph == 2) {
        DRAIN_TO(8) DRAIN_TO(7) DRAIN_TO(6) DRAIN_TO(5) DRAIN_TO(4) DRAIN_TO(3) DRAIN_TO(2) DRAIN_TO(1) DRAIN_TO(0)
      }
      for (;;) {
        run_slots(v[0], v[1], v[2], v[3], base + (seq - first) * SLOT, ph == 2 ? 12288 : 4096, seq, end, pub, dith, ph == 2 ? 1024 : w16, nt, lbase,
                  lbase + L_CTRL, va_pub);
        if (seq >= end) break;
        DRAIN_TO(0)
        int fr;
        do {
          __builtin_amdgcn_s_sleep(1);
          const v4u f = *(volatile LAS v4u*)(ctrl + C_FREED);
          fr = __builtin_amdgcn_readfirstlane(min(min(f[0], f[1]), min(f[2], f[3])));
        } while (fr <= seq - NSLOT && --budget > 0);
        if (budget <= 0) { ctrl[C_LANDED + lane] = g_wo; return; }
      }
    }
    DRAIN_TO(0)
#undef DRAIN_TO
    return;
  }

  if (role == 0) {
    u64 tag;
    v4u xb[16];
    {
      wait_vm<0>();
      tag = *(volatile LAS u64*)(L + L_W + 2048) + 1;
      const v4u xv0 = *(volatile LAS v4u*)(L + L_W + lane * 32), xv1 = *(volatile LAS v4u*)(L + L_W + lane * 32 + 16);
      float v[16];
#pragma unroll
      for (int e = 0; e < 4; ++e) { v[2 * e] = bf_lo(xv0[e]); v[2 * e + 1] = bf_hi(xv0[e]); v[8 + 2 * e] = bf_lo(xv1[e]); v[9 + 2 * e] = bf_hi(xv1[e]); }
      float s4[4], q4s[4];
#pragma unroll
      for (int e = 0; e < 4; ++e) s4[e] = (v[e] + v[4 + e]) + (v[8 + e] + v[12 + e]);
      const float mean = half_sum((s4[0] + s4[1]) + (s4[2] + s4[3])) * (1.f / D);
#pragma unroll
      for (int e = 0; e < 4; ++e) {
        const float d0 = v[e] - mean, d1 = v[4 + e] - mean, d2 = v[8 + e] - mean, d3 = v[12 + e] - mean;
        q4s[e] = fmaf(d0, d0, d1 * d1) + fmaf(d2, d2, d3 * d3);
      }
      const float var = half_sum((q4s[0] + q4s[1]) + (q4s[2] + q4s[3])) * (1.f / D);
      const float mb = rbf(mean), den = rbf(__builtin_amdgcn_sqrtf(rbf(rbf(var) + 1e-5f))), rden = __builtin_amdgcn_rcpf(den);
      unsigned o[8];
#pragma unroll
      for (int e = 0; e < 8; ++e) {
        const unsigned dp = pk_bf16(v[2 * e] - mb, v[2 * e + 1] - mb);
        const float d0 = bf_lo(dp), d1 = bf_hi(dp), q0 = d0 * rden, q1 = d1 * rden;
        o[e] = pk_bf16(fmaf(fmaf(-q0, den, d0), rden, q0), fmaf(fmaf(-q1, den, d1), rden, q1));
      }
      *(LAS v4u*)(L + L_XN + lane * 32) = v4u{o[0], o[1], o[2], o[3]};
      *(LAS v4u*)(L + L_XN + lane * 32 + 16) = v4u{o[4], o[5], o[6], o[7]};
      asm volatile("" ::: "memory");
      LAS char* xa = L + L_XN + (((col >> 1) & 1) * 512 + 4 * g) * 2;
#pragma unroll
      for (int ks = 0; ks < 16; ++ks) xb[ks] = cat(*(LAS v2u*)(xa + ks * 64), *(LAS v2u*)(xa + ks * 64 + 32));
      __builtin_amdgcn_sched_barrier(0);
      lds_wait_ge(ctrl, C_WQ, 1u, budget);
      f32x4 qp[2] = {{0.f, 0.f, 0.f, 0.f}, {0.f, 0.f, 0.f, 0.f}};
      LAS char* wa = L + L_S + 8 * lane;
      v4u fa[16];
#pragma unroll
      for (int ks = 0; ks < 16; ++ks) fa[ks] = cat(lds_tr(wa + ks * 1024), lds_tr(wa + ks * 1024 + 512));
      __builtin_amdgcn_sched_barrier(0);
#pragma unroll
      for (int ks = 0; ks < 16; ++ks) qp[ks & 1] = mfma32(fa[ks], xb[ks], qp[ks & 1]);
      const f32x4 qa = qp[0] + qp[1];
      if ((col & 13) == 0) *(LAS v2u*)(L + L_Q + (col >> 1) * 32 + g * 8) = v2u{pk_bf16(qa[0], qa[1]), pk_bf16(qa[2], qa[3])};
      asm volatile("" ::: "memory");
      const unsigned thi = (unsigned)(tag >> 16), tlo = (unsigned)tag << 16;
      const unsigned qv = *(LAS unsigned short*)(L + L_Q + (lane & 31) * 2);
      __builtin_amdgcn_raw_buffer_store_b64(v2u{tlo | qv, thi}, rs, lane < 32 ? WS_QG + ((kvh * 2 + g) * 512 + gi * 16 + col) * 8 : OOB, 0, AUX_SC1);
      if (lane == 0) *(volatile LAS u64*)(ctrl + C_TAG) = tag;
    }
    {
      const unsigned thi = (unsigned)(tag >> 16), tlo = (unsigned)tag << 16;
      while ((int)((unsigned)__builtin_amdgcn_s_memrealtime() - t_start) < QDELAY) __builtin_amdgcn_s_sleep(2);
      const int qo = WS_QG + (kvh * 2 + bb) * 4096 + lane * 8;
      v2u a[8];
#pragma unroll
      for (int k = 0; k < 8; ++k) a[k] = v2u{~tlo, ~thi};
      for (;;) {
        asm volatile("" ::: "memory");
        bool all = true;
        v2u nv[8];
#pragma unroll
        for (int k = 0; k < 8; ++k) {
          const bool ok = a[k][1] == thi && ((a[k][0] ^ tlo) >> 16) == 0;
          nv[k] = __builtin_amdgcn_raw_buffer_load_b64(rs, ok ? OOB : qo + 512 * k, 0, AUX_SC1);
        }
#pragma unroll
        for (int k = 0; k < 8; ++k) {
          const bool ok = a[k][1] == thi && ((a[k][0] ^ tlo) >> 16) == 0;
          a[k] = ok ? a[k] : nv[k];
          all = all && a[k][1] == thi && ((a[k][0] ^ tlo) >> 16) == 0;
        }
        if (__all(all)) break;
        if (--budget <= 0) break;
        __builtin_amdgcn_s_sleep(4);
      }
#pragma unroll
      for (int k = 0; k < 8; ++k) *(LAS unsigned short*)(L + L_Q + (64 * k + lane) * 2) = (unsigned short)a[k][0];
      wait_lgkm();
      if (lane == 0) ctrl[C_Q] = 1u;
    }
    auto ffn_slice = [&]() {
      const int u0 = 32 * ffn;
      const unsigned vo1 = (lane >> 2) * (FF * 2) + ((p4 ^ ((lane >> 3) & 2)) * 16);
      LAS char* wa = L + L_W + (4 * g + q4) * 64 + 8 * p4;
      f32x4 h0 = {0.f, 0.f, 0.f, 0.f}, h1 = h0;
#pragma unroll
      for (int rd = 0; rd < 4; ++rd) {
#pragma unroll
        for (int j = 0; j < 8; ++j) dma1<false>(vo1, w1 + u0 * 2 + (size_t)(rd * 128 + j * 16) * (FF * 2), lbase + L_W + j * 1024);
        wait_vm<0>();
        v4u fa[4][2];
#pragma unroll
        for (int ks = 0; ks < 4; ++ks)
#pragma unroll
          for (int nb = 0; nb < 2; ++nb) {
            LAS char* p = wa + ks * 2048 + ((nb ^ (g & 1)) * 32);
            fa[ks][nb] = cat(lds_tr(p), lds_tr(p + 1024));
          }
        __builtin_amdgcn_sched_barrier(0);
        wait_lgkm();
#pragma unroll
        for (int ks = 0; ks < 4; ++ks) { h0 = mfma32(fa[ks][0], xb[4 * rd + ks], h0); h1 = mfma32(fa[ks][1], xb[4 * rd + ks], h1); }
      }
      float hv[8];
#pragma unroll
      for (int j = 0; j < 8; ++j) hv[j] = silu(rbf(j < 4 ? h0[j & 3] : h1[j & 3]));
      const v4u hb = {pk_bf16(hv[0], hv[1]), pk_bf16(hv[2], hv[3]), pk_bf16(hv[4], hv[5]), pk_bf16(hv[6], hv[7])};
      const unsigned vo2[2] = {(unsigned)(g * 1024 + ((col ^ (g << 1)) * 16)), (unsigned)(g * 1024 + ((col ^ ((4 + g) << 1)) * 16))};
      const int sw = (4 * (g & 1) + q4) << 1;
      LAS char* w2a = L + L_W + (4 * g + q4) * 256 + 8 * (p4 & 1);
      const int mine = 2 * (col >> 2) + (col & 1);
#pragma unroll
      for (int cb = 0; cb < 4; ++cb) {
#pragma unroll
        for (int j = 0; j < 8; ++j) dma1(vo2[j & 1], w2 + (size_t)(u0 + 4 * j) * 1024 + cb * 256, lbase + L_W + j * 1024);
        wait_vm<0>();
        v4u wf[8];
#pragma unroll
        for (int nb = 0; nb < 8; ++nb) {
          LAS char* p = w2a + (((2 * nb + (p4 >> 1)) ^ sw) * 16);
          wf[nb] = cat(lds_tr(p), lds_tr(p + 4096));
        }
        __builtin_amdgcn_sched_barrier(0);
        wait_lgkm();
        f32x4 fo = {0.f, 0.f, 0.f, 0.f};
#pragma unroll
        for (int nb = 0; nb < 8; ++nb) fo = mfma32(wf[nb], mine == nb ? hb : v4u{0u, 0u, 0u, 0u}, fo);
        __builtin_amdgcn_raw_buffer_store_b128(not_sent(__builtin_bit_cast(v4u, fo)), rs, WS_PF + ffn * 4096 + cb * 1024 + lane * 16, 0, AUX_SC1);
      }
    };
    if (has_ffn) {
      ffn_slice();
      wait_vm<0>();
      if (lane == 0) __hip_atomic_store((GAS u64*)(ws + WS_FOK) + ffn, tag, RLX_AGENT);
    }

    lds_wait_ge<8>(ctrl, C_ALL, 1u, budget);
    const int cb = xg >> 1, pc = (xg & 1) * 32 + tg, cw = pc >> 4, gw = (pc >> 2) & 3;
    const int jj = (g >> 1) | ((g & 1) << 1), oidx = ((pc >> 1) & 1) * 512 + cb * 128 + cw * 32 + (pc & 1) * 16 + gw * 4 + jj;
    const float xin = __uint_as_float((unsigned)*(const GAS unsigned short*)(x + oidx * 2) << 16);
    float gate = 0.f;
    const __amdgpu_buffer_rsrc_t ro = __builtin_amdgcn_make_buffer_rsrc((void*)out, 0, 2 * D * 2, 0x00020000);
    int out_off = col == 0 ? oidx * 2 : OOB;
    __builtin_amdgcn_raw_buffer_store_b16((unsigned short)0, ro, out_off, 0, 0);
    const unsigned touch = *(const GAS unsigned short*)(wo + (size_t)(kvh * 512 + g * 128 + 4 * gi) * 1024);
    asm volatile("" : "+v"(out_off) : "v"(touch));
#pragma nounroll
    for (int pass = 0; pass < 2; ++pass) {
      const bool last = opaque(pass) != 0;
      if (last) { lds_wait_ge<8>(ctrl, C_LANDED, g_kv - 4, budget); lds_wait_ge(ctrl, C_PUB, 1u, budget); __builtin_amdgcn_s_sleep(H2D); }
      else {
        lds_wait_ge<8>(ctrl, C_LANDED, PFAT, budget);
        while (!__all(__hip_atomic_load((GAS u64*)(ws + WS_FOK) + min(lane, 55), RLX_AGENT) == tag) && --budget > 0) __builtin_amdgcn_s_sleep(8);
      }
      const int po = (last ? WS_P2 + pc * 16 : WS_PF + gw * 256 + cw * 64 + (pc & 3) * 16) + (last || lane < 56 ? lane * 4096 + cb * 1024 : OOB - WS_END);
      const v4u sv = poll_box(rs, po, budget);
      float t = rows_sum4(__uint_as_float(sv[0]), __uint_as_float(sv[1]), __uint_as_float(sv[2]), __uint_as_float(sv[3]));
      t += dpp<XOR1>(t); t += dpp<XOR2>(t); t += dpp<ROR8>(t); t += dpp<ROR4>(t);
      t = rbf(t);
      if (last) {
        const float y = xin + rbf(t * gate);
        __builtin_amdgcn_raw_buffer_store_b16(budget <= 0 ? (unsigned short)0x7fc0 : (unsigned short)pk_bf16(y, y), ro, out_off, 0, 0);
        break;
      }
      gate = rbf(silu(t));
    }
    return;
  }

  if (role == 6) {
    unsigned at_t = 0;
    int at_p = 0;
    lds_wait_ge<8>(ctrl, C_Q, 1u, budget);
    const u64 tag = *(volatile LAS u64*)(ctrl + C_TAG);
    lds_wait_ge<8>(ctrl, C_CLR, 4u, budget);
#pragma nounroll
    for (int k = 0; k < NROUND; ++k) {
      GAS u64* line = (GAS u64*)(ws + WS_TAU) + (k ? k * 256 : kvh * 32);
      const int at = MARK + k * MSTEP, mark = (at * nt) >> 5;
      lds_wait_ge<1>(ctrl, C_LANDED, mark, budget);
      const unsigned tau = (unsigned)__builtin_amdgcn_s_memrealtime();
      if (lane == 0 && (k == 0 || ii == 3)) __hip_atomic_store(line + (k ? xg * 8 + kvh : gi), (tag << 16) | (tau & 0xffffu), RLX_AGENT);
      lds_wait_ge<4>(ctrl, C_LANDED, mark + MDEL, budget);
      const u64 v = __hip_atomic_load(line + (lane & (k ? 63 : 31)), RLX_AGENT);
      const unsigned seen_t = ((v ^ (tag << 16)) >> 16) == 0 ? (unsigned)v : (unsigned)__builtin_amdgcn_s_memrealtime();
      const float d = wave_sum((float)(short)(seen_t - tau));
      const unsigned mean = tau + __builtin_amdgcn_readfirstlane((int)(d * (1.f / 64)));
      constexpr int rate = 65536 / TG16;
      const int common = k ? at_p + (((int)(mean - at_t) * rate) >> 8) - at * 16 : LAG0 * 16;
      at_t = mean;
      at_p = at * 16 + min(max(common, 0), LAGHI * 16);
      if (lane == 0) *(volatile LAS v4u*)(ctrl + C_SCHED) = v4u{at_t, (unsigned)at_p, (unsigned)rate, 0u};
      if (k == 0) {
        while (!__all(((__hip_atomic_load(line + (lane & 31), RLX_AGENT) ^ (tag << 16)) >> 16) == 0) && --budget > 0) __builtin_amdgcn_s_sleep(8);
        if (lane == 0) ctrl[C_FLAG] = 1u;
        if (gi == 0 && lane == 0) __hip_atomic_store((GAS u64*)(ws + WS_GOK) + kvh, tag, RLX_AGENT);
      } else if (k == 1) {
        while (!__all(__hip_atomic_load((GAS u64*)(ws + WS_GOK) + (lane & 7), RLX_AGENT) == tag) && --budget > 0) __builtin_amdgcn_s_sleep(8);
        if (lane == 0) ctrl[C_ALL] = 1u;
        if (bid == 0 && lane == 0) __hip_atomic_store((GAS u64*)(ws + WS_EPOCH), tag, RLX_AGENT);
      }
    }
    return;
  }

  const int c = role - 2;
  const int blk = col >> 2;
  constexpr float SCALE = 0.08838834764831845f, LOG2E = 1.4426950408889634f;
  unsigned seen = 0u;
  auto need = [&](unsigned want) {
    if (seen < want) seen = lds_wait_ge(ctrl, C_LANDED, want, budget);
    asm volatile("" ::: "memory");
  };
  float ms;
  {
    unsigned ka[4];
#pragma unroll
    for (int k = 0; k < 4; ++k) ka[k] = col * 256 + (((4 * k + g) ^ col) * 16);
    v4u kf[4][4];
    auto fetch_k = [&](int t) {
      need(4 * t + 4);
      LAS char* sb = L + (t & (NSLOT - 1)) * SLOT;
#pragma unroll
      for (int k = 0; k < 4; ++k)
#pragma unroll
        for (int a = 0; a < 4; ++a) kf[a][k] = *(LAS v4u*)(sb + ka[k] + a * 4096);
      __builtin_amdgcn_sched_barrier(0);
      wait_lgkm();
      if (lane == 0) ctrl[C_FREED + c] = min(t + 4, nt);
      __builtin_amdgcn_sched_barrier(0);
    };
    fetch_k(c);
    const v4u sv = {SENT, SENT, SENT, SENT};
    if (c < 3) __builtin_amdgcn_raw_buffer_store_b128(sv, rs, (c < 2 ? WS_P1 + wid * 2048 + c * 1024 : WS_P2 + wid * 1024) + lane * 16, 0, AUX_SC1);
    wait_vm<0>();
    if (lane == 0) __hip_atomic_fetch_add((LAS unsigned*)(ctrl + C_CLR), 1u, __ATOMIC_RELAXED, __HIP_MEMORY_SCOPE_WORKGROUP);
    lds_wait_ge<2>(ctrl, C_Q, 1u, budget);
    v4u qf[4][4];
#pragma unroll
    for (int k = 0; k < 4; ++k) {
      const v4u qv = *(LAS v4u*)(L + L_Q + ((col & 3) * 128 + 32 * k + 8 * g) * 2);
#pragma unroll
      for (int a = 0; a < 4; ++a) qf[a][k] = blk == a ? qv : v4u{0u, 0u, 0u, 0u};
    }
    float mx = -3.0e38f;
#pragma nounroll
    for (int t = c;; t += 4) {
      f32x4 s0 = {0.f, 0.f, 0.f, 0.f}, s1 = {0.f, 0.f, 0.f, 0.f};
#pragma unroll
      for (int k = 0; k < 4; ++k) {
        s0 = mfma32(kf[0][k], qf[0][k], s0); s1 = mfma32(kf[1][k], qf[1][k], s1);
        s0 = mfma32(kf[2][k], qf[2][k], s0); s1 = mfma32(kf[3][k], qf[3][k], s1);
      }
      s0 += s1;
      const unsigned a0 = pk_bf16(s0[0], s0[1]), a1 = pk_bf16(s0[2], s0[3]);
      const unsigned b0 = pk_bf16(bf_lo(a0) * SCALE, bf_hi(a0) * SCALE), b1 = pk_bf16(bf_lo(a1) * SCALE, bf_hi(a1) * SCALE);
      *(LAS v2u*)(L + L_S + t * 512 + lane * 8) = v2u{b0, b1};
      mx = max2(max2(mx, max2(bf_lo(b0), bf_hi(b0))), max2(bf_lo(b1), bf_hi(b1)));
      if (t + 4 >= nt) break;
      fetch_k(t + 4);
    }
    mx = max2(mx, dpp<ROR8>(mx)); mx = max2(mx, dpp<ROR4>(mx)); mx = xrow_max(mx);
    if (lane < 4) *(LAS float*)(L + L_M + lane * 16 + c * 4) = mx;
    wait_lgkm();
    if (lane == 0) __hip_atomic_fetch_add((LAS unsigned*)(ctrl + C_MAX), 1u, __ATOMIC_RELAXED, __HIP_MEMORY_SCOPE_WORKGROUP);
    lds_wait_ge(ctrl, C_MAX, 4u, budget);
    const f32x4 m4 = *(LAS f32x4*)(L + L_M + (col & 3) * 16);
    ms = max2(max2(m4[0], m4[1]), max2(m4[2], m4[3])) * LOG2E;
  }
  unsigned va[2];
#pragma unroll
  for (int n = 0; n < 2; ++n)
    va[n] = (4 * g + q4) * 256 + (((4 * c + 2 * n + (p4 >> 1)) ^ ((4 * (g & 1) + q4) << 1)) * 16) + 8 * (p4 & 1);
  int p1_off = col < 8 ? WS_P1 + wid * 2048 + c * 512 + g * 128 + col * 16 : OOB;
  int p2_off = col < 4 ? WS_P2 + wid * 1024 + c * 256 + g * 64 + col * 16 : OOB;
  int rec0 = WS_P1 + (kvh * 32 + (c >> 1) * 16 + (lane >> 3)) * 2048 + (gi >> 2) * 256 + (c & 1) * 128 + (lane & 7) * 16, rec1 = rec0 + 8 * 2048;
  unsigned o_wr = (lane & 40) == 0 ? L_O + c * 64 + col * 8 + g * 4 : L_NIL + lane * 4, o_rd = lbase + L_O + ((col >> 1) & 1) * 128 + g * 8;
  unsigned wo0 = ((2 * nt) & (NSLOT - 1)) * SLOT + va[0], wo1 = ((2 * nt) & (NSLOT - 1)) * SLOT + va[1];
  asm volatile("" : "+v"(p1_off), "+v"(p2_off), "+v"(rec0), "+v"(rec1), "+v"(o_wr), "+v"(o_rd), "+v"(wo0), "+v"(wo1));
  float lsum = 0.f, linv = 0.f;
  unsigned lam = 0;
  f32x4 acc0 = {0.f, 0.f, 0.f, 0.f}, acc1 = {0.f, 0.f, 0.f, 0.f};
#pragma nounroll
  for (int t = 0; t < nt; ++t) {
    const int seq = nt + t;
    const v2u sc = *(LAS v2u*)(L + L_S + t * 512 + lane * 8);
    const float e0 = __builtin_amdgcn_exp2f(fmaf(bf_lo(sc[0]), LOG2E, -ms)), e1 = __builtin_amdgcn_exp2f(fmaf(bf_hi(sc[0]), LOG2E, -ms));
    const float e2 = __builtin_amdgcn_exp2f(fmaf(bf_lo(sc[1]), LOG2E, -ms)), e3 = __builtin_amdgcn_exp2f(fmaf(bf_hi(sc[1]), LOG2E, -ms));
    lsum += (e0 + e1) + (e2 + e3);
    const unsigned p0 = pk_bf16(e0, e1), p1 = pk_bf16(e2, e3);
    v4u pA = {blk == 0 ? p0 : 0u, blk == 0 ? p1 : 0u, blk == 1 ? p0 : 0u, blk == 1 ? p1 : 0u};
    v4u pB = {blk == 2 ? p0 : 0u, blk == 2 ? p1 : 0u, blk == 3 ? p0 : 0u, blk == 3 ? p1 : 0u};
    if (t == nt - 1) {
      lds_wait_ge(ctrl, C_FLAG, 1u, budget);
      const float lt = quad_stride_sum(lsum);
      linv = __builtin_amdgcn_rcpf(lt);
      lam = __float_as_uint(ms + __builtin_amdgcn_logf(lt));
    }
    asm volatile("" : "+v"(pA), "+v"(pB), "+v"(lsum), "+v"(linv), "+v"(lam));
    LAS char* sb = L + (seq & (NSLOT - 1)) * SLOT;
    need(4 * seq + 2);
    acc0 = mfma32(cat(lds_tr(sb + va[0]), lds_tr(sb + va[0] + 4096)), pA, acc0);
    acc1 = mfma32(cat(lds_tr(sb + va[1]), lds_tr(sb + va[1] + 4096)), pA, acc1);
    need(4 * seq + 4);
    v4u v0 = cat(lds_tr(sb + va[0] + 8192), lds_tr(sb + va[0] + 12288)), v1 = cat(lds_tr(sb + va[1] + 8192), lds_tr(sb + va[1] + 12288));
    wait_lgkm();
    if (lane == 0) ctrl[C_FREED + c] = seq + 1;
    asm volatile("" : "+v"(v0), "+v"(v1) :: "memory");
    acc0 = mfma32(v0, pB, acc0);
    acc1 = mfma32(v1, pB, acc1);
  }

  v4u pk;
#pragma unroll
  for (int j = 0; j < 4; ++j) {
    float a = ((col & 4) ? acc1[j] : acc0[j]) + dpp<ROR4>((col & 4) ? acc0[j] : acc1[j]);
    a += dpp<ROR8>(a);
    pk[j] = ((__float_as_uint(a * linv) + 0x80u) & 0xffffff00u) | ((lam >> (8 * j)) & 0xffu);
  }
  __builtin_amdgcn_raw_buffer_store_b128(not_sent(pk), rs, p1_off, 0, AUX_SC1);
  v4u a0, a1, wa[4];
  need(g_wo);
#pragma unroll
  for (int n = 0; n < 4; ++n) wa[n] = cat(lds_tr(L + ((n & 1) ? wo1 : wo0) + (n >> 1) * 4096), lds_tr(L + ((n & 1) ? wo1 : wo0) + (n >> 1) * 4096 + 8192));
  __builtin_amdgcn_sched_barrier(0);
  poll_box2(rs, rec0, rec1, a0, a1, budget);
  const float lA = __uint_as_float(__builtin_amdgcn_perm(__builtin_amdgcn_perm(a0[3], a0[2], 0x04000c0cu), __builtin_amdgcn_perm(a0[1], a0[0], 0x0c0c0400u), 0x07060100u));
  const float lB = __uint_as_float(__builtin_amdgcn_perm(__builtin_amdgcn_perm(a1[3], a1[2], 0x04000c0cu), __builtin_amdgcn_perm(a1[1], a1[0], 0x0c0c0400u), 0x07060100u));
  float M = max2(lA, lB);
  M = xrow_max(max2(M, dpp<ROR8>(M)));
  const float wA = __builtin_amdgcn_exp2f(lA - M), wB = __builtin_amdgcn_exp2f(lB - M);
  float r[4];
#pragma unroll
  for (int j = 0; j < 4; ++j) r[j] = fmaf(wA, __uint_as_float(a0[j] & 0xffffff00u), wB * __uint_as_float(a1[j] & 0xffffff00u));
  float wsum = xrow_sum(wA + wB), ov = rows_sum4(r[0], r[1], r[2], r[3]);
  wsum += dpp<ROR8>(wsum); ov += dpp<ROR8>(ov);
  ov *= __builtin_amdgcn_rcpf(wsum);
  const auto pr = __builtin_amdgcn_permlane32_swap(__float_as_uint(ov), __float_as_uint(ov), false, false);
  *(LAS unsigned*)(L + o_wr) = min(pk_bf16(__uint_as_float(pr[0]), __uint_as_float(pr[1])), SENT - 1);
  v4u o0, o1;
  poll_lds_o(o_rd, o0, o1, budget);
  const f32x4 zero = {0.f, 0.f, 0.f, 0.f};
  const f32x4 sp0 = mfma32(wa[2], o1, mfma32(wa[0], o0, zero)), sp1 = mfma32(wa[3], o1, mfma32(wa[1], o0, zero));
  const f32x4 sp = (col & 1) ? sp1 : sp0;
  __builtin_amdgcn_raw_buffer_store_b128(not_sent(__builtin_bit_cast(v4u, sp)), rs, p2_off, 0, AUX_SC1);
  ctrl[C_PUB] = 1u;
}

extern "C" void kernel_launch(void* const* d_in, const int* in_sizes, int n_in, void* d_out, int out_size, void* d_ws, size_t ws_size,
                              hipStream_t stream) {
  (void)hipFuncSetAttribute((const void*)fused_decode, hipFuncAttributeMaxDynamicSharedMemorySize, L_BYTES);
  hipLaunchKernelGGL(fused_decode, dim3(NWG), dim3(448), L_BYTES, stream, (const char*)d_in[0], (const char*)d_in[1], (const char*)d_in[2],
                     (const char*)d_in[3], (const char*)d_in[4], (const char*)d_in[6], (const char*)d_in[7], (char*)d_out, (char*)d_ws);
}
```

```cpp
#include <hip/hip_runtime.h>
#include <hip/hip_bf16.h>
#include <stdint.h>

#define GAS __attribute__((address_space(1)))
#define LAS __attribute__((address_space(3)))
#define RLX_AGENT __ATOMIC_RELAXED, __HIP_MEMORY_SCOPE_AGENT
typedef unsigned long long u64;
typedef unsigned v4u __attribute__((ext_vector_type(4)));
typedef unsigned v2u __attribute__((ext_vector_type(2)));
typedef float f32x4 __attribute__((ext_vector_type(4)));
typedef float f32x2 __attribute__((ext_vector_type(2)));
typedef __bf16 bf16x8 __attribute__((ext_vector_type(8)));
typedef __bf16 bf16x4 __attribute__((ext_vector_type(4)));
typedef __bf16 bf16x2 __attribute__((ext_vector_type(2)));

constexpr int W0 = 16, WMIN = 12, WMAX = 32;
constexpr int KN = 3, KD = 2;
constexpr int FF0 = 32, FF1 = -16;
constexpr int TG16 = 216;
constexpr int NROUND = 4, MARK = 32, MSTEP = 58, MDEL = 14;
constexpr int LAG0 = 0, LAGHI = 2;
constexpr int QDELAY = 370, PFAT = 176;
constexpr int H2D = 12;

constexpr int D = 512, SK = 32768, FF = 1792, NWG = 256, NSLOT = 8, SLOT = 16384;
constexpr unsigned SENT = 0xFFFFFFFFu;
constexpr int AUX_SC1 = 16, OOB = 0x7ffffff0;

constexpr int WS_EPOCH = 0, WS_GOK = 4096, WS_FOK = 4608, WS_TAU = 8192, WS_QG = 32768, WS_P1 = WS_QG + 65536, WS_P2 = WS_P1 + NWG * 2048,
              WS_PF = WS_P2 + NWG * 1024, WS_END = WS_PF + 64 * 4096;

constexpr int L_S = NSLOT * SLOT, L_CTRL = L_S + 33 * 512, L_XN = L_CTRL + 512, L_Q = L_XN + 2048, L_O = L_Q + 1024, L_M = L_O + 256,
              L_NIL = L_M + 64, L_W = L_NIL + 256, L_BYTES = L_W + 8192;
constexpr int C_LANDED = 64, C_ALL = 0, C_WQ = 1, C_Q = 2, C_CLR = 3, C_FREED = 4, C_PUB = 8, C_FLAG = 9, C_TAG = 10, C_SCHED = 12, C_MAX = 16;
static_assert(NROUND >= 2 && WS_TAU + NROUND * 2048 <= WS_QG && MARK + (NROUND - 1) * MSTEP + MDEL <= 248 && L_BYTES <= 163840, "");
constexpr int SPIN_BUDGET = 1 << 20;

__device__ __forceinline__ unsigned pk_bf16(float lo, float hi) {
  return __builtin_bit_cast(unsigned, __builtin_convertvector(f32x2{lo, hi}, bf16x2));
}
__device__ __forceinline__ float bf_lo(unsigned u) { return __uint_as_float(u << 16); }
__device__ __forceinline__ float bf_hi(unsigned u) { return __uint_as_float(u & 0xffff0000u); }
__device__ __forceinline__ float rbf(float f) { return bf_lo(pk_bf16(f, f)); }
template <int CTRL> __device__ __forceinline__ float dpp(float x) {
  return __builtin_bit_cast(float, __builtin_amdgcn_mov_dpp(__builtin_bit_cast(int, x), CTRL, 0xf, 0xf, true));
}
constexpr int XOR1 = 0xB1, XOR2 = 0x4E, ROR4 = 0x124, ROR8 = 0x128;
__device__ __forceinline__ float xrow_sum(float x) {
  auto s = __builtin_amdgcn_permlane16_swap(__float_as_uint(x), __float_as_uint(x), false, false);
  x = __uint_as_float(s[0]) + __uint_as_float(s[1]);
  auto t = __builtin_amdgcn_permlane32_swap(__float_as_uint(x), __float_as_uint(x), false, false);
  return __uint_as_float(t[0]) + __uint_as_float(t[1]);
}
__device__ __forceinline__ float quad_stride_sum(float x) {
  x += dpp<ROR8>(x); x += dpp<ROR4>(x);
  return xrow_sum(x);
}
__device__ __forceinline__ float rows_sum4(float a, float b, float c, float d) {
  const auto s = __builtin_amdgcn_permlane32_swap(__float_as_uint(a), __float_as_uint(b), false, false);
  const auto t = __builtin_amdgcn_permlane32_swap(__float_as_uint(c), __float_as_uint(d), false, false);
  const float ab = __uint_as_float(s[0]) + __uint_as_float(s[1]), cd = __uint_as_float(t[0]) + __uint_as_float(t[1]);
  const auto u = __builtin_amdgcn_permlane16_swap(__float_as_uint(ab), __float_as_uint(cd), false, false);
  return __uint_as_float(u[0]) + __uint_as_float(u[1]);
}
__device__ __forceinline__ float max2(float a, float b) {
  float top = 3.4028235e38f;
  asm("" : "+s"(top));
  return __builtin_amdgcn_fmed3f(a, b, top);
}
__device__ __forceinline__ float xrow_max(float x) {
  const auto s = __builtin_amdgcn_permlane16_swap(__float_as_uint(x), __float_as_uint(x), false, false);
  x = max2(__uint_as_float(s[0]), __uint_as_float(s[1]));
  const auto t = __builtin_amdgcn_permlane32_swap(__float_as_uint(x), __float_as_uint(x), false, false);
  return max2(__uint_as_float(t[0]), __uint_as_float(t[1]));
}
__device__ __forceinline__ float wave_sum(float x) {
  x += dpp<XOR1>(x); x += dpp<XOR2>(x);
  return quad_stride_sum(x);
}
__device__ __forceinline__ float half_sum(float x) {
  x += dpp<XOR1>(x); x += dpp<XOR2>(x); x += dpp<ROR8>(x); x += dpp<ROR4>(x);
  const auto s = __builtin_amdgcn_permlane16_swap(__float_as_uint(x), __float_as_uint(x), false, false);
  return __uint_as_float(s[0]) + __uint_as_float(s[1]);
}
__device__ __forceinline__ f32x4 mfma32(v4u a, v4u b, f32x4 c) {
  return __builtin_amdgcn_mfma_f32_16x16x32_bf16(__builtin_bit_cast(bf16x8, a), __builtin_bit_cast(bf16x8, b), c, 0, 0, 0);
}
__device__ __forceinline__ v2u lds_tr(LAS char* p) {
  return __builtin_bit_cast(v2u, __builtin_amdgcn_ds_read_tr16_b64_v4bf16((LAS bf16x4*)p));
}
__device__ __forceinline__ v4u cat(v2u a, v2u b) { return v4u{a[0], a[1], b[0], b[1]}; }
__device__ __forceinline__ v4u not_sent(v4u u) {
  return v4u{min(u[0], SENT - 1), min(u[1], SENT - 1), min(u[2], SENT - 1), min(u[3], SENT - 1)};
}
__device__ __forceinline__ float silu(float v) { return v / (1.f + __expf(-v)); }

template <bool NT = true> __device__ __forceinline__ void dma1(unsigned voff, const char* sbase, unsigned ldsaddr) {
  if (NT) asm volatile("s_mov_b32 m0, %2\n\ts_nop 0\n\tglobal_load_lds_dwordx4 %0, %1 nt" :: "v"(voff), "s"(sbase), "s"(ldsaddr) : "memory");
  else asm volatile("s_mov_b32 m0, %2\n\ts_nop 0\n\tglobal_load_lds_dwordx4 %0, %1" :: "v"(voff), "s"(sbase), "s"(ldsaddr) : "memory");
}
__device__ __forceinline__ void dma4(unsigned v0, unsigned v1, unsigned v2, unsigned v3, const char* sbase, unsigned ldsaddr) {
  asm volatile("s_mov_b32 m0, %5\n\ts_nop 0\n\t"
               "global_load_lds_dwordx4 %0, %4 nt\n\tglobal_load_lds_dwordx4 %1, %4 offset:1024 nt\n\t"
               "global_load_lds_dwordx4 %2, %4 offset:2048 nt\n\tglobal_load_lds_dwordx4 %3, %4 offset:3072 nt"
               :: "v"(v0), "v"(v1), "v"(v2), "v"(v3), "s"(sbase), "s"(ldsaddr) : "memory");
}
template <int N> __device__ __forceinline__ void wait_vm() { asm volatile("s_waitcnt vmcnt(%0)" :: "i"(N) : "memory"); }
#define RUN_FETCH "ds_read_b128 v[100:103], %[ac] offset:48\n\tds_read_b128 v[104:107], %[ac] offset:16\n\ts_memrealtime s[86:87]\n\t"
#define RUN_GROUP(W, STEP, PRE) \
  "s_max_i32 %[pub], %[pub], %[t0]\n\tv_mov_b32 v99, %[pub]\n\ts_mov_b32 m0, %[t1]\n\ts_add_u32 %[t1], %[t1], 0x1000\n\t" \
  "s_add_i32 %[t0], %[t0], 1\n\t" PRE "s_waitcnt vmcnt(" #W ")\n\t" \
  "global_load_lds_dwordx4 %[v0], vcc nt\n\tds_write_b32 %[va], v99\n\tglobal_load_lds_dwordx4 %[v1], vcc offset:1024 nt\n\t" \
  "global_load_lds_dwordx4 %[v2], vcc offset:2048 nt\n\tglobal_load_lds_dwordx4 %[v3], vcc offset:3072 nt\n\t" \
  "s_add_u32 vcc_lo, vcc_lo, " STEP "\n\ts_addc_u32 vcc_hi, vcc_hi, 0\n\t"
#define RUN_LEVEL(W) \
  RUN_GROUP(W, "0x1000", "") RUN_GROUP(W, "%[mid]", "") RUN_GROUP(W, "0x1000", "") RUN_GROUP(W, "0x1000", RUN_FETCH) \
  "s_add_i32 %[seq], %[seq], 1\n\ts_cmp_lt_i32 %[seq], %[end]\n\ts_cbranch_scc1 5b\n\ts_branch 9f\n\t"
__device__ __forceinline__ void run_slots(unsigned v0, unsigned v1, unsigned v2, unsigned v3, const char* src, int mid, int& seq, int end,
                                          int& pub, int& dith, int w16, int ntn, unsigned lbase, unsigned actrl, unsigned va) {
  int t0, t1, t2;
  asm volatile("s_getpc_b64 s[84:85]\n4:\n\ts_add_u32 s84, s84, 7f-4b\n\ts_addc_u32 s85, s85, 0\n\t"
               "s_mov_b64 vcc, %[src]\n\t" RUN_FETCH "5:\n\ts_waitcnt lgkmcnt(0)\n\t"
               "v_min_u32 v104, v104, v105\n\tv_min3_u32 v104, v104, v106, v107\n\tv_readfirstlane_b32 %[t0], v100\n\t"
               "v_readfirstlane_b32 %[t1], v102\n\tv_readfirstlane_b32 %[t2], v101\n\t"
               "s_sub_i32 %[t0], s86, %[t0]\n\tv_readfirstlane_b32 s87, v104\n\ts_mul_i32 %[t0], %[t0], %[t1]\n\ts_ashr_i32 %[t0], %[t0], 8\n\t"
               "s_add_i32 %[t0], %[t0], %[t2]\n\ts_mul_i32 %[t0], %[t0], %[ntn]\n\ts_ashr_i32 %[t0], %[t0], 5\n\ts_lshl_b32 %[t2], %[pub], 4\n\t"
               "s_sub_i32 %[t0], %[t0], %[t2]\n\ts_mul_i32 %[t0], %[t0], %[kn]\n\ts_ashr_i32 %[t0], %[t0], %[kd]\n\t"
               "s_cmp_eq_u32 %[t1], 0\n\ts_cselect_b32 %[t0], 0, %[t0]\n\ts_add_i32 %[dith], %[dith], %[w16]\n\ts_add_i32 %[dith], %[dith], %[t0]\n\t"
               "s_add_i32 %[t2], %[dith], 32\n\ts_ashr_i32 %[t2], %[t2], 6\n\ts_max_i32 %[t2], %[t2], %[lo]\n\ts_min_i32 %[t2], %[t2], %[hi]\n\t"
               "s_lshl_b32 %[t0], %[t2], 6\n\ts_sub_i32 %[dith], %[dith], %[t0]\n\ts_max_i32 %[dith], %[dith], -32\n\ts_min_i32 %[dith], %[dith], 32\n\t"
               "s_add_i32 %[t0], %[seq], -8\n\ts_cmp_le_i32 s87, %[t0]\n\ts_cbranch_scc1 9f\n\t"
               "s_add_i32 s86, %[t2], -3\n\ts_mul_i32 s86, s86, 8f-7f\n\ts_lshl_b32 %[t0], %[seq], 2\n\ts_sub_i32 %[t0], %[t0], %[t2]\n\t"
               "s_and_b32 %[t1], %[seq], 7\n\ts_lshl_b32 %[t1], %[t1], 14\n\ts_add_u32 %[t1], %[t1], %[lb]\n\t"
               "s_add_u32 s86, s84, s86\n\ts_addc_u32 s87, s85, 0\n\ts_setpc_b64 s[86:87]\n7:\n\t"
               RUN_LEVEL(12) "8:\n\t" RUN_LEVEL(16) RUN_LEVEL(20) RUN_LEVEL(24) RUN_LEVEL(28) RUN_LEVEL(32) "9:\n\ts_waitcnt lgkmcnt(0)"
               : [seq] "+&s"(seq), [pub] "+&s"(pub), [dith] "+&s"(dith), [t0] "=&s"(t0), [t1] "=&s"(t1), [t2] "=&s"(t2)
               : [v0] "v"(v0), [v1] "v"(v1), [v2] "v"(v2), [v3] "v"(v3), [src] "s"(src), [mid] "s"(mid), [end] "s"(end),
                 [w16] "s"(w16), [ntn] "s"(ntn), [lb] "s"(lbase), [ac] "v"(actrl), [va] "v"(va), [kn] "i"(KN), [kd] "i"(KD / 2),
                 [lo] "i"(WMIN / 4), [hi] "i"(WMAX / 4)
               : "v99", "v100", "v101", "v102", "v103", "v104", "v105", "v106", "v107", "s84", "s85", "s86", "s87", "vcc", "scc", "memory");
}
static_assert(NSLOT == 8 && SLOT == 16384 && C_FREED == 4 && C_SCHED == 12 && (KD == 1 || KD == 2) && WMIN >= 12 && WMAX <= 32, "");
__device__ __forceinline__ int opaque(int v) { asm volatile("" : "+s"(v)); return v; }
__device__ __forceinline__ void wait_lgkm() { asm volatile("s_waitcnt lgkmcnt(0)" ::: "memory"); }

typedef volatile LAS unsigned* ctrl_t;
template <int SLEEP = 0> __device__ __forceinline__ unsigned lds_wait_ge(ctrl_t ctrl, int word, unsigned want, int& budget) {
  unsigned v, tmp;
  int go;
  budget = __builtin_amdgcn_readfirstlane(budget);
  want = __builtin_amdgcn_readfirstlane(want);
  asm volatile("1:\n\tds_read_b32 %[t], %[a]\n\t.if %[slp]\n\ts_sleep %[slp]\n\t.endif\n\ts_waitcnt lgkmcnt(0)\n\tv_readfirstlane_b32 %[v], %[t]\n\t"
               "s_cmp_lt_u32 %[v], %[w]\n\ts_cselect_b32 %[g], %[b], 0\n\ts_subb_u32 %[b], %[b], 0\n\ts_cmp_gt_i32 %[g], 0\n\ts_cbranch_scc1 1b"
               : [v] "=&s"(v), [t] "=&v"(tmp), [g] "=&s"(go), [b] "+&s"(budget)
               : [a] "v"((unsigned)(uintptr_t)(ctrl + word)), [w] "s"(want), [slp] "i"(SLEEP) : "scc", "memory");
  return v;
}
#define BOX_LOOP_END \
  "v_cmpx_eq_u32 vcc, -1, %[t]\n\ts_sub_i32 %[b], %[b], 1\n\ts_cmp_gt_i32 %[b], 0\n\ts_cselect_b64 vcc, exec, 0\n\t" \
  "s_cmp_lg_u64 vcc, 0\n\ts_cbranch_scc1 1b\n\ts_mov_b64 exec, -1"
__device__ __forceinline__ v4u poll_box(__amdgpu_buffer_rsrc_t rs, int off, int& budget) {
  v4u a;
  unsigned tmp;
  budget = __builtin_amdgcn_readfirstlane(budget);
  asm volatile("1:\n\tbuffer_load_dwordx4 v[100:103], %[o], %[rs], 0 offen sc1\n\ts_waitcnt vmcnt(0)\n\t"
               "v_max_u32 %[t], v100, v101\n\tv_max3_u32 %[t], %[t], v102, v103\n\t" BOX_LOOP_END
               : "=&{v[100:103]}"(a), [t] "=&v"(tmp), [b] "+&s"(budget) : [o] "v"(off), [rs] "s"(rs) : "vcc", "scc", "memory");
  return a;
}
__device__ __forceinline__ void poll_box2(__amdgpu_buffer_rsrc_t rs, int off0, int off1, v4u& a, v4u& b, int& budget) {
  unsigned tmp;
  budget = __builtin_amdgcn_readfirstlane(budget);
  asm volatile("1:\n\tbuffer_load_dwordx4 v[100:103], %[o0], %[rs], 0 offen sc1\n\tbuffer_load_dwordx4 v[104:107], %[o1], %[rs], 0 offen sc1\n\t"
               "s_waitcnt vmcnt(0)\n\tv_max_u32 %[t], v100, v101\n\tv_max3_u32 %[t], %[t], v102, v103\n\t"
               "v_max3_u32 %[t], %[t], v104, v105\n\tv_max3_u32 %[t], %[t], v106, v107\n\t" BOX_LOOP_END
               : "=&{v[100:103]}"(a), "=&{v[104:107]}"(b), [t] "=&v"(tmp), [b] "+&s"(budget)
               : [o0] "v"(off0), [o1] "v"(off1), [rs] "s"(rs) : "vcc", "scc", "memory");
}
__device__ __forceinline__ void poll_lds_o(unsigned addr, v4u& a, v4u& b, int& budget) {
  unsigned tmp;
  budget = __builtin_amdgcn_readfirstlane(budget);
  asm volatile("1:\n\tds_read_b64 v[100:101], %[o]\n\tds_read_b64 v[102:103], %[o] offset:32\n\tds_read_b64 v[104:105], %[o] offset:64\n\t"
               "ds_read_b64 v[106:107], %[o] offset:96\n\ts_waitcnt lgkmcnt(0)\n\tv_max_u32 %[t], v100, v101\n\tv_max3_u32 %[t], %[t], v102, v103\n\t"
               "v_max3_u32 %[t], %[t], v104, v105\n\tv_max3_u32 %[t], %[t], v106, v107\n\t" BOX_LOOP_END
               : "=&{v[100:103]}"(a), "=&{v[104:107]}"(b), [t] "=&v"(tmp), [b] "+&s"(budget) : [o] "v"(addr) : "vcc", "scc", "memory");
}

typedef unsigned v16u __attribute__((ext_vector_type(16)));
__device__ __forceinline__ char* as_ptr(unsigned lo, unsigned hi) { return (char*)(((u64)hi << 32) | lo); }

__global__ void __launch_bounds__(448, 2) fused_decode(const char*, const char*, const char*, const char*, const char*, const char*, const char*,
                                                    char*, char*) {
  v16u ka16;
  v2u ka2;
  asm volatile("s_load_dwordx16 s[36:51], %2, 0x0\n\ts_load_dwordx2 s[52:53], %2, 0x40"
               : "=&{s[36:51]}"(ka16), "=&{s[52:53]}"(ka2) : "s"(__builtin_amdgcn_kernarg_segment_ptr()));
  __builtin_amdgcn_sched_barrier(0);
  extern __shared__ __attribute__((aligned(16))) char smem[];
  LAS char* L = (LAS char*)smem;
  const unsigned lbase = (unsigned)(uintptr_t)L;
  ctrl_t ctrl = (ctrl_t)(L + L_CTRL);
  const int lane = threadIdx.x & 63, bid = blockIdx.x;
  int wave = __builtin_amdgcn_readfirstlane(threadIdx.x >> 6);
  const int xg = bid & 7, tg = bid >> 3, kvh = tg >> 2, ii = tg & 3, gi = 4 * xg + ii, wid = kvh * 32 + gi, bb = gi >> 4;
  const bool has_ffn = ii == 1 && kvh < 7, has_33 = ii == 0 && kvh < 7;
  const int nt = has_33 ? 33 : has_ffn ? 31 : 32, tile0 = (xg & 3) * 128 + (has_ffn ? 33 : 32 * ii);
  const int g_kv = 8 * nt, g_wo = g_kv + 4;
  const int g = lane >> 4, col = lane & 15, q4 = (lane >> 2) & 3, p4 = lane & 3;
  const int ffn = xg * 7 + kvh;
  unsigned vk[4], vx = lane * 16, vq = (lane >> 1) * 8192 + (lane & 1) * 16;
#pragma unroll
  for (int j = 0; j < 4; ++j) vk[j] = g * 256 + ((col ^ (4 * j + g)) * 16);
  unsigned kvoff = ((bb * 8 + kvh) * SK + tile0 * 64) * 256, qoff = (kvh * 512 + gi * 16) * 2;
  asm volatile("" : "+v"(vk[0]), "+v"(vk[1]), "+v"(vk[2]), "+v"(vk[3]), "+v"(vx), "+v"(vq));
  asm volatile("" : "+s"(kvoff), "+s"(qoff), "+s"(wave));
  ctrl[lane] = 0u;
  ctrl[C_LANDED + lane] = 0u;
  *(volatile LAS unsigned*)(L + L_O + lane * 4) = SENT;
  asm volatile("s_waitcnt lgkmcnt(0)\n\ts_barrier" : "+{s[36:51]}"(ka16), "+{s[52:53]}"(ka2) :: "memory");
  const char *x = as_ptr(ka16[0], ka16[1]), *kc = as_ptr(ka16[2], ka16[3]), *vc = as_ptr(ka16[4], ka16[5]), *wq = as_ptr(ka16[6], ka16[7]),
             *wo = as_ptr(ka16[8], ka16[9]), *w1 = as_ptr(ka16[10], ka16[11]), *w2 = as_ptr(ka16[12], ka16[13]);
  char *out = as_ptr(ka16[14], ka16[15]), *ws = as_ptr(ka2[0], ka2[1]);
  const __amdgpu_buffer_rsrc_t rs = __builtin_amdgcn_make_buffer_rsrc((void*)ws, 0, WS_END, 0x00020000);
  const unsigned t_start = (unsigned)__builtin_amdgcn_s_memrealtime();
  if (wave == 1) {
    __builtin_amdgcn_s_setprio(3);
    const u64 pk_ = (u64)(kc + kvoff), pv_ = (u64)(vc + kvoff), pq_ = (u64)(wq + qoff), pq2 = pq_ + 511 * 8192, pw_ = (u64)(ws + 2048);
    asm volatile("v_writelane_b32 v100, %0, 0\n\tv_writelane_b32 v101, %1, 0\n\tv_writelane_b32 v100, %2, 1\n\tv_writelane_b32 v101, %3, 1\n\t"
                 "v_writelane_b32 v100, %4, 2\n\tv_writelane_b32 v101, %5, 2\n\tv_writelane_b32 v100, %6, 3\n\tv_writelane_b32 v101, %7, 3\n\t"
                 "v_writelane_b32 v100, %8, 4\n\tv_writelane_b32 v101, %9, 4\n\tv_writelane_b32 v100, %10, 5\n\tv_writelane_b32 v101, %11, 5\n\t"
                 "s_mov_b32 m0, %12\n\ts_mov_b64 exec, 63\n\tglobal_load_lds_dword v[100:101], off nt\n\ts_mov_b64 exec, -1"
                 :: "s"((unsigned)pk_), "s"((unsigned)(pk_ >> 32)), "s"((unsigned)pv_), "s"((unsigned)(pv_ >> 32)), "s"(ka16[0]), "s"(ka16[1]),
                    "s"((unsigned)pw_), "s"((unsigned)(pw_ >> 32)), "s"((unsigned)pq_), "s"((unsigned)(pq_ >> 32)), "s"((unsigned)pq2), "s"((unsigned)(pq2 >> 32)),
                    "s"(lbase + L_NIL) : "v100", "v101", "memory");
#pragma unroll
    for (int j = 0; j < 4; ++j) dma4(vk[0], vk[1], vk[2], vk[3], kc + kvoff + j * 4096, lbase + j * 4096);
  }
  if (opaque(wave) == 0) {
    asm volatile("s_mov_b32 m0, %4\n\ts_nop 0\n\tglobal_load_lds_dwordx4 %0, %1\n\tglobal_load_lds_dwordx4 %0, %1 offset:1024\n\t"
                 "s_mov_b32 m0, %5\n\ts_nop 0\n\tglobal_load_lds_dwordx4 %2, %3 sc1"
                 :: "v"(vx), "s"(x), "v"(WS_EPOCH), "s"(ws), "s"(lbase + L_W), "s"(lbase + L_W + 2048) : "memory");
  }
  if (opaque(wave) == 6) {
#pragma nounroll
    for (int j = 0; j < 16; ++j) dma1(vq, wq + qoff + (size_t)j * (32 * 8192), lbase + L_S + j * 1024);
    wait_vm<0>();
    if (lane == 0) ctrl[C_WQ] = 1u;
  }
  const int role = opaque(wave);
  int budget = SPIN_BUDGET;

  if (__builtin_expect(role == 1, 1)) {
    unsigned vv[2];
#pragma unroll
    for (int j = 0; j < 2; ++j) vv[j] = g * 256 + ((col ^ ((4 * j + g) << 1)) * 16);
    const char* kb = kc + kvoff;
    const char* vb = vc + kvoff;
    const char* wb = wo + (size_t)(kvh * 512 + 32 * (gi >> 3) + 8 * ((gi >> 2) & 1)) * 1024 + ii * 256;
    const unsigned va_pub = lbase + L_CTRL + (C_LANDED + lane) * 4;
    const int w16 = W0 * 16 + (has_33 ? FF0 : has_ffn ? FF1 : 0);
    int seq = 1, pub = 0, dith = 0;
#define DRAIN_TO(N) { wait_vm<4 * (N)>(); pub = max(pub, 4 * seq - (N)); ctrl[C_LANDED + lane] = pub; }
#pragma nounroll
    for (int ph = 0; ph < 3; ++ph) {
      unsigned v[4];
#pragma unroll
      for (int j = 0; j < 4; ++j) v[j] = ph == 0 ? vk[j] : vv[j & 1] + (ph == 2 ? g * 768 + j * (127 * 1024) : 0);
      const char* base = ph == 0 ? kb : ph == 1 ? vb : wb;
      const int first = ph * nt, end = ph == 2 ? first + 1 : first + nt;
      if (ph == 2) {
        DRAIN_TO(8) DRAIN_TO(7) DRAIN_TO(6) DRAIN_TO(5) DRAIN_TO(4) DRAIN_TO(3) DRAIN_TO(2) DRAIN_TO(1) DRAIN_TO(0)
      }
      for (;;) {
        run_slots(v[0], v[1], v[2], v[3], base + (seq - first) * SLOT, ph == 2 ? 12288 : 4096, seq, end, pub, dith, ph == 2 ? 1024 : w16, nt, lbase,
                  lbase + L_CTRL, va_pub);
        if (seq >= end) break;
        DRAIN_TO(0)
        int fr;
        do {
          __builtin_amdgcn_s_sleep(1);
          const v4u f = *(volatile LAS v4u*)(ctrl + C_FREED);
          fr = __builtin_amdgcn_readfirstlane(min(min(f[0], f[1]), min(f[2], f[3])));
        } while (fr <= seq - NSLOT && --budget > 0);
        if (budget <= 0) { ctrl[C_LANDED + lane] = g_wo; return; }
      }
    }
    DRAIN_TO(0)
#undef DRAIN_TO
    return;
  }

  if (role == 0) {
    u64 tag;
    v4u xb[16];
    {
      wait_vm<0>();
      tag = *(volatile LAS u64*)(L + L_W + 2048) + 1;
      const v4u xv0 = *(volatile LAS v4u*)(L + L_W + lane * 32), xv1 = *(volatile LAS v4u*)(L + L_W + lane * 32 + 16);
      float v[16];
#pragma unroll
      for (int e = 0; e < 4; ++e) { v[2 * e] = bf_lo(xv0[e]); v[2 * e + 1] = bf_hi(xv0[e]); v[8 + 2 * e] = bf_lo(xv1[e]); v[9 + 2 * e] = bf_hi(xv1[e]); }
      float s4[4], q4s[4];
#pragma unroll
      for (int e = 0; e < 4; ++e) s4[e] = (v[e] + v[4 + e]) + (v[8 + e] + v[12 + e]);
      const float mean = half_sum((s4[0] + s4[1]) + (s4[2] + s4[3])) * (1.f / D);
#pragma unroll
      for (int e = 0; e < 4; ++e) {
        const float d0 = v[e] - mean, d1 = v[4 + e] - mean, d2 = v[8 + e] - mean, d3 = v[12 + e] - mean;
        q4s[e] = fmaf(d0, d0, d1 * d1) + fmaf(d2, d2, d3 * d3);
      }
      const float var = half_sum((q4s[0] + q4s[1]) + (q4s[2] + q4s[3])) * (1.f / D);
      const float mb = rbf(mean), den = rbf(__builtin_amdgcn_sqrtf(rbf(rbf(var) + 1e-5f))), rden = __builtin_amdgcn_rcpf(den);
      unsigned o[8];
#pragma unroll
      for (int e = 0; e < 8; ++e) {
        const unsigned dp = pk_bf16(v[2 * e] - mb, v[2 * e + 1] - mb);
        const float d0 = bf_lo(dp), d1 = bf_hi(dp), q0 = d0 * rden, q1 = d1 * rden;
        o[e] = pk_bf16(fmaf(fmaf(-q0, den, d0), rden, q0), fmaf(fmaf(-q1, den, d1), rden, q1));
      }
      *(LAS v4u*)(L + L_XN + lane * 32) = v4u{o[0], o[1], o[2], o[3]};
      *(LAS v4u*)(L + L_XN + lane * 32 + 16) = v4u{o[4], o[5], o[6], o[7]};
      asm volatile("" ::: "memory");
      LAS char* xa = L + L_XN + (((col >> 1) & 1) * 512 + 4 * g) * 2;
#pragma unroll
      for (int ks = 0; ks < 16; ++ks) xb[ks] = cat(*(LAS v2u*)(xa + ks * 64), *(LAS v2u*)(xa + ks * 64 + 32));
      __builtin_amdgcn_sched_barrier(0);
      lds_wait_ge(ctrl, C_WQ, 1u, budget);
      f32x4 qp[2] = {{0.f, 0.f, 0.f, 0.f}, {0.f, 0.f, 0.f, 0.f}};
      LAS char* wa = L + L_S + 8 * lane;
      v4u fa[16];
#pragma unroll
      for (int ks = 0; ks < 16; ++ks) fa[ks] = cat(lds_tr(wa + ks * 1024), lds_tr(wa + ks * 1024 + 512));
      __builtin_amdgcn_sched_barrier(0);
#pragma unroll
      for (int ks = 0; ks < 16; ++ks) qp[ks & 1] = mfma32(fa[ks], xb[ks], qp[ks & 1]);
      const f32x4 qa = qp[0] + qp[1];
      if ((col & 13) == 0) *(LAS v2u*)(L + L_Q + (col >> 1) * 32 + g * 8) = v2u{pk_bf16(qa[0], qa[1]), pk_bf16(qa[2], qa[3])};
      asm volatile("" ::: "memory");
      const unsigned thi = (unsigned)(tag >> 16), tlo = (unsigned)tag << 16;
      const unsigned qv = *(LAS unsigned short*)(L + L_Q + (lane & 31) * 2);
      __builtin_amdgcn_raw_buffer_store_b64(v2u{tlo | qv, thi}, rs, lane < 32 ? WS_QG + ((kvh * 2 + g) * 512 + gi * 16 + col) * 8 : OOB, 0, AUX_SC1);
      if (lane == 0) *(volatile LAS u64*)(ctrl + C_TAG) = tag;
    }
    {
      const unsigned thi = (unsigned)(tag >> 16), tlo = (unsigned)tag << 16;
      while ((int)((unsigned)__builtin_amdgcn_s_memrealtime() - t_start) < QDELAY) __builtin_amdgcn_s_sleep(2);
      const int qo = WS_QG + (kvh * 2 + bb) * 4096 + lane * 8;
      v2u a[8];
#pragma unroll
      for (int k = 0; k < 8; ++k) a[k] = v2u{~tlo, ~thi};
      for (;;) {
        asm volatile("" ::: "memory");
        bool all = true;
        v2u nv[8];
#pragma unroll
        for (int k = 0; k < 8; ++k) {
          const bool ok = a[k][1] == thi && ((a[k][0] ^ tlo) >> 16) == 0;
          nv[k] = __builtin_amdgcn_raw_buffer_load_b64(rs, ok ? OOB : qo + 512 * k, 0, AUX_SC1);
        }
#pragma unroll
        for (int k = 0; k < 8; ++k) {
          const bool ok = a[k][1] == thi && ((a[k][0] ^ tlo) >> 16) == 0;
          a[k] = ok ? a[k] : nv[k];
          all = all && a[k][1] == thi && ((a[k][0] ^ tlo) >> 16) == 0;
        }
        if (__all(all)) break;
        if (--budget <= 0) break;
        __builtin_amdgcn_s_sleep(4);
      }
#pragma unroll
      for (int k = 0; k < 8; ++k) *(LAS unsigned short*)(L + L_Q + (64 * k + lane) * 2) = (unsigned short)a[k][0];
      wait_lgkm();
      if (lane == 0) ctrl[C_Q] = 1u;
    }
    auto ffn_slice = [&]() {
      const int u0 = 32 * ffn;
      const unsigned vo1 = (lane >> 2) * (FF * 2) + ((p4 ^ ((lane >> 3) & 2)) * 16);
      LAS char* wa = L + L_W + (4 * g + q4) * 64 + 8 * p4;
      f32x4 h0 = {0.f, 0.f, 0.f, 0.f}, h1 = h0;
#pragma unroll
      for (int rd = 0; rd < 4; ++rd) {
#pragma unroll
        for (int j = 0; j < 8; ++j) dma1<false>(vo1, w1 + u0 * 2 + (size_t)(rd * 128 + j * 16) * (FF * 2), lbase + L_W + j * 1024);
        wait_vm<0>();
        v4u fa[4][2];
#pragma unroll
        for (int ks = 0; ks < 4; ++ks)
#pragma unroll
          for (int nb = 0; nb < 2; ++nb) {
            LAS char* p = wa + ks * 2048 + ((nb ^ (g & 1)) * 32);
            fa[ks][nb] = cat(lds_tr(p), lds_tr(p + 1024));
          }
        __builtin_amdgcn_sched_barrier(0);
        wait_lgkm();
#pragma unroll
        for (int ks = 0; ks < 4; ++ks) { h0 = mfma32(fa[ks][0], xb[4 * rd + ks], h0); h1 = mfma32(fa[ks][1], xb[4 * rd + ks], h1); }
      }
      float hv[8];
#pragma unroll
      for (int j = 0; j < 8; ++j) hv[j] = silu(rbf(j < 4 ? h0[j & 3] : h1[j & 3]));
      const v4u hb = {pk_bf16(hv[0], hv[1]), pk_bf16(hv[2], hv[3]), pk_bf16(hv[4], hv[5]), pk_bf16(hv[6], hv[7])};
      const unsigned vo2[2] = {(unsigned)(g * 1024 + ((col ^ (g << 1)) * 16)), (unsigned)(g * 1024 + ((col ^ ((4 + g) << 1)) * 16))};
      const int sw = (4 * (g & 1) + q4) << 1;
      LAS char* w2a = L + L_W + (4 * g + q4) * 256 + 8 * (p4 & 1);
      const int mine = 2 * (col >> 2) + (col & 1);
#pragma unroll
      for (int cb = 0; cb < 4; ++cb) {
#pragma unroll
        for (int j = 0; j < 8; ++j) dma1(vo2[j & 1], w2 + (size_t)(u0 + 4 * j) * 1024 + cb * 256, lbase + L_W + j * 1024);
        wait_vm<0>();
        v4u wf[8];
#pragma unroll
        for (int nb = 0; nb < 8; ++nb) {
          LAS char* p = w2a + (((2 * nb + (p4 >> 1)) ^ sw) * 16);
          wf[nb] = cat(lds_tr(p), lds_tr(p + 4096));
        }
        __builtin_amdgcn_sched_barrier(0);
        wait_lgkm();
        f32x4 fo = {0.f, 0.f, 0.f, 0.f};
#pragma unroll
        for (int nb = 0; nb < 8; ++nb) fo = mfma32(wf[nb], mine == nb ? hb : v4u{0u, 0u, 0u, 0u}, fo);
        __builtin_amdgcn_raw_buffer_store_b128(not_sent(__builtin_bit_cast(v4u, fo)), rs, WS_PF + ffn * 4096 + cb * 1024 + lane * 16, 0, AUX_SC1);
      }
    };
    if (has_ffn) {
      ffn_slice();
      wait_vm<0>();
      if (lane == 0) __hip_atomic_store((GAS u64*)(ws + WS_FOK) + ffn, tag, RLX_AGENT);
    }

    lds_wait_ge<8>(ctrl, C_ALL, 1u, budget);
    const int cb = xg >> 1, pc = (xg & 1) * 32 + tg, cw = pc >> 4, gw = (pc >> 2) & 3;
    const int jj = (g >> 1) | ((g & 1) << 1), oidx = ((pc >> 1) & 1) * 512 + cb * 128 + cw * 32 + (pc & 1) * 16 + gw * 4 + jj;
    const float xin = __uint_as_float((unsigned)*(const GAS unsigned short*)(x + oidx * 2) << 16);
    float gate = 0.f;
    const __amdgpu_buffer_rsrc_t ro = __builtin_amdgcn_make_buffer_rsrc((void*)out, 0, 2 * D * 2, 0x00020000);
    int out_off = col == 0 ? oidx * 2 : OOB;
    __builtin_amdgcn_raw_buffer_store_b16((unsigned short)0, ro, out_off, 0, 0);
    const unsigned touch = *(const GAS unsigned short*)(wo + (size_t)(kvh * 512 + g * 128 + 4 * gi) * 1024);
    asm volatile("" : "+v"(out_off) : "v"(touch));
#pragma nounroll
    for (int pass = 0; pass < 2; ++pass) {
      const bool last = opaque(pass) != 0;
      if (last) { lds_wait_ge<8>(ctrl, C_LANDED, g_kv - 4, budget); lds_wait_ge(ctrl, C_PUB, 1u, budget); __builtin_amdgcn_s_sleep(H2D); }
      else {
        lds_wait_ge<8>(ctrl, C_LANDED, PFAT, budget);
        while (!__all(__hip_atomic_load((GAS u64*)(ws + WS_FOK) + min(lane, 55), RLX_AGENT) == tag) && --budget > 0) __builtin_amdgcn_s_sleep(8);
      }
      const int po = (last ? WS_P2 + pc * 16 : WS_PF + gw * 256 + cw * 64 + (pc & 3) * 16) + (last || lane < 56 ? lane * 4096 + cb * 1024 : OOB - WS_END);
      const v4u sv = poll_box(rs, po, budget);
      float t = rows_sum4(__uint_as_float(sv[0]), __uint_as_float(sv[1]), __uint_as_float(sv[2]), __uint_as_float(sv[3]));
      t += dpp<XOR1>(t); t += dpp<XOR2>(t); t += dpp<ROR8>(t); t += dpp<ROR4>(t);
      t = rbf(t);
      if (last) {
        const float y = xin + rbf(t * gate);
        __builtin_amdgcn_raw_buffer_store_b16(budget <= 0 ? (unsigned short)0x7fc0 : (unsigned short)pk_bf16(y, y), ro, out_off, 0, 0);
        break;
      }
      gate = rbf(silu(t));
    }
    return;
  }

  if (role == 6) {
    unsigned at_t = 0;
    int at_p = 0;
    lds_wait_ge<8>(ctrl, C_Q, 1u, budget);
    const u64 tag = *(volatile LAS u64*)(ctrl + C_TAG);
    lds_wait_ge<8>(ctrl, C_CLR, 4u, budget);
#pragma nounroll
    for (int k = 0; k < NROUND; ++k) {
      GAS u64* line = (GAS u64*)(ws + WS_TAU) + (k ? k * 256 : kvh * 32);
      const int at = MARK + k * MSTEP, mark = (at * nt) >> 5;
      lds_wait_ge<1>(ctrl, C_LANDED, mark, budget);
      const unsigned tau = (unsigned)__builtin_amdgcn_s_memrealtime();
      if (lane == 0 && (k == 0 || ii == 3)) __hip_atomic_store(line + (k ? xg * 8 + kvh : gi), (tag << 16) | (tau & 0xffffu), RLX_AGENT);
      lds_wait_ge<4>(ctrl, C_LANDED, mark + MDEL, budget);
      const u64 v = __hip_atomic_load(line + (lane & (k ? 63 : 31)), RLX_AGENT);
      const unsigned seen_t = ((v ^ (tag << 16)) >> 16) == 0 ? (unsigned)v : (unsigned)__builtin_amdgcn_s_memrealtime();
      const float d = wave_sum((float)(short)(seen_t - tau));
      const unsigned mean = tau + __builtin_amdgcn_readfirstlane((int)(d * (1.f / 64)));
      constexpr int rate = 65536 / TG16;
      const int common = k ? at_p + (((int)(mean - at_t) * rate) >> 8) - at * 16 : LAG0 * 16;
      at_t = mean;
      at_p = at * 16 + min(max(common, 0), LAGHI * 16);
      if (lane == 0) *(volatile LAS v4u*)(ctrl + C_SCHED) = v4u{at_t, (unsigned)at_p, (unsigned)rate, 0u};
      if (k == 0) {
        while (!__all(((__hip_atomic_load(line + (lane & 31), RLX_AGENT) ^ (tag << 16)) >> 16) == 0) && --budget > 0) __builtin_amdgcn_s_sleep(8);
        if (lane == 0) ctrl[C_FLAG] = 1u;
        if (gi == 0 && lane == 0) __hip_atomic_store((GAS u64*)(ws + WS_GOK) + kvh, tag, RLX_AGENT);
      } else if (k == 1) {
        while (!__all(__hip_atomic_load((GAS u64*)(ws + WS_GOK) + (lane & 7), RLX_AGENT) == tag) && --budget > 0) __builtin_amdgcn_s_sleep(8);
        if (lane == 0) ctrl[C_ALL] = 1u;
        if (bid == 0 && lane == 0) __hip_atomic_store((GAS u64*)(ws + WS_EPOCH), tag, RLX_AGENT);
      }
    }
    return;
  }

  const int c = role - 2;
  const int blk = col >> 2;
  constexpr float SCALE = 0.08838834764831845f, LOG2E = 1.4426950408889634f;
  unsigned seen = 0u;
  auto need = [&](unsigned want) {
    if (seen < want) seen = lds_wait_ge(ctrl, C_LANDED, want, budget);
    asm volatile("" ::: "memory");
  };
  float ms;
  {
    unsigned ka[4];
#pragma unroll
    for (int k = 0; k < 4; ++k) ka[k] = col * 256 + (((4 * k + g) ^ col) * 16);
    v4u kf[4][4];
    auto fetch_k = [&](int t) {
      need(4 * t + 4);
      LAS char* sb = L + (t & (NSLOT - 1)) * SLOT;
#pragma unroll
      for (int k = 0; k < 4; ++k)
#pragma unroll
        for (int a = 0; a < 4; ++a) kf[a][k] = *(LAS v4u*)(sb + ka[k] + a * 4096);
      __builtin_amdgcn_sched_barrier(0);
      wait_lgkm();
      if (lane == 0) ctrl[C_FREED + c] = min(t + 4, nt);
      __builtin_amdgcn_sched_barrier(0);
    };
    fetch_k(c);
    const v4u sv = {SENT, SENT, SENT, SENT};
    if (c < 3) __builtin_amdgcn_raw_buffer_store_b128(sv, rs, (c < 2 ? WS_P1 + wid * 2048 + c * 1024 : WS_P2 + wid * 1024) + lane * 16, 0, AUX_SC1);
    wait_vm<0>();
    if (lane == 0) __hip_atomic_fetch_add((LAS unsigned*)(ctrl + C_CLR), 1u, __ATOMIC_RELAXED, __HIP_MEMORY_SCOPE_WORKGROUP);
    lds_wait_ge<2>(ctrl, C_Q, 1u, budget);
    v4u qf[4][4];
#pragma unroll
    for (int k = 0; k < 4; ++k) {
      const v4u qv = *(LAS v4u*)(L + L_Q + ((col & 3) * 128 + 32 * k + 8 * g) * 2);
#pragma unroll
      for (int a = 0; a < 4; ++a) qf[a][k] = blk == a ? qv : v4u{0u, 0u, 0u, 0u};
    }
    float mx = -3.0e38f;
#pragma nounroll
    for (int t = c;; t += 4) {
      f32x4 s0 = {0.f, 0.f, 0.f, 0.f}, s1 = {0.f, 0.f, 0.f, 0.f};
#pragma unroll
      for (int k = 0; k < 4; ++k) {
        s0 = mfma32(kf[0][k], qf[0][k], s0); s1 = mfma32(kf[1][k], qf[1][k], s1);
        s0 = mfma32(kf[2][k], qf[2][k], s0); s1 = mfma32(kf[3][k], qf[3][k], s1);
      }
      s0 += s1;
      const unsigned a0 = pk_bf16(s0[0], s0[1]), a1 = pk_bf16(s0[2], s0[3]);
      const unsigned b0 = pk_bf16(bf_lo(a0) * SCALE, bf_hi(a0) * SCALE), b1 = pk_bf16(bf_lo(a1) * SCALE, bf_hi(a1) * SCALE);
      *(LAS v2u*)(L + L_S + t * 512 + lane * 8) = v2u{b0, b1};
      mx = max2(max2(mx, max2(bf_lo(b0), bf_hi(b0))), max2(bf_lo(b1), bf_hi(b1)));
      if (t + 4 >= nt) break;
      fetch_k(t + 4);
    }
    mx = max2(mx, dpp<ROR8>(mx)); mx = max2(mx, dpp<ROR4>(mx)); mx = xrow_max(mx);
    if (lane < 4) *(LAS float*)(L + L_M + lane * 16 + c * 4) = mx;
    wait_lgkm();
    if (lane == 0) __hip_atomic_fetch_add((LAS unsigned*)(ctrl + C_MAX), 1u, __ATOMIC_RELAXED, __HIP_MEMORY_SCOPE_WORKGROUP);
    lds_wait_ge(ctrl, C_MAX, 4u, budget);
    const f32x4 m4 = *(LAS f32x4*)(L + L_M + (col & 3) * 16);
    ms = max2(max2(m4[0], m4[1]), max2(m4[2], m4[3])) * LOG2E;
  }
  unsigned va[2];
#pragma unroll
  for (int n = 0; n < 2; ++n)
    va[n] = (4 * g + q4) * 256 + (((4 * c + 2 * n + (p4 >> 1)) ^ ((4 * (g & 1) + q4) << 1)) * 16) + 8 * (p4 & 1);
  int p1_off = col < 8 ? WS_P1 + wid * 2048 + c * 512 + g * 128 + col * 16 : OOB;
  int p2_off = col < 4 ? WS_P2 + wid * 1024 + c * 256 + g * 64 + col * 16 : OOB;
  int rec0 = WS_P1 + (kvh * 32 + (c >> 1) * 16 + (lane >> 3)) * 2048 + (gi >> 2) * 256 + (c & 1) * 128 + (lane & 7) * 16, rec1 = rec0 + 8 * 2048;
  unsigned o_wr = (lane & 40) == 0 ? L_O + c * 64 + col * 8 + g * 4 : L_NIL + lane * 4, o_rd = lbase + L_O + ((col >> 1) & 1) * 128 + g * 8;
  unsigned wo0 = ((2 * nt) & (NSLOT - 1)) * SLOT + va[0], wo1 = ((2 * nt) & (NSLOT - 1)) * SLOT + va[1];
  asm volatile("" : "+v"(p1_off), "+v"(p2_off), "+v"(rec0), "+v"(rec1), "+v"(o_wr), "+v"(o_rd), "+v"(wo0), "+v"(wo1));
  float lsum = 0.f, linv = 0.f;
  unsigned lam = 0;
  f32x4 acc0 = {0.f, 0.f, 0.f, 0.f}, acc1 = {0.f, 0.f, 0.f, 0.f};
#pragma nounroll
  for (int t = 0; t < nt; ++t) {
    const int seq = nt + t;
    const v2u sc = *(LAS v2u*)(L + L_S + t * 512 + lane * 8);
    const float e0 = __builtin_amdgcn_exp2f(fmaf(bf_lo(sc[0]), LOG2E, -ms)), e1 = __builtin_amdgcn_exp2f(fmaf(bf_hi(sc[0]), LOG2E, -ms));
    const float e2 = __builtin_amdgcn_exp2f(fmaf(bf_lo(sc[1]), LOG2E, -ms)), e3 = __builtin_amdgcn_exp2f(fmaf(bf_hi(sc[1]), LOG2E, -ms));
    lsum += (e0 + e1) + (e2 + e3);
    const unsigned p0 = pk_bf16(e0, e1), p1 = pk_bf16(e2, e3);
    v4u pA = {blk == 0 ? p0 : 0u, blk == 0 ? p1 : 0u, blk == 1 ? p0 : 0u, blk == 1 ? p1 : 0u};
    v4u pB = {blk == 2 ? p0 : 0u, blk == 2 ? p1 : 0u, blk == 3 ? p0 : 0u, blk == 3 ? p1 : 0u};
    if (t == nt - 1) {
      lds_wait_ge(ctrl, C_FLAG, 1u, budget);
      const float lt = quad_stride_sum(lsum);
      linv = __builtin_amdgcn_rcpf(lt);
      lam = __float_as_uint(ms + __builtin_amdgcn_logf(lt));
    }
    asm volatile("" : "+v"(pA), "+v"(pB), "+v"(lsum), "+v"(linv), "+v"(lam));
    LAS char* sb = L + (seq & (NSLOT - 1)) * SLOT;
    need(4 * seq + 2);
    acc0 = mfma32(cat(lds_tr(sb + va[0]), lds_tr(sb + va[0] + 4096)), pA, acc0);
    acc1 = mfma32(cat(lds_tr(sb + va[1]), lds_tr(sb + va[1] + 4096)), pA, acc1);
    need(4 * seq + 4);
    v4u v0 = cat(lds_tr(sb + va[0] + 8192), lds_tr(sb + va[0] + 12288)), v1 = cat(lds_tr(sb + va[1] + 8192), lds_tr(sb + va[1] + 12288));
    wait_lgkm();
    if (lane == 0) ctrl[C_FREED + c] = seq + 1;
    asm volatile("" : "+v"(v0), "+v"(v1) :: "memory");
    acc0 = mfma32(v0, pB, acc0);
    acc1 = mfma32(v1, pB, acc1);
  }

  v4u pk;
#pragma unroll
  for (int j = 0; j < 4; ++j) {
    float a = ((col & 4) ? acc1[j] : acc0[j]) + dpp<ROR4>((col & 4) ? acc0[j] : acc1[j]);
    a += dpp<ROR8>(a);
    pk[j] = ((__float_as_uint(a * linv) + 0x80u) & 0xffffff00u) | ((lam >> (8 * j)) & 0xffu);
  }
  __builtin_amdgcn_raw_buffer_store_b128(not_sent(pk), rs, p1_off, 0, AUX_SC1);
  v4u a0, a1, wa[4];
  need(g_wo);
#pragma unroll
  for (int n = 0; n < 4; ++n) wa[n] = cat(lds_tr(L + ((n & 1) ? wo1 : wo0) + (n >> 1) * 4096), lds_tr(L + ((n & 1) ? wo1 : wo0) + (n >> 1) * 4096 + 8192));
  __builtin_amdgcn_sched_barrier(0);
  poll_box2(rs, rec0, rec1, a0, a1, budget);
  const float lA = __uint_as_float(__builtin_amdgcn_perm(__builtin_amdgcn_perm(a0[3], a0[2], 0x04000c0cu), __builtin_amdgcn_perm(a0[1], a0[0], 0x0c0c0400u), 0x07060100u));
  const float lB = __uint_as_float(__builtin_amdgcn_perm(__builtin_amdgcn_perm(a1[3], a1[2], 0x04000c0cu), __builtin_amdgcn_perm(a1[1], a1[0], 0x0c0c0400u), 0x07060100u));
  float M = max2(lA, lB);
  M = xrow_max(max2(M, dpp<ROR8>(M)));
  const float wA = __builtin_amdgcn_exp2f(lA - M), wB = __builtin_amdgcn_exp2f(lB - M);
  float r[4];
#pragma unroll
  for (int j = 0; j < 4; ++j) r[j] = fmaf(wA, __uint_as_float(a0[j] & 0xffffff00u), wB * __uint_as_float(a1[j] & 0xffffff00u));
  float wsum = xrow_sum(wA + wB), ov = rows_sum4(r[0], r[1], r[2], r[3]);
  wsum += dpp<ROR8>(wsum); ov += dpp<ROR8>(ov);
  ov *= __builtin_amdgcn_rcpf(wsum);
  const auto pr = __builtin_amdgcn_permlane32_swap(__float_as_uint(ov), __float_as_uint(ov), false, false);
  *(LAS unsigned*)(L + o_wr) = min(pk_bf16(__uint_as_float(pr[0]), __uint_as_float(pr[1])), SENT - 1);
  v4u o0, o1;
  poll_lds_o(o_rd, o0, o1, budget);
  const f32x4 zero = {0.f, 0.f, 0.f, 0.f};
  const f32x4 sp0 = mfma32(wa[2], o1, mfma32(wa[0], o0, zero)), sp1 = mfma32(wa[3], o1, mfma32(wa[1], o0, zero));
  const f32x4 sp = (col & 1) ? sp1 : sp0;
  __builtin_amdgcn_raw_buffer_store_b128(not_sent(__builtin_bit_cast(v4u, sp)), rs, p2_off, 0, AUX_SC1);
  ctrl[C_PUB] = 1u;
}

extern "C" void kernel_launch(void* const* d_in, const int* in_sizes, int n_in, void* d_out, int out_size, void* d_ws, size_t ws_size,
                              hipStream_t stream) {
  (void)hipFuncSetAttribute((const void*)fused_decode, hipFuncAttributeMaxDynamicSharedMemorySize, L_BYTES);
  hipLaunchKernelGGL(fused_decode, dim3(NWG), dim3(448), L_BYTES, stream, (const char*)d_in[0], (const char*)d_in[1], (const char*)d_in[2],
                     (const char*)d_in[3], (const char*)d_in[4], (const char*)d_in[6], (const char*)d_in[7], (char*)d_out, (char*)d_ws);
}
```
